```python
import torch

def _softmax(x, axis=-1):
    return torch.softmax(x, dim=axis)
def _gelu(x):
    return torch.nn.functional.gelu(x, approximate="tanh")
def _silu(x):
    return torch.nn.functional.silu(x)
def _sigmoid(x):
    return torch.sigmoid(x)
def _layernorm(x, axis=-1, eps=1e-5):
    m = x.mean(dim=axis, keepdim=True)
    v = x.var(dim=axis, keepdim=True, unbiased=False)
    return (x - m) / torch.sqrt(v + eps)
def _scatter_add(tgt, idx, src, axis=0):
    return tgt.clone().index_add_(axis, idx, src)
def _segment_sum(src, seg, n):
    out = torch.zeros((n,) + src.shape[1:], dtype=src.dtype)
    return out.index_add_(0, seg, src)
def _pad(x, pw):
    flat = tuple(v for pair in reversed(pw) for v in pair)
    return torch.nn.functional.pad(x, flat)

kernel_name = 'recurrent_scan__sliding_block_cached__glu_gated_glu__prompt'

def setup_inputs():

    _w = (lambda a: (lambda t: t.to(torch.bfloat16) if t.is_floating_point() else t)(torch.as_tensor(a)))
    def _A_setup():
        B, Sq, D = 1, 2048, 512
        Hq, Hkv, Sk, Dh = 32, 32, 2048, 128
        assert Hq % Hkv == 0, (Hq, Hkv)
        return {
            "x": _w((lambda *s: torch.randn(*s, dtype=torch.bfloat16) * 0.05)(B, Sq, D)),
            "k_cache": _w((lambda *s: torch.randn(*s, dtype=torch.bfloat16) * 0.8)(B, Hkv, Sk, Dh)),
            "v_cache": _w((lambda *s: torch.randn(*s, dtype=torch.bfloat16) * 0.2)(B, Hkv, Sk, Dh)),
            "wq": _w((lambda *s: torch.randn(*s, dtype=torch.bfloat16) * 0.8)(D, Hq * Dh) * (D ** -0.5)),
            "wo": _w((lambda *s: torch.randn(*s, dtype=torch.bfloat16) * 0.2)(Hq * Dh, D) * ((Hq * Dh) ** -0.5)),
        }
    _A_in = _A_setup()

    def _B_setup():
        B, S, D, F = 1, 2048, 512, 1376
        return {
            "x": _w((lambda *s: torch.randn(*s, dtype=torch.bfloat16) * 0.05)(B, S, D)),
            "w_gate": _w((lambda *s: torch.randn(*s, dtype=torch.bfloat16) * 0.2)(D, F)),
            "w_up": _w((lambda *s: torch.randn(*s, dtype=torch.bfloat16) * 0.1)(D, F)),
            "w_down": _w((lambda *s: torch.randn(*s, dtype=torch.bfloat16) * 0.1)(F, D)),
        }
    _B_in = _B_setup()

    return {
        "A_x": _A_in["x"],
        "A_k_cache": _A_in["k_cache"],
        "A_v_cache": _A_in["v_cache"],
        "A_wq": _A_in["wq"],
        "A_wo": _A_in["wo"],
        "B_x": _B_in["x"],
        "B_w_gate": _B_in["w_gate"],
        "B_w_up": _B_in["w_up"],
        "B_w_down": _B_in["w_down"],
    }

def reference(A_x, A_k_cache, A_v_cache, A_wq, A_wo, B_x, B_w_gate, B_w_up, B_w_down):
    def _A_ref(x, k_cache, v_cache, wq, wo):
        B, Sq, D = x.shape
        Hq, Dh = 32, 128
        Hkv, Sk = k_cache.shape[1], k_cache.shape[2]
        W = min(4096, max(256, Sk // 4))
        rep = Hq // Hkv
        q = (lambda x, *d: x.permute(*d))((x @ wq).reshape(B, Sq, Hq, Dh), 0, 2, 1, 3)
        k = (lambda x, n, axis: torch.repeat_interleave(x, n, dim=axis))(k_cache[:, :, Sk - W:, :], rep, axis=1)
        v = (lambda x, n, axis: torch.repeat_interleave(x, n, dim=axis))(v_cache[:, :, Sk - W:, :], rep, axis=1)
        scores = torch.einsum("bhqd,bhkd->bhqk", q, k) * (Dh ** -0.5)
        p = _softmax(scores, axis=-1)
        o = torch.einsum("bhqk,bhkd->bhqd", p, v)
        return (lambda x, *d: x.permute(*d))(o, 0, 2, 1, 3).reshape(B, Sq, Hq * Dh) @ wo

    def _B_ref(x, w_gate, w_up, w_down):
        g = torch.einsum("bsd,df->bsf", x, w_gate)
        u = torch.einsum("bsd,df->bsf", x, w_up)
        h = _sigmoid(g) * u
        return torch.einsum("bsf,fd->bsd", h, w_down)

    x = A_x
    xn = _layernorm(x, axis=-1)
    s = _A_ref(xn, A_k_cache, A_v_cache, A_wq, A_wo)
    g = _silu(_B_ref(xn, B_w_gate, B_w_up, B_w_down))
    return x + s * g
```

```cpp
#include <hip/hip_runtime.h>
#include <hip/hip_bf16.h>
#include <stdint.h>
typedef unsigned short u16;
__device__ __forceinline__ float bf2f(u16 x){ return __uint_as_float(((unsigned)x)<<16); }
__device__ __forceinline__ u16 f2bf(float f){ unsigned u=__float_as_uint(f); u += 0x7fffu + ((u>>16)&1u); return (u16)(u>>16); }

__global__ void k_ln(const u16* x, u16* xn){
  int row=blockIdx.x; int t=threadIdx.x;
  float v[8]; float s=0;
  for(int j=0;j<8;j++){ v[j]=bf2f(x[row*512+t*8+j]); s+=v[j]; }
  for(int o=32;o>0;o>>=1) s+=__shfl_xor(s,o);
  float m=s/512.f; float q=0;
  for(int j=0;j<8;j++){ float d=v[j]-m; q+=d*d; }
  for(int o=32;o>0;o>>=1) q+=__shfl_xor(q,o);
  float r=1.f/sqrtf(q/512.f+1e-5f);
  for(int j=0;j<8;j++) xn[row*512+t*8+j]=f2bf((v[j]-m)*r);
}
__global__ void k_gemm(const u16* A,const u16* B,float* C,int M,int N,int K){
  __shared__ float As[16][65], Bs[16][65];
  int tx=threadIdx.x%16, ty=threadIdx.x/16;
  int m0=blockIdx.y*64, n0=blockIdx.x*64;
  float acc[4][4]={};
  for(int k0=0;k0<K;k0+=16){
    for(int i=threadIdx.x;i<64*16;i+=256){ int r=i/16,c=i%16; As[c][r]=bf2f(A[(long)(m0+r)*K+k0+c]); }
    for(int i=threadIdx.x;i<64*16;i+=256){ int r=i/64,c=i%64; Bs[r][c]=(n0+c<N)?bf2f(B[(long)(k0+r)*N+n0+c]):0.f; }
    __syncthreads();
    for(int k=0;k<16;k++){
      float a[4],b[4];
      for(int i=0;i<4;i++){a[i]=As[k][ty*4+i]; b[i]=Bs[k][tx*4+i];}
      for(int i=0;i<4;i++)for(int j=0;j<4;j++)acc[i][j]+=a[i]*b[j];
    }
    __syncthreads();
  }
  for(int i=0;i<4;i++)for(int j=0;j<4;j++) if(n0+tx*4+j<N) C[(long)(m0+ty*4+i)*N+n0+tx*4+j]=acc[i][j];
}
__global__ void k_attn(const float* q,const u16* kc,const u16* vc,u16* o){
  int row=blockIdx.x, h=blockIdx.y, t=threadIdx.x;
  __shared__ float qs[128], p[512], red[128];
  qs[t]=bf2f(f2bf(q[(long)row*4096+h*128+t])); __syncthreads();
  float mx=-1e30f;
  for(int j=t;j<512;j+=128){ const u16* kp=kc+((long)h*2048+1536+j)*128; float s=0; for(int d=0;d<128;d++) s+=qs[d]*bf2f(kp[d]); s*=0.08838834764831845f; p[j]=s; mx=fmaxf(mx,s);}
  red[t]=mx; __syncthreads(); for(int o2=64;o2>0;o2>>=1){ if(t<o2) red[t]=fmaxf(red[t],red[t+o2]); __syncthreads(); } mx=red[0]; __syncthreads();
  float sm=0; for(int j=t;j<512;j+=128){ p[j]=__expf(p[j]-mx); sm+=p[j]; }
  red[t]=sm; __syncthreads(); for(int o2=64;o2>0;o2>>=1){ if(t<o2) red[t]+=red[t+o2]; __syncthreads(); } sm=red[0];
  float acc=0; for(int j=0;j<512;j++) acc+=p[j]*bf2f(vc[((long)h*2048+1536+j)*128+t]);
  o[(long)row*4096+h*128+t]=f2bf(acc/sm);
}
__global__ void k_h(const float* g,const float* u,u16* h,int n){ int i=blockIdx.x*256+threadIdx.x; if(i<n){ float gg=g[i]; h[i]=f2bf(u[i]/(1.f+__expf(-gg))); } }
__global__ void k_fin(const u16* x,const float* s,const float* b,u16* out,int n){ int i=blockIdx.x*256+threadIdx.x; if(i<n){ float bb=b[i]; float g=bb/(1.f+__expf(-bb)); out[i]=f2bf(bf2f(x[i])+s[i]*g);} }

extern "C" void kernel_launch(void* const* d_in, const int* in_sizes, int n_in, void* d_out, int out_size, void* d_ws, size_t ws_size, hipStream_t stream){
  const u16* x=(const u16*)d_in[0]; const u16* kc=(const u16*)d_in[1]; const u16* vc=(const u16*)d_in[2];
  const u16* wq=(const u16*)d_in[3]; const u16* wo=(const u16*)d_in[4];
  const u16* wg=(const u16*)d_in[6]; const u16* wu=(const u16*)d_in[7]; const u16* wd=(const u16*)d_in[8];
  char* ws=(char*)d_ws;
  u16* xn=(u16*)ws; ws+=2048*512*2;
  float* q=(float*)ws; ws+=2048*4096*4;
  u16* o=(u16*)ws; ws+=2048*4096*2;
  float* g=(float*)ws; ws+=2048*1376*4;
  float* u=(float*)ws; ws+=2048*1376*4;
  u16* h=(u16*)ws; ws+=2048*1376*2;
  float* s=(float*)ws; ws+=2048*512*4;
  float* b=(float*)ws; ws+=2048*512*4;
  k_ln<<<2048,64,0,stream>>>(x,xn);
  k_gemm<<<dim3(64,32),256,0,stream>>>(xn,wq,q,2048,4096,512);
  k_attn<<<dim3(2048,32),128,0,stream>>>(q,kc,vc,o);
  k_gemm<<<dim3(8,32),256,0,stream>>>(o,wo,s,2048,512,4096);
  k_gemm<<<dim3(22,32),256,0,stream>>>(xn,wg,g,2048,1376,512);
  k_gemm<<<dim3(22,32),256,0,stream>>>(xn,wu,u,2048,1376,512);
  k_h<<<(2048*1376+255)/256,256,0,stream>>>(g,u,h,2048*1376);
  k_gemm<<<dim3(8,32),256,0,stream>>>(h,wd,b,2048,512,1376);
  k_fin<<<(2048*512+255)/256,256,0,stream>>>(x,s,b,(u16*)d_out,2048*512);
}
```

```cpp
#include <hip/hip_runtime.h>

extern "C" __global__ __launch_bounds__(512, 2) void fused_block(const void* x, const void* kc, const void* vc, const void* wq, const void* wo,
    const void* wg, const void* wu, const void* wd, void* out, void* ws) {
  __shared__ __attribute__((aligned(16))) char lds[163840];
  unsigned tid = threadIdx.x + (unsigned)(size_t)lds;
  asm volatile(
    "s_getpc_b64 s[80:81]\n"
    "L_code0_%=:\n"
    "s_mov_b64 s[4:5], %1\n"
    "s_mov_b32 s6, %2\n"
    "s_load_dwordx16 s[40:55], s[4:5], 0\n"
    "s_load_dwordx4 s[56:59], s[4:5], 64\n"
    "v_lshrrev_b32 v1, 6, v0\n"
    "s_lshr_b32 s62, s6, 3\n"
    "s_lshl_b32 s62, s62, 11\n"
    "v_and_b32 v230, 1, v0\n"
    "v_lshl_add_u32 v230, v1, 1, v230\n"
    "v_lshl_add_u32 v230, v230, 7, s62\n"
    "v_min_u32 v230, L_code1_%=-L_code0_%=-4, v230\n"
    "global_load_dword v231, v230, s[80:81]\n"
    "v_and_b32 v248, 63, v0\n"
    "v_readfirstlane_b32 s7, v1\n"
    "v_lshlrev_b32 v15, 4, v248\n"
    "v_lshrrev_b32 v253, 3, v248\n"
    "v_bfe_u32 v254, v248, 4, 2\n"
    "v_and_b32 v255, 7, v248\n"
    "v_xor_b32 v254, v254, v255\n"
    "v_lshlrev_b32 v254, 4, v254\n"
    "v_lshl_add_u32 v8, v253, 10, v254\n"
    "v_lshl_add_u32 v12, v253, 8, v254\n"
    "v_xor_b32 v9, 64, v8\n"
    "v_add_u32 v9, 8192, v9\n"
    "v_bfe_u32 v253, v248, 4, 1\n"
    "v_lshlrev_b32 v254, 1, v253\n"
    "v_and_b32 v251, 3, v248\n"
    "v_xor_b32 v254, v254, v251\n"
    "v_lshrrev_b32 v255, 5, v248\n"
    "v_lshl_add_u32 v254, v255, 2, v254\n"
    "v_lshlrev_b32 v254, 4, v254\n"
    "v_bfe_u32 v250, v248, 2, 2\n"
    "v_lshl_add_u32 v253, v253, 3, v250\n"
    "v_lshl_add_u32 v10, v253, 13, v254\n"
    "v_bfe_u32 v253, v248, 2, 3\n"
    "v_lshl_add_u32 v14, v253, 8, v254\n"
    "v_lshl_add_u32 v18, v253, 10, v254\n"
    "s_and_b32 s62, s6, 7\n"
    "s_lshr_b32 s63, s6, 3\n"
    "s_and_b32 s64, s63, 3\n"
    "s_lshl_b32 s60, s62, 2\n"
    "s_add_u32 s60, s60, s64\n"
    "s_lshr_b32 s61, s63, 2\n"
    "s_lshl_b32 s74, s7, 11\n"
    "s_lshl_b32 s77, s7, 12\n"
    "s_add_u32 s78, s77, 32768\n"
    "s_mov_b32 s88, s74\n"
    "s_lshl_b32 s62, s7, 10\n"
    "s_add_u32 s75, s62, 16384\n"
    "s_add_u32 s76, s62, 22528\n"
    "s_cmp_lt_u32 s7, 6\n"
    "s_cselect_b32 s75, s75, 28672\n"
    "s_cselect_b32 s76, s76, 29696\n"
    "s_lshr_b32 s62, s7, 2\n"
    "s_lshl_b32 s62, s62, 5\n"
    "s_and_b32 s63, s7, 1\n"
    "s_lshl_b32 s63, s63, 4\n"
    "s_add_u32 s62, s62, s63\n"
    "s_and_b32 s63, s7, 2\n"
    "s_lshl_b32 s63, s63, 1\n"
    "s_add_u32 s62, s62, s63\n"
    "s_lshl_b32 s70, s62, 13\n"
    "s_mul_i32 s64, s60, 3\n"
    "s_lshl_b32 s63, s60, 1\n"
    "s_add_u32 s63, s63, 22\n"
    "s_cmp_lt_u32 s60, 22\n"
    "s_cselect_b32 s66, s64, s63\n"
    "s_cselect_b32 s65, 6, 4\n"
    "s_cselect_b32 s79, 2048, 0x40000000\n"
    "v_mul_u32_u24 v254, 10923, v0\n"
    "v_lshrrev_b32 v254, 16, v254\n"
    "v_mul_u32_u24 v255, 6, v254\n"
    "v_sub_u32 v255, v0, v255\n"
    "v_and_b32 v253, 0x23, v254\n"
    "v_bfe_u32 v249, v254, 2, 2\n"
    "v_lshl_add_u32 v253, v249, 3, v253\n"
    "v_bfe_u32 v249, v254, 4, 1\n"
    "v_lshl_add_u32 v253, v249, 2, v253\n"
    "v_mul_u32_u24 v11, 2752, v253\n"
    "v_lshl_add_u32 v11, v255, 4, v11\n"
    "v_cmp_le_u32 vcc, s65, v255\n"
    "v_mov_b32 v249, 0x40000000\n"
    "s_nop 0\n"
    "v_cndmask_b32 v11, v11, v249, vcc\n"
    "v_cmp_le_u32 vcc, 384, v0\n"
    "s_nop 1\n"
    "v_cndmask_b32 v11, v11, v249, vcc\n"
    "s_lshl_b32 s71, s66, 5\n"
    "s_add_u32 s62, s61, s6\n"
    "s_and_b32 s62, s62, 7\n"
    "s_mov_b32 s82, s62\n"
    "s_and_b32 s62, s62, 6\n"
    "s_lshl_b32 s63, s62, 19\n"
    "s_add_u32 s70, s70, s63\n"
    "s_mul_i32 s63, s62, 176128\n"
    "s_add_u32 s71, s71, s63\n"
    "s_lshl_b32 s72, s62, 7\n"
    "s_add_u32 s73, s72, 16384\n"
    "s_lshl_b32 s62, s61, 18\n"
    "s_lshl_b32 s63, s7, 15\n"
    "s_add_u32 s64, s62, s63\n"
    "s_lshl_b32 s65, s60, 8\n"
    "s_lshl_b32 s62, s6, 3\n"
    "s_add_u32 s86, s62, s7\n"
    "s_lshl_b32 s62, s86, 10\n"
    "v_add_u32 v253, s62, v15\n"
    "s_waitcnt lgkmcnt(0)\n"
    "s_load_dwordx2 s[84:85], s[58:59], 0\n"
    "s_mov_b32 s4, s82\n"
    "s_add_u32 s8, s40, s64\n"
    "s_addc_u32 s9, s41, 0\n"
    "s_and_b32 s9, s9, 0xffff\n"
    "s_mov_b32 s10, 32768\n"
    "s_mov_b32 s11, 0x20000\n"
    "s_mov_b32 m0, s78\n"
    "s_nop 0\n"
    "buffer_load_dwordx4 v8, s[8:11], s72 offen lds\n"
    "s_add_u32 m0, s78, 1024\n"
    "s_nop 0\n"
    "buffer_load_dwordx4 v9, s[8:11], s72 offen lds\n"
    "s_add_u32 m0, s78, 2048\n"
    "s_nop 0\n"
    "buffer_load_dwordx4 v8, s[8:11], s73 offen lds\n"
    "s_add_u32 m0, s78, 3072\n"
    "s_nop 0\n"
    "buffer_load_dwordx4 v9, s[8:11], s73 offen lds\n"
    "s_add_u32 s72, s72, 128\n"
    "s_and_b32 s72, s72, 1023\n"
    "s_add_u32 s73, s72, 16384\n"
    "s_xor_b32 s78, s78, 0x10000\n"
    "s_add_u32 s12, s46, s65\n"
    "s_addc_u32 s13, s47, 0\n"
    "s_and_b32 s13, s13, 0xffff\n"
    "s_sub_u32 s14, 4194304, s65\n"
    "s_mov_b32 s15, 0x20000\n"
    "s_mov_b32 s16, s50\n"
    "s_mov_b32 s17, s51\n"
    "s_and_b32 s17, s17, 0xffff\n"
    "s_mov_b32 s18, 1409024\n"
    "s_mov_b32 s19, 0x20000\n"
    "s_mov_b32 s20, s52\n"
    "s_mov_b32 s21, s53\n"
    "s_and_b32 s21, s21, 0xffff\n"
    "s_mov_b32 s22, 1409024\n"
    "s_mov_b32 s23, 0x20000\n"
    "s_add_u32 m0, 65536, s74\n"
    "s_nop 0\n"
    "buffer_load_dwordx4 v10, s[12:15], s70 offen lds\n"
    "s_add_u32 s62, s70, 128\n"
    "s_add_u32 s63, 65536, s74\n"
    "s_add_u32 m0, s63, 1024\n"
    "s_nop 0\n"
    "buffer_load_dwordx4 v10, s[12:15], s62 offen lds\n"
    "s_add_u32 s70, s70, 524288\n"
    "s_and_b32 s70, s70, 0x3fffff\n"
    "s_add_u32 m0, 65536, s75\n"
    "s_nop 0\n"
    "buffer_load_dwordx4 v11, s[16:19], s71 offen lds\n"
    "s_add_u32 m0, 65536, s76\n"
    "s_nop 0\n"
    "buffer_load_dwordx4 v11, s[20:23], s71 offen lds\n"
    "s_add_u32 s71, s71, 176128\n"
    "s_sub_u32 s62, s71, 1409024\n"
    "s_cselect_b32 s71, s71, s62\n"
    "global_load_dwordx4 v[224:227], v253, s[40:41]\n"
    "v_lshrrev_b32 v249, 4, v248\n"
    "v_and_b32 v252, 15, v248\n"
    "v_lshrrev_b32 v253, 1, v249\n"
    "v_mul_u32_u24 v17, 2048, v253\n"
    "v_and_b32 v253, 1, v249\n"
    "v_mul_u32_u24 v254, 288, v253\n"
    "v_add_u32 v17, v17, v254\n"
    "v_lshl_add_u32 v17, v250, 6, v17\n"
    "v_lshl_add_u32 v17, v251, 3, v17\n"
    "v_lshrrev_b32 v253, 3, v252\n"
    "v_lshlrev_b32 v16, 10, v253\n"
    "v_and_b32 v253, 7, v252\n"
    "v_lshl_add_u32 v16, v253, 7, v16\n"
    "v_lshrrev_b32 v253, 1, v252\n"
    "v_xor_b32 v253, v253, v249\n"
    "v_lshl_add_u32 v16, v253, 4, v16\n"
    "v_lshrrev_b32 v253, 2, v248\n"
    "v_mul_u32_u24 v5, 96, v253\n"
    "v_lshl_add_u32 v5, v251, 3, v5\n"
    "v_add_u32 v5, 65536, v5\n"
    "v_xor_b32 v13, 64, v12\n"
    "v_add_u32 v13, 2048, v13\n"
    "v_lshlrev_b32 v19, 4, v249\n"
    "v_add_u32 v19, 161792, v19\n"
    "s_lshr_b32 s62, s7, 1\n"
    "s_lshl_b32 s82, s62, 12\n"
    "s_and_b32 s63, s7, 1\n"
    "s_lshl_b32 s63, s63, 7\n"
    "s_add_u32 s82, s82, s63\n"
    "s_mov_b32 s83, s74\n"
    "s_lshl_b32 s62, s60, 19\n"
    "s_add_u32 s24, s42, s62\n"
    "s_addc_u32 s25, s43, 0\n"
    "s_add_u32 s24, s24, 393216\n"
    "s_addc_u32 s25, s25, 0\n"
    "s_and_b32 s25, s25, 0xffff\n"
    "s_mov_b32 s26, 131072\n"
    "s_mov_b32 s27, 0x20000\n"
    "s_add_u32 s28, s44, s62\n"
    "s_addc_u32 s29, s45, 0\n"
    "s_add_u32 s28, s28, 393216\n"
    "s_addc_u32 s29, s29, 0\n"
    "s_and_b32 s29, s29, 0xffff\n"
    "s_mov_b32 s30, 131072\n"
    "s_mov_b32 s31, 0x20000\n"
    "s_lshl_b32 s62, s61, 3\n"
    "s_add_u32 s62, s62, s7\n"
    "s_mul_i32 s63, s62, 264192\n"
    "s_lshl_b32 s64, s60, 12\n"
    "s_add_u32 s63, s63, s64\n"
    "s_add_u32 s32, s58, s63\n"
    "s_addc_u32 s33, s59, 0\n"
    "s_add_u32 s32, s32, 1048576\n"
    "s_addc_u32 s33, s33, 0\n"
    "s_and_b32 s33, s33, 0xffff\n"
    "s_mov_b32 s34, 136192\n"
    "s_mov_b32 s35, 0x20000\n"
    "s_mul_i32 s63, s62, 87\n"
    "s_add_u32 s63, s63, s66\n"
    "s_lshl_b32 s63, s63, 10\n"
    "s_add_u32 s36, s58, s63\n"
    "s_addc_u32 s37, s59, 0\n"
    "s_add_u32 s36, s36, 17956864\n"
    "s_addc_u32 s37, s37, 0\n"
    "s_and_b32 s37, s37, 0xffff\n"
    "s_mov_b32 s38, 3072\n"
    "s_mov_b32 s39, 0x20000\n"
    "s_mov_b32 s67, 65536\n"
    "v_add_u32 v1, 65536, v17\n"
    "v_xor_b32 v2, 32, v1\n"
    "s_and_b32 s62, s7, 1\n"
    "s_lshl_b32 s62, s62, 5\n"
    "v_xor_b32 v6, s62, v1\n"
    "s_lshr_b32 s62, s7, 1\n"
    "s_lshl_b32 s62, s62, 9\n"
    "v_add_u32 v6, s62, v6\n"
    "s_min_u32 s62, s7, 5\n"
    "s_lshr_b32 s63, s62, 1\n"
    "s_lshl_b32 s63, s63, 5\n"
    "s_and_b32 s62, s62, 1\n"
    "s_mul_i32 s62, s62, 6144\n"
    "s_add_u32 s62, s62, s63\n"
    "v_add_u32 v7, s62, v5\n"
    "s_add_u32 s62, s77, 32768\n"
    "v_add_u32 v3, s62, v16\n"
    "v_xor_b32 v4, 64, v3\n"
    "s_bitcmp1_b32 s4, 0\n"
    "s_cselect_b64 vcc, -1, 0\n"
    "v_add_u32 v48, 0, v6\n"
    "v_add_u32 v248, 16384, v7\n"
    "v_cndmask_b32 v48, v48, v248, vcc\n"
    "v_add_u32 v49, 4096, v6\n"
    "v_add_u32 v248, 17920, v7\n"
    "v_cndmask_b32 v49, v49, v248, vcc\n"
    "v_add_u32 v50, 8192, v6\n"
    "v_add_u32 v248, 19456, v7\n"
    "v_cndmask_b32 v50, v50, v248, vcc\n"
    "v_add_u32 v51, 12288, v6\n"
    "v_add_u32 v248, 20992, v7\n"
    "v_cndmask_b32 v51, v51, v248, vcc\n"
    "v_mov_b32 v212, 0x3f803f80\n"
    "v_mov_b32 v213, 0x3f803f80\n"
    "v_mov_b32 v214, 0x3f803f80\n"
    "v_mov_b32 v215, 0x3f803f80\n"
    "v_mov_b32 v232, 0\n"
    "v_mov_b32 v233, 0\n"
    "v_mov_b32 v234, 0\n"
    "v_mov_b32 v235, 0\n"
    "v_mov_b32 v236, 0\n"
    "v_mov_b32 v237, 0\n"
    "v_mov_b32 v238, 0\n"
    "v_mov_b32 v239, 0\n"
    "s_mov_b32 s80, 0x49800000\n"
    "s_mov_b32 s81, 0x37800000\n"
    "s_waitcnt lgkmcnt(0)\n"
    "s_add_u32 s84, s84, 1\n"
    "s_barrier\n"
    "s_mov_b32 m0, s78\n"
    "s_nop 0\n"
    "buffer_load_dwordx4 v8, s[8:11], s72 offen lds\n"
    "s_add_u32 m0, s78, 1024\n"
    "s_nop 0\n"
    "buffer_load_dwordx4 v9, s[8:11], s72 offen lds\n"
    "s_add_u32 m0, s78, 2048\n"
    "s_nop 0\n"
    "buffer_load_dwordx4 v8, s[8:11], s73 offen lds\n"
    "s_add_u32 m0, s78, 3072\n"
    "s_nop 0\n"
    "buffer_load_dwordx4 v9, s[8:11], s73 offen lds\n"
    "s_add_u32 s72, s72, 128\n"
    "s_and_b32 s72, s72, 1023\n"
    "s_add_u32 s73, s72, 16384\n"
    "s_xor_b32 s78, s78, 0x10000\n"
    "s_waitcnt vmcnt(9)\n"
    "ds_read_b128 v[164:167], v3 offset:0\n"
    "ds_read_b128 v[168:171], v3 offset:2048\n"
    "v_xor_b32 v3, 0x10000, v3\n"
    "s_waitcnt vmcnt(5)\n"
    "s_waitcnt lgkmcnt(0)\n"
    "s_barrier\n"
    "ds_read_b64_tr_b16 v[20:21], v1 offset:0\n"
    "ds_read_b64_tr_b16 v[22:23], v1 offset:4096\n"
    "ds_read_b64_tr_b16 v[24:25], v2 offset:0\n"
    "ds_read_b64_tr_b16 v[26:27], v2 offset:4096\n"
    "s_add_u32 m0, 131072, s74\n"
    "s_nop 0\n"
    "buffer_load_dwordx4 v10, s[12:15], s70 offen lds\n"
    "ds_read_b64_tr_b16 v[28:29], v1 offset:512\n"
    "ds_read_b64_tr_b16 v[30:31], v1 offset:4608\n"
    "ds_read_b64_tr_b16 v[32:33], v2 offset:512\n"
    "ds_read_b64_tr_b16 v[34:35], v2 offset:4608\n"
    "ds_read_b64_tr_b16 v[36:37], v1 offset:1024\n"
    "ds_read_b64_tr_b16 v[38:39], v1 offset:5120\n"
    "s_add_u32 s62, s70, 128\n"
    "s_add_u32 s63, 131072, s74\n"
    "s_add_u32 m0, s63, 1024\n"
    "s_nop 0\n"
    "buffer_load_dwordx4 v10, s[12:15], s62 offen lds\n"
    "s_add_u32 s70, s70, 524288\n"
    "s_and_b32 s70, s70, 0x3fffff\n"
    "s_waitcnt lgkmcnt(8)\n"
    "v_mfma_f32_16x16x32_bf16 v[52:55], v[20:23], v[164:167], 0\n"
    "s_waitcnt vmcnt(6)\n"
    "v_lshlrev_b32 v188, 16, v224\n"
    "v_and_b32 v189, 0xffff0000, v224\n"
    "v_lshlrev_b32 v190, 16, v225\n"
    "v_and_b32 v191, 0xffff0000, v225\n"
    "ds_read_b64_tr_b16 v[40:41], v2 offset:1024\n"
    "v_mfma_f32_16x16x32_bf16 v[56:59], v[20:23], v[168:171], 0\n"
    "v_lshlrev_b32 v192, 16, v226\n"
    "v_and_b32 v193, 0xffff0000, v226\n"
    "ds_read_b64_tr_b16 v[42:43], v2 offset:5120\n"
    "ds_read_b128 v[172:175], v4 offset:0\n"
    "s_waitcnt lgkmcnt(9)\n"
    "v_mfma_f32_16x16x32_bf16 v[60:63], v[24:27], v[164:167], 0\n"
    "v_lshlrev_b32 v194, 16, v227\n"
    "v_and_b32 v195, 0xffff0000, v227\n"
    "ds_read_b64_tr_b16 v[44:45], v1 offset:1536\n"
    "v_mfma_f32_16x16x32_bf16 v[64:67], v[24:27], v[168:171], 0\n"
    "v_add_f32 v196, v188, v189\n"
    "v_add_f32 v197, v190, v191\n"
    "ds_read_b64_tr_b16 v[46:47], v1 offset:5632\n"
    "s_add_u32 m0, 131072, s75\n"
    "s_nop 0\n"
    "buffer_load_dwordx4 v11, s[16:19], s71 offen lds\n"
    "ds_read_b128 v[176:179], v4 offset:2048\n"
    "s_waitcnt lgkmcnt(10)\n"
    "v_mfma_f32_16x16x32_bf16 v[68:71], v[28:31], v[164:167], 0\n"
    "v_add_f32 v198, v192, v193\n"
    "v_add_f32 v199, v194, v195\n"
    "ds_read_b64_tr_b16 v[20:21], v2 offset:1536\n"
    "v_mfma_f32_16x16x32_bf16 v[72:75], v[28:31], v[168:171], 0\n"
    "v_add_f32 v196, v196, v197\n"
    "v_add_f32 v198, v198, v199\n"
    "v_xor_b32 v4, 0x10000, v4\n"
    "ds_read_b64_tr_b16 v[22:23], v2 offset:5632\n"
    "s_waitcnt lgkmcnt(10)\n"
    "v_mfma_f32_16x16x32_bf16 v[76:79], v[32:35], v[164:167], 0\n"
    "v_add_f32 v196, v196, v198\n"
    "ds_read_b64_tr_b16 v[24:25], v5 offset:16384\n"
    "ds_read_b64_tr_b16 v[180:181], v48 offset:0\n"
    "v_mfma_f32_16x16x32_bf16 v[80:83], v[32:35], v[168:171], 0\n"
    "v_add_f32_dpp v196, v196, v196 row_ror:8 row_mask:0xf bank_mask:0xf\n"
    "s_nop 1\n"
    "v_add_f32_dpp v196, v196, v196 row_ror:4 row_mask:0xf bank_mask:0xf\n"
    "ds_read_b64_tr_b16 v[26:27], v5 offset:17920\n"
    "ds_read_b64_tr_b16 v[182:183], v49 offset:0\n"
    "s_add_u32 m0, 131072, s76\n"
    "s_nop 0\n"
    "buffer_load_dwordx4 v11, s[20:23], s71 offen lds\n"
    "s_add_u32 s71, s71, 176128\n"
    "s_sub_u32 s62, s71, 1409024\n"
    "s_cselect_b32 s71, s71, s62\n"
    "s_waitcnt lgkmcnt(12)\n"
    "v_mfma_f32_16x16x32_bf16 v[84:87], v[36:39], v[164:167], 0\n"
    "v_add_f32_dpp v196, v196, v196 row_ror:2 row_mask:0xf bank_mask:0xf\n"
    "ds_read_b64_tr_b16 v[28:29], v5 offset:22528\n"
    "v_mfma_f32_16x16x32_bf16 v[88:91], v[36:39], v[168:171], 0\n"
    "v_add_f32_dpp v196, v196, v196 row_ror:1 row_mask:0xf bank_mask:0xf\n"
    "ds_read_b64_tr_b16 v[30:31], v5 offset:24064\n"
    "s_waitcnt lgkmcnt(12)\n"
    "v_mfma_f32_16x16x32_bf16 v[92:95], v[40:43], v[164:167], 0\n"
    "v_mov_b32 v197, v196\n"
    "ds_read_b64_tr_b16 v[32:33], v5 offset:16416\n"
    "v_mfma_f32_16x16x32_bf16 v[96:99], v[40:43], v[168:171], 0\n"
    "v_permlane32_swap_b32 v196, v197\n"
    "ds_read_b64_tr_b16 v[34:35], v5 offset:17952\n"
    "s_add_u32 m0, 0, s74\n"
    "s_nop 0\n"
    "buffer_load_dwordx4 v10, s[12:15], s70 offen lds\n"
    "s_waitcnt lgkmcnt(11)\n"
    "v_mfma_f32_16x16x32_bf16 v[100:103], v[44:47], v[164:167], 0\n"
    "v_add_f32 v196, v196, v197\n"
    "ds_read_b64_tr_b16 v[36:37], v5 offset:22560\n"
    "v_mfma_f32_16x16x32_bf16 v[104:107], v[44:47], v[168:171], 0\n"
    "v_mov_b32 v197, v196\n"
    "ds_read_b64_tr_b16 v[38:39], v5 offset:24096\n"
    "s_waitcnt lgkmcnt(10)\n"
    "v_mfma_f32_16x16x32_bf16 v[108:111], v[20:23], v[164:167], 0\n"
    "v_permlane16_swap_b32 v196, v197\n"
    "ds_read_b64_tr_b16 v[40:41], v5 offset:16448\n"
    "v_mfma_f32_16x16x32_bf16 v[112:115], v[20:23], v[168:171], 0\n"
    "v_add_f32 v196, v196, v197\n"
    "v_mul_f32 v196, 0x3b000000, v196\n"
    "ds_read_b64_tr_b16 v[42:43], v5 offset:17984\n"
    "s_waitcnt lgkmcnt(8)\n"
    "v_mfma_f32_16x16x32_bf16 v[216:219], v[180:183], v[212:215], 0\n"
    "s_add_u32 s62, s70, 128\n"
    "s_add_u32 s63, 0, s74\n"
    "s_add_u32 m0, s63, 1024\n"
    "s_nop 0\n"
    "buffer_load_dwordx4 v10, s[12:15], s62 offen lds\n"
    "s_add_u32 s70, s70, 524288\n"
    "s_and_b32 s70, s70, 0x3fffff\n"
    "v_mfma_f32_16x16x32_bf16 v[116:119], v[24:27], v[164:167], 0\n"
    "v_sub_f32 v188, v188, v196\n"
    "v_sub_f32 v189, v189, v196\n"
    "ds_read_b64_tr_b16 v[44:45], v5 offset:22592\n"
    "ds_read_b64_tr_b16 v[180:181], v50 offset:0\n"
    "v_mfma_f32_16x16x32_bf16 v[120:123], v[24:27], v[168:171], 0\n"
    "v_sub_f32 v190, v190, v196\n"
    "v_sub_f32 v191, v191, v196\n"
    "ds_read_b64_tr_b16 v[46:47], v5 offset:24128\n"
    "ds_read_b64_tr_b16 v[182:183], v51 offset:0\n"
    "s_waitcnt lgkmcnt(10)\n"
    "v_mfma_f32_16x16x32_bf16 v[124:127], v[28:31], v[164:167], 0\n"
    "v_sub_f32 v192, v192, v196\n"
    "v_sub_f32 v193, v193, v196\n"
    "ds_read_b64_tr_b16 v[20:21], v1 offset:8192\n"
    "v_mfma_f32_16x16x32_bf16 v[128:131], v[28:31], v[168:171], 0\n"
    "v_sub_f32 v194, v194, v196\n"
    "v_sub_f32 v195, v195, v196\n"
    "ds_read_b64_tr_b16 v[22:23], v1 offset:12288\n"
    "s_add_u32 m0, 0, s75\n"
    "s_nop 0\n"
    "buffer_load_dwordx4 v11, s[16:19], s71 offen lds\n"
    "s_waitcnt lgkmcnt(10)\n"
    "v_mfma_f32_16x16x32_bf16 v[132:135], v[32:35], v[164:167], 0\n"
    "v_mul_f32 v198, v188, v188\n"
    "v_mul_f32 v199, v189, v189\n"
    "ds_read_b64_tr_b16 v[24:25], v2 offset:8192\n"
    "v_mfma_f32_16x16x32_bf16 v[136:139], v[32:35], v[168:171], 0\n"
    "v_fmac_f32 v198, v190, v190\n"
    "v_fmac_f32 v199, v191, v191\n"
    "ds_read_b64_tr_b16 v[26:27], v2 offset:12288\n"
    "s_waitcnt lgkmcnt(10)\n"
    "v_mfma_f32_16x16x32_bf16 v[140:143], v[36:39], v[164:167], 0\n"
    "v_fmac_f32 v198, v192, v192\n"
    "v_fmac_f32 v199, v193, v193\n"
    "v_fmac_f32 v198, v194, v194\n"
    "v_fmac_f32 v199, v195, v195\n"
    "ds_read_b64_tr_b16 v[28:29], v1 offset:8704\n"
    "v_mfma_f32_16x16x32_bf16 v[144:147], v[36:39], v[168:171], 0\n"
    "v_add_f32 v198, v198, v199\n"
    "ds_read_b64_tr_b16 v[30:31], v1 offset:12800\n"
    "s_add_u32 m0, 0, s76\n"
    "s_nop 0\n"
    "buffer_load_dwordx4 v11, s[20:23], s71 offen lds\n"
    "s_add_u32 s71, s71, 176128\n"
    "s_sub_u32 s62, s71, 1409024\n"
    "s_cselect_b32 s71, s71, s62\n"
    "s_waitcnt lgkmcnt(10)\n"
    "v_mfma_f32_16x16x32_bf16 v[148:151], v[40:43], v[164:167], 0\n"
    "v_add_f32_dpp v198, v198, v198 row_ror:8 row_mask:0xf bank_mask:0xf\n"
    "ds_read_b64_tr_b16 v[32:33], v2 offset:8704\n"
    "v_mfma_f32_16x16x32_bf16 v[152:155], v[40:43], v[168:171], 0\n"
    "v_add_f32_dpp v198, v198, v198 row_ror:4 row_mask:0xf bank_mask:0xf\n"
    "ds_read_b64_tr_b16 v[34:35], v2 offset:12800\n"
    "s_waitcnt lgkmcnt(8)\n"
    "v_mfma_f32_16x16x32_bf16 v[216:219], v[180:183], v[212:215], v[216:219]\n"
    "v_mfma_f32_16x16x32_bf16 v[156:159], v[44:47], v[164:167], 0\n"
    "v_add_f32_dpp v198, v198, v198 row_ror:2 row_mask:0xf bank_mask:0xf\n"
    "ds_read_b64_tr_b16 v[36:37], v1 offset:9216\n"
    "v_mfma_f32_16x16x32_bf16 v[160:163], v[44:47], v[168:171], 0\n"
    "v_add_f32_dpp v198, v198, v198 row_ror:1 row_mask:0xf bank_mask:0xf\n"
    "ds_read_b64_tr_b16 v[38:39], v1 offset:13312\n"
    "s_mov_b32 m0, s78\n"
    "s_nop 0\n"
    "buffer_load_dwordx4 v8, s[8:11], s72 offen lds\n"
    "s_waitcnt lgkmcnt(8)\n"
    "v_mfma_f32_16x16x32_bf16 v[52:55], v[20:23], v[172:175], v[52:55]\n"
    "v_mov_b32 v199, v198\n"
    "ds_read_b64_tr_b16 v[40:41], v2 offset:9216\n"
    "v_mfma_f32_16x16x32_bf16 v[56:59], v[20:23], v[176:179], v[56:59]\n"
    "v_permlane32_swap_b32 v198, v199\n"
    "ds_read_b64_tr_b16 v[42:43], v2 offset:13312\n"
    "s_waitcnt lgkmcnt(8)\n"
    "v_mfma_f32_16x16x32_bf16 v[60:63], v[24:27], v[172:175], v[60:63]\n"
    "v_add_f32 v198, v198, v199\n"
    "v_mov_b32 v199, v198\n"
    "v_add_u32 v48, 0x10000, v48\n"
    "v_add_u32 v49, 0x10000, v49\n"
    "ds_read_b64_tr_b16 v[44:45], v1 offset:9728\n"
    "v_mfma_f32_16x16x32_bf16 v[64:67], v[24:27], v[176:179], v[64:67]\n"
    "v_permlane16_swap_b32 v198, v199\n"
    "ds_read_b64_tr_b16 v[46:47], v1 offset:13824\n"
    "s_add_u32 m0, s78, 1024\n"
    "s_nop 0\n"
    "buffer_load_dwordx4 v9, s[8:11], s72 offen lds\n"
    "s_waitcnt lgkmcnt(8)\n"
    "v_mfma_f32_16x16x32_bf16 v[68:71], v[28:31], v[172:175], v[68:71]\n"
    "v_add_f32 v198, v198, v199\n"
    "v_add_u32 v50, 0x10000, v50\n"
    "v_add_u32 v51, 0x10000, v51\n"
    "ds_read_b64_tr_b16 v[20:21], v2 offset:9728\n"
    "v_mfma_f32_16x16x32_bf16 v[72:75], v[28:31], v[176:179], v[72:75]\n"
    "v_mov_b32 v199, 0x3727c5ac\n"
    "v_fmac_f32 v199, 0x3b000000, v198\n"
    "ds_read_b64_tr_b16 v[22:23], v2 offset:13824\n"
    "s_waitcnt vmcnt(10)\n"
    "ds_read_b128 v[164:167], v3 offset:0\n"
    "s_waitcnt lgkmcnt(9)\n"
    "v_mfma_f32_16x16x32_bf16 v[76:79], v[32:35], v[172:175], v[76:79]\n"
    "v_rsq_f32 v198, v199\n"
    "ds_read_b64_tr_b16 v[24:25], v5 offset:19456\n"
    "v_mfma_f32_16x16x32_bf16 v[80:83], v[32:35], v[176:179], v[80:83]\n"
    "v_mov_b32 v197, s84\n"
    "v_mov_b32 v199, s84\n"
    "ds_read_b64_tr_b16 v[26:27], v5 offset:20992\n"
    "s_add_u32 m0, s78, 2048\n"
    "s_nop 0\n"
    "buffer_load_dwordx4 v8, s[8:11], s73 offen lds\n"
    "ds_read_b128 v[168:171], v3 offset:2048\n"
    "s_waitcnt lgkmcnt(10)\n"
    "v_mfma_f32_16x16x32_bf16 v[84:87], v[36:39], v[172:175], v[84:87]\n"
    "ds_read_b64_tr_b16 v[28:29], v5 offset:25600\n"
    "s_lshl_b32 s89, s86, 4\n"
    "s_add_u32 s89, s89, 8192\n"
    "v_mov_b32 v188, s89\n"
    "v_mfma_f32_16x16x32_bf16 v[88:91], v[36:39], v[176:179], v[88:91]\n"
    "ds_read_b64_tr_b16 v[30:31], v5 offset:27136\n"
    "s_mov_b64 exec, 1\n"
    "global_store_dwordx4 v188, v[196:199], s[58:59] sc1\n"
    "s_mov_b64 exec, -1\n"
    "v_xor_b32 v3, 0x10000, v3\n"
    "s_waitcnt lgkmcnt(10)\n"
    "v_mfma_f32_16x16x32_bf16 v[92:95], v[40:43], v[172:175], v[92:95]\n"
    "ds_read_b64_tr_b16 v[32:33], v5 offset:19488\n"
    "v_mfma_f32_16x16x32_bf16 v[96:99], v[40:43], v[176:179], v[96:99]\n"
    "ds_read_b64_tr_b16 v[34:35], v5 offset:21024\n"
    "s_add_u32 m0, s78, 3072\n"
    "s_nop 0\n"
    "buffer_load_dwordx4 v9, s[8:11], s73 offen lds\n"
    "s_add_u32 s72, s72, 128\n"
    "s_and_b32 s72, s72, 1023\n"
    "s_add_u32 s73, s72, 16384\n"
    "s_xor_b32 s78, s78, 0x10000\n"
    "s_waitcnt lgkmcnt(10)\n"
    "v_mfma_f32_16x16x32_bf16 v[100:103], v[44:47], v[172:175], v[100:103]\n"
    "ds_read_b64_tr_b16 v[36:37], v5 offset:25632\n"
    "v_mfma_f32_16x16x32_bf16 v[104:107], v[44:47], v[176:179], v[104:107]\n"
    "ds_read_b64_tr_b16 v[38:39], v5 offset:27168\n"
    "s_waitcnt lgkmcnt(10)\n"
    "v_mfma_f32_16x16x32_bf16 v[108:111], v[20:23], v[172:175], v[108:111]\n"
    "ds_read_b64_tr_b16 v[40:41], v5 offset:19520\n"
    "v_mfma_f32_16x16x32_bf16 v[112:115], v[20:23], v[176:179], v[112:115]\n"
    "ds_read_b64_tr_b16 v[42:43], v5 offset:21056\n"
    "s_waitcnt lgkmcnt(9)\n"
    "v_mfma_f32_16x16x32_bf16 v[116:119], v[24:27], v[172:175], v[116:119]\n"
    "ds_read_b64_tr_b16 v[44:45], v5 offset:25664\n"
    "v_mfma_f32_16x16x32_bf16 v[120:123], v[24:27], v[176:179], v[120:123]\n"
    "ds_read_b64_tr_b16 v[46:47], v5 offset:27200\n"
    "s_waitcnt lgkmcnt(8)\n"
    "v_mfma_f32_16x16x32_bf16 v[124:127], v[28:31], v[172:175], v[124:127]\n"
    "s_mov_b32 s68, s67\n"
    "s_add_u32 s67, s67, 0x10000\n"
    "s_cmp_eq_u32 s67, 0x30000\n"
    "s_cselect_b32 s67, 0, s67\n"
    "s_sub_u32 s69, s67, s68\n"
    "v_mfma_f32_16x16x32_bf16 v[128:131], v[28:31], v[176:179], v[128:131]\n"
    "v_add_u32 v1, s69, v1\n"
    "v_add_u32 v2, s69, v2\n"
    "s_waitcnt lgkmcnt(6)\n"
    "v_mfma_f32_16x16x32_bf16 v[132:135], v[32:35], v[172:175], v[132:135]\n"
    "v_add_u32 v5, s69, v5\n"
    "v_mfma_f32_16x16x32_bf16 v[136:139], v[32:35], v[176:179], v[136:139]\n"
    "s_waitcnt vmcnt(9)\n"
    "s_waitcnt lgkmcnt(0)\n"
    "s_barrier\n"
    "ds_read_b64_tr_b16 v[20:21], v1 offset:0\n"
    "ds_read_b64_tr_b16 v[22:23], v1 offset:4096\n"
    "v_mfma_f32_16x16x32_bf16 v[140:143], v[36:39], v[172:175], v[140:143]\n"
    "ds_read_b64_tr_b16 v[24:25], v2 offset:0\n"
    "ds_read_b64_tr_b16 v[26:27], v2 offset:4096\n"
    "v_mfma_f32_16x16x32_bf16 v[144:147], v[36:39], v[176:179], v[144:147]\n"
    "ds_read_b64_tr_b16 v[28:29], v1 offset:512\n"
    "ds_read_b64_tr_b16 v[30:31], v1 offset:4608\n"
    "v_mfma_f32_16x16x32_bf16 v[148:151], v[40:43], v[172:175], v[148:151]\n"
    "ds_read_b64_tr_b16 v[32:33], v2 offset:512\n"
    "ds_read_b64_tr_b16 v[34:35], v2 offset:4608\n"
    "v_mfma_f32_16x16x32_bf16 v[152:155], v[40:43], v[176:179], v[152:155]\n"
    "s_add_u32 m0, s68, s74\n"
    "s_nop 0\n"
    "buffer_load_dwordx4 v10, s[12:15], s70 offen lds\n"
    "v_mfma_f32_16x16x32_bf16 v[156:159], v[44:47], v[172:175], v[156:159]\n"
    "ds_read_b64_tr_b16 v[36:37], v1 offset:1024\n"
    "v_mfma_f32_16x16x32_bf16 v[160:163], v[44:47], v[176:179], v[160:163]\n"
    "ds_read_b64_tr_b16 v[38:39], v1 offset:5120\n"
    "s_waitcnt lgkmcnt(8)\n"
    "v_mfma_f32_16x16x32_bf16 v[52:55], v[20:23], v[164:167], v[52:55]\n"
    "ds_read_b64_tr_b16 v[40:41], v2 offset:1024\n"
    "v_mfma_f32_16x16x32_bf16 v[56:59], v[20:23], v[168:171], v[56:59]\n"
    "ds_read_b64_tr_b16 v[42:43], v2 offset:5120\n"
    "ds_read_b128 v[172:175], v4 offset:0\n"
    "s_waitcnt lgkmcnt(9)\n"
    "v_mfma_f32_16x16x32_bf16 v[60:63], v[24:27], v[164:167], v[60:63]\n"
    "ds_read_b64_tr_b16 v[44:45], v1 offset:1536\n"
    "v_mfma_f32_16x16x32_bf16 v[64:67], v[24:27], v[168:171], v[64:67]\n"
    "ds_read_b64_tr_b16 v[46:47], v1 offset:5632\n"
    "ds_read_b128 v[176:179], v4 offset:2048\n"
    "s_waitcnt lgkmcnt(10)\n"
    "v_mfma_f32_16x16x32_bf16 v[68:71], v[28:31], v[164:167], v[68:71]\n"
    "ds_read_b64_tr_b16 v[20:21], v2 offset:1536\n"
    "v_mfma_f32_16x16x32_bf16 v[72:75], v[28:31], v[168:171], v[72:75]\n"
    "v_xor_b32 v4, 0x10000, v4\n"
    "ds_read_b64_tr_b16 v[22:23], v2 offset:5632\n"
    "s_add_u32 s62, s70, 128\n"
    "s_add_u32 s63, s68, s74\n"
    "s_add_u32 m0, s63, 1024\n"
    "s_nop 0\n"
    "buffer_load_dwordx4 v10, s[12:15], s62 offen lds\n"
    "s_add_u32 s70, s70, 524288\n"
    "s_and_b32 s70, s70, 0x3fffff\n"
    "s_waitcnt lgkmcnt(10)\n"
    "v_mfma_f32_16x16x32_bf16 v[76:79], v[32:35], v[164:167], v[76:79]\n"
    "ds_read_b64_tr_b16 v[24:25], v5 offset:16384\n"
    "ds_read_b64_tr_b16 v[180:181], v48 offset:0\n"
    "v_mfma_f32_16x16x32_bf16 v[80:83], v[32:35], v[168:171], v[80:83]\n"
    "ds_read_b64_tr_b16 v[26:27], v5 offset:17920\n"
    "ds_read_b64_tr_b16 v[182:183], v49 offset:0\n"
    "s_waitcnt lgkmcnt(12)\n"
    "v_mfma_f32_16x16x32_bf16 v[84:87], v[36:39], v[164:167], v[84:87]\n"
    "ds_read_b64_tr_b16 v[28:29], v5 offset:22528\n"
    "v_mfma_f32_16x16x32_bf16 v[88:91], v[36:39], v[168:171], v[88:91]\n"
    "ds_read_b64_tr_b16 v[30:31], v5 offset:24064\n"
    "s_waitcnt lgkmcnt(12)\n"
    "v_mfma_f32_16x16x32_bf16 v[92:95], v[40:43], v[164:167], v[92:95]\n"
    "ds_read_b64_tr_b16 v[32:33], v5 offset:16416\n"
    "v_mfma_f32_16x16x32_bf16 v[96:99], v[40:43], v[168:171], v[96:99]\n"
    "ds_read_b64_tr_b16 v[34:35], v5 offset:17952\n"
    "s_add_u32 m0, s68, s75\n"
    "s_nop 0\n"
    "buffer_load_dwordx4 v11, s[16:19], s71 offen lds\n"
    "s_waitcnt lgkmcnt(11)\n"
    "v_mfma_f32_16x16x32_bf16 v[100:103], v[44:47], v[164:167], v[100:103]\n"
    "ds_read_b64_tr_b16 v[36:37], v5 offset:22560\n"
    "v_mfma_f32_16x16x32_bf16 v[104:107], v[44:47], v[168:171], v[104:107]\n"
    "ds_read_b64_tr_b16 v[38:39], v5 offset:24096\n"
    "s_waitcnt lgkmcnt(10)\n"
    "v_mfma_f32_16x16x32_bf16 v[108:111], v[20:23], v[164:167], v[108:111]\n"
    "ds_read_b64_tr_b16 v[40:41], v5 offset:16448\n"
    "v_mfma_f32_16x16x32_bf16 v[112:115], v[20:23], v[168:171], v[112:115]\n"
    "ds_read_b64_tr_b16 v[42:43], v5 offset:17984\n"
    "s_waitcnt lgkmcnt(8)\n"
    "v_mfma_f32_16x16x32_bf16 v[216:219], v[180:183], v[212:215], v[216:219]\n"
    "v_mfma_f32_16x16x32_bf16 v[116:119], v[24:27], v[164:167], v[116:119]\n"
    "ds_read_b64_tr_b16 v[44:45], v5 offset:22592\n"
    "ds_read_b64_tr_b16 v[180:181], v50 offset:0\n"
    "v_mfma_f32_16x16x32_bf16 v[120:123], v[24:27], v[168:171], v[120:123]\n"
    "ds_read_b64_tr_b16 v[46:47], v5 offset:24128\n"
    "ds_read_b64_tr_b16 v[182:183], v51 offset:0\n"
    "s_waitcnt lgkmcnt(10)\n"
    "v_mfma_f32_16x16x32_bf16 v[124:127], v[28:31], v[164:167], v[124:127]\n"
    "ds_read_b64_tr_b16 v[20:21], v1 offset:8192\n"
    "v_mfma_f32_16x16x32_bf16 v[128:131], v[28:31], v[168:171], v[128:131]\n"
    "ds_read_b64_tr_b16 v[22:23], v1 offset:12288\n"
    "s_add_u32 m0, s68, s76\n"
    "s_nop 0\n"
    "buffer_load_dwordx4 v11, s[20:23], s71 offen lds\n"
    "s_add_u32 s71, s71, 176128\n"
    "s_sub_u32 s62, s71, 1409024\n"
    "s_cselect_b32 s71, s71, s62\n"
    "s_waitcnt lgkmcnt(10)\n"
    "v_mfma_f32_16x16x32_bf16 v[132:135], v[32:35], v[164:167], v[132:135]\n"
    "ds_read_b64_tr_b16 v[24:25], v2 offset:8192\n"
    "v_mfma_f32_16x16x32_bf16 v[136:139], v[32:35], v[168:171], v[136:139]\n"
    "ds_read_b64_tr_b16 v[26:27], v2 offset:12288\n"
    "s_waitcnt lgkmcnt(10)\n"
    "v_mfma_f32_16x16x32_bf16 v[140:143], v[36:39], v[164:167], v[140:143]\n"
    "ds_read_b64_tr_b16 v[28:29], v1 offset:8704\n"
    "v_mfma_f32_16x16x32_bf16 v[144:147], v[36:39], v[168:171], v[144:147]\n"
    "ds_read_b64_tr_b16 v[30:31], v1 offset:12800\n"
    "s_waitcnt lgkmcnt(10)\n"
    "v_mfma_f32_16x16x32_bf16 v[148:151], v[40:43], v[164:167], v[148:151]\n"
    "ds_read_b64_tr_b16 v[32:33], v2 offset:8704\n"
    "v_mfma_f32_16x16x32_bf16 v[152:155], v[40:43], v[168:171], v[152:155]\n"
    "ds_read_b64_tr_b16 v[34:35], v2 offset:12800\n"
    "s_waitcnt lgkmcnt(8)\n"
    "v_mfma_f32_16x16x32_bf16 v[216:219], v[180:183], v[212:215], v[216:219]\n"
    "s_mov_b32 m0, s78\n"
    "s_nop 0\n"
    "buffer_load_dwordx4 v8, s[8:11], s72 offen lds\n"
    "v_mfma_f32_16x16x32_bf16 v[156:159], v[44:47], v[164:167], v[156:159]\n"
    "ds_read_b64_tr_b16 v[36:37], v1 offset:9216\n"
    "v_mfma_f32_16x16x32_bf16 v[160:163], v[44:47], v[168:171], v[160:163]\n"
    "ds_read_b64_tr_b16 v[38:39], v1 offset:13312\n"
    "s_waitcnt lgkmcnt(8)\n"
    "v_mfma_f32_16x16x32_bf16 v[52:55], v[20:23], v[172:175], v[52:55]\n"
    "ds_read_b64_tr_b16 v[40:41], v2 offset:9216\n"
    "v_mfma_f32_16x16x32_bf16 v[56:59], v[20:23], v[176:179], v[56:59]\n"
    "ds_read_b64_tr_b16 v[42:43], v2 offset:13312\n"
    "s_waitcnt lgkmcnt(8)\n"
    "v_mfma_f32_16x16x32_bf16 v[60:63], v[24:27], v[172:175], v[60:63]\n"
    "ds_read_b64_tr_b16 v[44:45], v1 offset:9728\n"
    "v_mfma_f32_16x16x32_bf16 v[64:67], v[24:27], v[176:179], v[64:67]\n"
    "ds_read_b64_tr_b16 v[46:47], v1 offset:13824\n"
    "s_add_u32 m0, s78, 1024\n"
    "s_nop 0\n"
    "buffer_load_dwordx4 v9, s[8:11], s72 offen lds\n"
    "s_waitcnt lgkmcnt(8)\n"
    "v_mfma_f32_16x16x32_bf16 v[68:71], v[28:31], v[172:175], v[68:71]\n"
    "v_and_b32 v208, 1, v0\n"
    "v_cmp_eq_u32 s[64:65], 1, v208\n"
    "ds_read_b64_tr_b16 v[20:21], v2 offset:9728\n"
    "v_mfma_f32_16x16x32_bf16 v[72:75], v[28:31], v[176:179], v[72:75]\n"
    "v_cndmask_b32 v200, v216, v218, s[64:65]\n"
    "v_cndmask_b32 v202, v217, v219, s[64:65]\n"
    "ds_read_b64_tr_b16 v[22:23], v2 offset:13824\n"
    "s_waitcnt vmcnt(6)\n"
    "ds_read_b128 v[164:167], v3 offset:0\n"
    "s_waitcnt lgkmcnt(9)\n"
    "v_mfma_f32_16x16x32_bf16 v[76:79], v[32:35], v[172:175], v[76:79]\n"
    "ds_read_b64_tr_b16 v[24:25], v5 offset:19456\n"
    "v_mfma_f32_16x16x32_bf16 v[80:83], v[32:35], v[176:179], v[80:83]\n"
    "v_mov_b32 v201, s84\n"
    "v_mov_b32 v203, s84\n"
    "ds_read_b64_tr_b16 v[26:27], v5 offset:20992\n"
    "ds_read_b128 v[168:171], v3 offset:2048\n"
    "s_waitcnt lgkmcnt(10)\n"
    "v_mfma_f32_16x16x32_bf16 v[84:87], v[36:39], v[172:175], v[84:87]\n"
    "v_bfe_u32 v209, v0, 4, 2\n"
    "v_lshl_add_u32 v208, v209, 1, v208\n"
    "ds_read_b64_tr_b16 v[28:29], v5 offset:25600\n"
    "v_mfma_f32_16x16x32_bf16 v[88:91], v[36:39], v[176:179], v[88:91]\n"
    "v_xor_b32 v3, 0x10000, v3\n"
    "ds_read_b64_tr_b16 v[30:31], v5 offset:27136\n"
    "s_add_u32 m0, s78, 2048\n"
    "s_nop 0\n"
    "buffer_load_dwordx4 v8, s[8:11], s73 offen lds\n"
    "s_waitcnt lgkmcnt(10)\n"
    "v_mfma_f32_16x16x32_bf16 v[92:95], v[40:43], v[172:175], v[92:95]\n"
    "ds_read_b64_tr_b16 v[32:33], v5 offset:19488\n"
    "s_lshl_b32 s64, s60, 13\n"
    "s_lshl_b32 s65, s7, 10\n"
    "s_add_u32 s64, s64, s65\n"
    "s_add_u32 s87, s64, 65536\n"
    "v_mfma_f32_16x16x32_bf16 v[96:99], v[40:43], v[176:179], v[96:99]\n"
    "ds_read_b64_tr_b16 v[34:35], v5 offset:21024\n"
    "s_waitcnt lgkmcnt(10)\n"
    "v_mfma_f32_16x16x32_bf16 v[100:103], v[44:47], v[172:175], v[100:103]\n"
    "ds_read_b64_tr_b16 v[36:37], v5 offset:25632\n"
    "s_and_b32 s64, s4, 1\n"
    "s_lshl_b32 s64, s64, 9\n"
    "s_and_b32 s65, s4, 6\n"
    "s_lshl_b32 s65, s65, 6\n"
    "v_mfma_f32_16x16x32_bf16 v[104:107], v[44:47], v[176:179], v[104:107]\n"
    "ds_read_b64_tr_b16 v[38:39], v5 offset:27168\n"
    "s_add_u32 s64, s64, s65\n"
    "s_add_u32 s64, s64, s87\n"
    "v_lshl_add_u32 v208, v208, 4, s64\n"
    "s_waitcnt lgkmcnt(10)\n"
    "v_mfma_f32_16x16x32_bf16 v[108:111], v[20:23], v[172:175], v[108:111]\n"
    "ds_read_b64_tr_b16 v[40:41], v5 offset:19520\n"
    "v_mfma_f32_16x16x32_bf16 v[112:115], v[20:23], v[176:179], v[112:115]\n"
    "ds_read_b64_tr_b16 v[42:43], v5 offset:21056\n"
    "s_mov_b32 exec_lo, 0x00030003\n"
    "s_mov_b32 exec_hi, 0x00030003\n"
    "global_store_dwordx4 v208, v[200:203], s[58:59] sc1\n"
    "s_mov_b64 exec, -1\n"
    "s_add_u32 m0, s78, 3072\n"
    "s_nop 0\n"
    "buffer_load_dwordx4 v9, s[8:11], s73 offen lds\n"
    "s_add_u32 s72, s72, 128\n"
    "s_and_b32 s72, s72, 1023\n"
    "s_add_u32 s73, s72, 16384\n"
    "s_xor_b32 s78, s78, 0x10000\n"
    "s_waitcnt lgkmcnt(9)\n"
    "v_mfma_f32_16x16x32_bf16 v[116:119], v[24:27], v[172:175], v[116:119]\n"
    "ds_read_b64_tr_b16 v[44:45], v5 offset:25664\n"
    "v_mfma_f32_16x16x32_bf16 v[120:123], v[24:27], v[176:179], v[120:123]\n"
    "ds_read_b64_tr_b16 v[46:47], v5 offset:27200\n"
    "s_mov_b32 s66, 3\n"
    "L_p1_%=:\n"
    "s_waitcnt lgkmcnt(8)\n"
    "v_mfma_f32_16x16x32_bf16 v[124:127], v[28:31], v[172:175], v[124:127]\n"
    "s_mov_b32 s68, s67\n"
    "s_add_u32 s67, s67, 0x10000\n"
    "s_cmp_eq_u32 s67, 0x30000\n"
    "s_cselect_b32 s67, 0, s67\n"
    "s_sub_u32 s69, s67, s68\n"
    "v_mfma_f32_16x16x32_bf16 v[128:131], v[28:31], v[176:179], v[128:131]\n"
    "v_add_u32 v1, s69, v1\n"
    "v_add_u32 v2, s69, v2\n"
    "s_waitcnt lgkmcnt(6)\n"
    "v_mfma_f32_16x16x32_bf16 v[132:135], v[32:35], v[172:175], v[132:135]\n"
    "v_add_u32 v5, s69, v5\n"
    "v_mfma_f32_16x16x32_bf16 v[136:139], v[32:35], v[176:179], v[136:139]\n"
    "s_waitcnt vmcnt(63)\n"
    "s_waitcnt lgkmcnt(0)\n"
    "s_barrier\n"
    "ds_read_b64_tr_b16 v[20:21], v1 offset:0\n"
    "ds_read_b64_tr_b16 v[22:23], v1 offset:4096\n"
    "v_mfma_f32_16x16x32_bf16 v[140:143], v[36:39], v[172:175], v[140:143]\n"
    "ds_read_b64_tr_b16 v[24:25], v2 offset:0\n"
    "ds_read_b64_tr_b16 v[26:27], v2 offset:4096\n"
    "v_mfma_f32_16x16x32_bf16 v[144:147], v[36:39], v[176:179], v[144:147]\n"
    "ds_read_b64_tr_b16 v[28:29], v1 offset:512\n"
    "ds_read_b64_tr_b16 v[30:31], v1 offset:4608\n"
    "v_mfma_f32_16x16x32_bf16 v[148:151], v[40:43], v[172:175], v[148:151]\n"
    "ds_read_b64_tr_b16 v[32:33], v2 offset:512\n"
    "ds_read_b64_tr_b16 v[34:35], v2 offset:4608\n"
    "v_mfma_f32_16x16x32_bf16 v[152:155], v[40:43], v[176:179], v[152:155]\n"
    "s_add_u32 m0, s68, s74\n"
    "s_nop 0\n"
    "buffer_load_dwordx4 v10, s[12:15], s70 offen lds\n"
    "v_mfma_f32_16x16x32_bf16 v[156:159], v[44:47], v[172:175], v[156:159]\n"
    "ds_read_b64_tr_b16 v[36:37], v1 offset:1024\n"
    "v_mfma_f32_16x16x32_bf16 v[160:163], v[44:47], v[176:179], v[160:163]\n"
    "ds_read_b64_tr_b16 v[38:39], v1 offset:5120\n"
    "s_waitcnt lgkmcnt(8)\n"
    "v_mfma_f32_16x16x32_bf16 v[52:55], v[20:23], v[164:167], v[52:55]\n"
    "ds_read_b64_tr_b16 v[40:41], v2 offset:1024\n"
    "v_mfma_f32_16x16x32_bf16 v[56:59], v[20:23], v[168:171], v[56:59]\n"
    "ds_read_b64_tr_b16 v[42:43], v2 offset:5120\n"
    "ds_read_b128 v[172:175], v4 offset:0\n"
    "s_waitcnt lgkmcnt(9)\n"
    "v_mfma_f32_16x16x32_bf16 v[60:63], v[24:27], v[164:167], v[60:63]\n"
    "ds_read_b64_tr_b16 v[44:45], v1 offset:1536\n"
    "v_mfma_f32_16x16x32_bf16 v[64:67], v[24:27], v[168:171], v[64:67]\n"
    "ds_read_b64_tr_b16 v[46:47], v1 offset:5632\n"
    "ds_read_b128 v[176:179], v4 offset:2048\n"
    "s_waitcnt lgkmcnt(10)\n"
    "v_mfma_f32_16x16x32_bf16 v[68:71], v[28:31], v[164:167], v[68:71]\n"
    "ds_read_b64_tr_b16 v[20:21], v2 offset:1536\n"
    "v_mfma_f32_16x16x32_bf16 v[72:75], v[28:31], v[168:171], v[72:75]\n"
    "v_xor_b32 v4, 0x10000, v4\n"
    "ds_read_b64_tr_b16 v[22:23], v2 offset:5632\n"
    "s_add_u32 s62, s70, 128\n"
    "s_add_u32 s63, s68, s74\n"
    "s_add_u32 m0, s63, 1024\n"
    "s_nop 0\n"
    "buffer_load_dwordx4 v10, s[12:15], s62 offen lds\n"
    "s_add_u32 s70, s70, 524288\n"
    "s_and_b32 s70, s70, 0x3fffff\n"
    "s_waitcnt lgkmcnt(10)\n"
    "v_mfma_f32_16x16x32_bf16 v[76:79], v[32:35], v[164:167], v[76:79]\n"
    "ds_read_b64_tr_b16 v[24:25], v5 offset:16384\n"
    "v_mfma_f32_16x16x32_bf16 v[80:83], v[32:35], v[168:171], v[80:83]\n"
    "ds_read_b64_tr_b16 v[26:27], v5 offset:17920\n"
    "s_waitcnt lgkmcnt(10)\n"
    "v_mfma_f32_16x16x32_bf16 v[84:87], v[36:39], v[164:167], v[84:87]\n"
    "ds_read_b64_tr_b16 v[28:29], v5 offset:22528\n"
    "v_mfma_f32_16x16x32_bf16 v[88:91], v[36:39], v[168:171], v[88:91]\n"
    "ds_read_b64_tr_b16 v[30:31], v5 offset:24064\n"
    "s_waitcnt lgkmcnt(10)\n"
    "v_mfma_f32_16x16x32_bf16 v[92:95], v[40:43], v[164:167], v[92:95]\n"
    "ds_read_b64_tr_b16 v[32:33], v5 offset:16416\n"
    "v_mfma_f32_16x16x32_bf16 v[96:99], v[40:43], v[168:171], v[96:99]\n"
    "ds_read_b64_tr_b16 v[34:35], v5 offset:17952\n"
    "s_add_u32 m0, s68, s75\n"
    "s_nop 0\n"
    "buffer_load_dwordx4 v11, s[16:19], s71 offen lds\n"
    "s_waitcnt lgkmcnt(9)\n"
    "v_mfma_f32_16x16x32_bf16 v[100:103], v[44:47], v[164:167], v[100:103]\n"
    "ds_read_b64_tr_b16 v[36:37], v5 offset:22560\n"
    "v_mfma_f32_16x16x32_bf16 v[104:107], v[44:47], v[168:171], v[104:107]\n"
    "ds_read_b64_tr_b16 v[38:39], v5 offset:24096\n"
    "s_waitcnt lgkmcnt(8)\n"
    "v_mfma_f32_16x16x32_bf16 v[108:111], v[20:23], v[164:167], v[108:111]\n"
    "ds_read_b64_tr_b16 v[40:41], v5 offset:16448\n"
    "v_mfma_f32_16x16x32_bf16 v[112:115], v[20:23], v[168:171], v[112:115]\n"
    "ds_read_b64_tr_b16 v[42:43], v5 offset:17984\n"
    "s_waitcnt lgkmcnt(8)\n"
    "v_mfma_f32_16x16x32_bf16 v[116:119], v[24:27], v[164:167], v[116:119]\n"
    "ds_read_b64_tr_b16 v[44:45], v5 offset:22592\n"
    "v_mfma_f32_16x16x32_bf16 v[120:123], v[24:27], v[168:171], v[120:123]\n"
    "ds_read_b64_tr_b16 v[46:47], v5 offset:24128\n"
    "s_waitcnt lgkmcnt(8)\n"
    "v_mfma_f32_16x16x32_bf16 v[124:127], v[28:31], v[164:167], v[124:127]\n"
    "ds_read_b64_tr_b16 v[20:21], v1 offset:8192\n"
    "v_mfma_f32_16x16x32_bf16 v[128:131], v[28:31], v[168:171], v[128:131]\n"
    "ds_read_b64_tr_b16 v[22:23], v1 offset:12288\n"
    "s_add_u32 m0, s68, s76\n"
    "s_nop 0\n"
    "buffer_load_dwordx4 v11, s[20:23], s71 offen lds\n"
    "s_add_u32 s71, s71, 176128\n"
    "s_sub_u32 s62, s71, 1409024\n"
    "s_cselect_b32 s71, s71, s62\n"
    "s_waitcnt lgkmcnt(8)\n"
    "v_mfma_f32_16x16x32_bf16 v[132:135], v[32:35], v[164:167], v[132:135]\n"
    "ds_read_b64_tr_b16 v[24:25], v2 offset:8192\n"
    "v_mfma_f32_16x16x32_bf16 v[136:139], v[32:35], v[168:171], v[136:139]\n"
    "ds_read_b64_tr_b16 v[26:27], v2 offset:12288\n"
    "s_waitcnt lgkmcnt(8)\n"
    "v_mfma_f32_16x16x32_bf16 v[140:143], v[36:39], v[164:167], v[140:143]\n"
    "ds_read_b64_tr_b16 v[28:29], v1 offset:8704\n"
    "v_mfma_f32_16x16x32_bf16 v[144:147], v[36:39], v[168:171], v[144:147]\n"
    "ds_read_b64_tr_b16 v[30:31], v1 offset:12800\n"
    "s_waitcnt lgkmcnt(8)\n"
    "v_mfma_f32_16x16x32_bf16 v[148:151], v[40:43], v[164:167], v[148:151]\n"
    "ds_read_b64_tr_b16 v[32:33], v2 offset:8704\n"
    "v_mfma_f32_16x16x32_bf16 v[152:155], v[40:43], v[168:171], v[152:155]\n"
    "ds_read_b64_tr_b16 v[34:35], v2 offset:12800\n"
    "s_mov_b32 m0, s78\n"
    "s_nop 0\n"
    "buffer_load_dwordx4 v8, s[8:11], s72 offen lds\n"
    "s_waitcnt lgkmcnt(8)\n"
    "v_mfma_f32_16x16x32_bf16 v[156:159], v[44:47], v[164:167], v[156:159]\n"
    "ds_read_b64_tr_b16 v[36:37], v1 offset:9216\n"
    "v_mfma_f32_16x16x32_bf16 v[160:163], v[44:47], v[168:171], v[160:163]\n"
    "ds_read_b64_tr_b16 v[38:39], v1 offset:13312\n"
    "s_waitcnt lgkmcnt(8)\n"
    "v_mfma_f32_16x16x32_bf16 v[52:55], v[20:23], v[172:175], v[52:55]\n"
    "ds_read_b64_tr_b16 v[40:41], v2 offset:9216\n"
    "v_mfma_f32_16x16x32_bf16 v[56:59], v[20:23], v[176:179], v[56:59]\n"
    "ds_read_b64_tr_b16 v[42:43], v2 offset:13312\n"
    "s_waitcnt lgkmcnt(8)\n"
    "v_mfma_f32_16x16x32_bf16 v[60:63], v[24:27], v[172:175], v[60:63]\n"
    "ds_read_b64_tr_b16 v[44:45], v1 offset:9728\n"
    "v_mfma_f32_16x16x32_bf16 v[64:67], v[24:27], v[176:179], v[64:67]\n"
    "ds_read_b64_tr_b16 v[46:47], v1 offset:13824\n"
    "s_add_u32 m0, s78, 1024\n"
    "s_nop 0\n"
    "buffer_load_dwordx4 v9, s[8:11], s72 offen lds\n"
    "s_waitcnt lgkmcnt(8)\n"
    "v_mfma_f32_16x16x32_bf16 v[68:71], v[28:31], v[172:175], v[68:71]\n"
    "ds_read_b64_tr_b16 v[20:21], v2 offset:9728\n"
    "v_mfma_f32_16x16x32_bf16 v[72:75], v[28:31], v[176:179], v[72:75]\n"
    "ds_read_b64_tr_b16 v[22:23], v2 offset:13824\n"
    "s_waitcnt vmcnt(6)\n"
    "ds_read_b128 v[164:167], v3 offset:0\n"
    "s_waitcnt lgkmcnt(9)\n"
    "v_mfma_f32_16x16x32_bf16 v[76:79], v[32:35], v[172:175], v[76:79]\n"
    "ds_read_b64_tr_b16 v[24:25], v5 offset:19456\n"
    "v_mfma_f32_16x16x32_bf16 v[80:83], v[32:35], v[176:179], v[80:83]\n"
    "ds_read_b64_tr_b16 v[26:27], v5 offset:20992\n"
    "ds_read_b128 v[168:171], v3 offset:2048\n"
    "s_waitcnt lgkmcnt(10)\n"
    "v_mfma_f32_16x16x32_bf16 v[84:87], v[36:39], v[172:175], v[84:87]\n"
    "ds_read_b64_tr_b16 v[28:29], v5 offset:25600\n"
    "v_mfma_f32_16x16x32_bf16 v[88:91], v[36:39], v[176:179], v[88:91]\n"
    "v_xor_b32 v3, 0x10000, v3\n"
    "ds_read_b64_tr_b16 v[30:31], v5 offset:27136\n"
    "s_add_u32 m0, s78, 2048\n"
    "s_nop 0\n"
    "buffer_load_dwordx4 v8, s[8:11], s73 offen lds\n"
    "s_waitcnt lgkmcnt(10)\n"
    "v_mfma_f32_16x16x32_bf16 v[92:95], v[40:43], v[172:175], v[92:95]\n"
    "ds_read_b64_tr_b16 v[32:33], v5 offset:19488\n"
    "v_mfma_f32_16x16x32_bf16 v[96:99], v[40:43], v[176:179], v[96:99]\n"
    "ds_read_b64_tr_b16 v[34:35], v5 offset:21024\n"
    "s_waitcnt lgkmcnt(10)\n"
    "v_mfma_f32_16x16x32_bf16 v[100:103], v[44:47], v[172:175], v[100:103]\n"
    "ds_read_b64_tr_b16 v[36:37], v5 offset:25632\n"
    "v_mfma_f32_16x16x32_bf16 v[104:107], v[44:47], v[176:179], v[104:107]\n"
    "ds_read_b64_tr_b16 v[38:39], v5 offset:27168\n"
    "s_waitcnt lgkmcnt(10)\n"
    "v_mfma_f32_16x16x32_bf16 v[108:111], v[20:23], v[172:175], v[108:111]\n"
    "ds_read_b64_tr_b16 v[40:41], v5 offset:19520\n"
    "v_mfma_f32_16x16x32_bf16 v[112:115], v[20:23], v[176:179], v[112:115]\n"
    "ds_read_b64_tr_b16 v[42:43], v5 offset:21056\n"
    "s_add_u32 m0, s78, 3072\n"
    "s_nop 0\n"
    "buffer_load_dwordx4 v9, s[8:11], s73 offen lds\n"
    "s_add_u32 s72, s72, 128\n"
    "s_and_b32 s72, s72, 1023\n"
    "s_add_u32 s73, s72, 16384\n"
    "s_xor_b32 s78, s78, 0x10000\n"
    "s_waitcnt lgkmcnt(9)\n"
    "v_mfma_f32_16x16x32_bf16 v[116:119], v[24:27], v[172:175], v[116:119]\n"
    "ds_read_b64_tr_b16 v[44:45], v5 offset:25664\n"
    "v_mfma_f32_16x16x32_bf16 v[120:123], v[24:27], v[176:179], v[120:123]\n"
    "ds_read_b64_tr_b16 v[46:47], v5 offset:27200\n"
    "s_sub_u32 s66, s66, 1\n"
    "s_cmp_lg_u32 s66, 0\n"
    "s_cbranch_scc1 L_p1_%=\n"
    "s_waitcnt lgkmcnt(8)\n"
    "v_mfma_f32_16x16x32_bf16 v[124:127], v[28:31], v[172:175], v[124:127]\n"
    "s_mov_b32 s68, s67\n"
    "s_add_u32 s67, s67, 0x10000\n"
    "s_cmp_eq_u32 s67, 0x30000\n"
    "s_cselect_b32 s67, 0, s67\n"
    "s_sub_u32 s69, s67, s68\n"
    "v_mfma_f32_16x16x32_bf16 v[128:131], v[28:31], v[176:179], v[128:131]\n"
    "v_add_u32 v1, s69, v1\n"
    "v_add_u32 v2, s69, v2\n"
    "s_waitcnt lgkmcnt(6)\n"
    "v_mfma_f32_16x16x32_bf16 v[132:135], v[32:35], v[172:175], v[132:135]\n"
    "v_add_u32 v5, s69, v5\n"
    "v_mfma_f32_16x16x32_bf16 v[136:139], v[32:35], v[176:179], v[136:139]\n"
    "v_add_u32 v248, s87, v15\n"
    "s_waitcnt vmcnt(63)\n"
    "s_waitcnt lgkmcnt(0)\n"
    "s_barrier\n"
    "ds_read_b64_tr_b16 v[20:21], v1 offset:0\n"
    "ds_read_b64_tr_b16 v[22:23], v1 offset:4096\n"
    "v_mfma_f32_16x16x32_bf16 v[140:143], v[36:39], v[172:175], v[140:143]\n"
    "v_and_b32 v250, 0xf0, v15\n"
    "ds_read_b64_tr_b16 v[24:25], v2 offset:0\n"
    "ds_read_b64_tr_b16 v[26:27], v2 offset:4096\n"
    "s_lshl_b32 s64, s61, 12\n"
    "s_lshl_b32 s65, s7, 9\n"
    "s_add_u32 s64, s64, s65\n"
    "s_add_u32 s64, s64, 8192\n"
    "v_add_u32 v250, s64, v250\n"
    "v_mfma_f32_16x16x32_bf16 v[144:147], v[36:39], v[176:179], v[144:147]\n"
    "global_load_dwordx4 v[188:191], v248, s[58:59] sc1\n"
    "global_load_dwordx4 v[196:199], v250, s[58:59] sc1\n"
    "global_load_dwordx4 v[200:203], v250, s[58:59] offset:256 sc1\n"
    "ds_read_b64_tr_b16 v[28:29], v1 offset:512\n"
    "ds_read_b64_tr_b16 v[30:31], v1 offset:4608\n"
    "v_mfma_f32_16x16x32_bf16 v[148:151], v[40:43], v[172:175], v[148:151]\n"
    "ds_read_b64_tr_b16 v[32:33], v2 offset:512\n"
    "ds_read_b64_tr_b16 v[34:35], v2 offset:4608\n"
    "v_mfma_f32_16x16x32_bf16 v[152:155], v[40:43], v[176:179], v[152:155]\n"
    "v_mfma_f32_16x16x32_bf16 v[156:159], v[44:47], v[172:175], v[156:159]\n"
    "ds_read_b64_tr_b16 v[36:37], v1 offset:1024\n"
    "v_mfma_f32_16x16x32_bf16 v[160:163], v[44:47], v[176:179], v[160:163]\n"
    "ds_read_b64_tr_b16 v[38:39], v1 offset:5120\n"
    "s_add_u32 m0, s68, s74\n"
    "s_nop 0\n"
    "buffer_load_dwordx4 v10, s[12:15], s70 offen lds\n"
    "s_waitcnt lgkmcnt(8)\n"
    "v_mfma_f32_16x16x32_bf16 v[52:55], v[20:23], v[164:167], v[52:55]\n"
    "ds_read_b64_tr_b16 v[40:41], v2 offset:1024\n"
    "v_mfma_f32_16x16x32_bf16 v[56:59], v[20:23], v[168:171], v[56:59]\n"
    "ds_read_b64_tr_b16 v[42:43], v2 offset:5120\n"
    "ds_read_b128 v[172:175], v4 offset:0\n"
    "s_waitcnt lgkmcnt(9)\n"
    "v_mfma_f32_16x16x32_bf16 v[60:63], v[24:27], v[164:167], v[60:63]\n"
    "ds_read_b64_tr_b16 v[44:45], v1 offset:1536\n"
    "v_mfma_f32_16x16x32_bf16 v[64:67], v[24:27], v[168:171], v[64:67]\n"
    "ds_read_b64_tr_b16 v[46:47], v1 offset:5632\n"
    "ds_read_b128 v[176:179], v4 offset:2048\n"
    "s_waitcnt lgkmcnt(10)\n"
    "v_mfma_f32_16x16x32_bf16 v[68:71], v[28:31], v[164:167], v[68:71]\n"
    "ds_read_b64_tr_b16 v[20:21], v2 offset:1536\n"
    "v_mfma_f32_16x16x32_bf16 v[72:75], v[28:31], v[168:171], v[72:75]\n"
    "v_xor_b32 v4, 0x10000, v4\n"
    "ds_read_b64_tr_b16 v[22:23], v2 offset:5632\n"
    "s_add_u32 s62, s70, 128\n"
    "s_add_u32 s63, s68, s74\n"
    "s_add_u32 m0, s63, 1024\n"
    "s_nop 0\n"
    "buffer_load_dwordx4 v10, s[12:15], s62 offen lds\n"
    "s_add_u32 s70, s70, 524288\n"
    "s_and_b32 s70, s70, 0x3fffff\n"
    "s_waitcnt lgkmcnt(10)\n"
    "v_mfma_f32_16x16x32_bf16 v[76:79], v[32:35], v[164:167], v[76:79]\n"
    "ds_read_b64_tr_b16 v[24:25], v5 offset:16384\n"
    "v_mfma_f32_16x16x32_bf16 v[80:83], v[32:35], v[168:171], v[80:83]\n"
    "ds_read_b64_tr_b16 v[26:27], v5 offset:17920\n"
    "s_waitcnt lgkmcnt(10)\n"
    "v_mfma_f32_16x16x32_bf16 v[84:87], v[36:39], v[164:167], v[84:87]\n"
    "ds_read_b64_tr_b16 v[28:29], v5 offset:22528\n"
    "v_mfma_f32_16x16x32_bf16 v[88:91], v[36:39], v[168:171], v[88:91]\n"
    "ds_read_b64_tr_b16 v[30:31], v5 offset:24064\n"
    "s_waitcnt lgkmcnt(10)\n"
    "v_mfma_f32_16x16x32_bf16 v[92:95], v[40:43], v[164:167], v[92:95]\n"
    "ds_read_b64_tr_b16 v[32:33], v5 offset:16416\n"
    "v_mfma_f32_16x16x32_bf16 v[96:99], v[40:43], v[168:171], v[96:99]\n"
    "ds_read_b64_tr_b16 v[34:35], v5 offset:17952\n"
    "s_add_u32 m0, s68, s75\n"
    "s_nop 0\n"
    "buffer_load_dwordx4 v11, s[16:19], s71 offen lds\n"
    "s_waitcnt lgkmcnt(9)\n"
    "v_mfma_f32_16x16x32_bf16 v[100:103], v[44:47], v[164:167], v[100:103]\n"
    "ds_read_b64_tr_b16 v[36:37], v5 offset:22560\n"
    "v_mfma_f32_16x16x32_bf16 v[104:107], v[44:47], v[168:171], v[104:107]\n"
    "ds_read_b64_tr_b16 v[38:39], v5 offset:24096\n"
    "s_waitcnt lgkmcnt(8)\n"
    "v_mfma_f32_16x16x32_bf16 v[108:111], v[20:23], v[164:167], v[108:111]\n"
    "s_waitcnt vmcnt(5)\n"
    "v_cmp_ne_u32 vcc, s84, v189\n"
    "v_cmp_ne_u32 s[64:65], s84, v191\n"
    "ds_read_b64_tr_b16 v[40:41], v5 offset:16448\n"
    "s_or_b64 vcc, vcc, s[64:65]\n"
    "v_mfma_f32_16x16x32_bf16 v[112:115], v[20:23], v[168:171], v[112:115]\n"
    "s_waitcnt vmcnt(4)\n"
    "v_cmp_ne_u32 s[64:65], s84, v197\n"
    "ds_read_b64_tr_b16 v[42:43], v5 offset:17984\n"
    "s_or_b64 vcc, vcc, s[64:65]\n"
    "v_cmp_ne_u32 s[64:65], s84, v199\n"
    "s_nop 0\n"
    "s_or_b64 vcc, vcc, s[64:65]\n"
    "s_waitcnt lgkmcnt(8)\n"
    "v_mfma_f32_16x16x32_bf16 v[116:119], v[24:27], v[164:167], v[116:119]\n"
    "s_waitcnt vmcnt(3)\n"
    "v_cmp_ne_u32 s[64:65], s84, v201\n"
    "ds_read_b64_tr_b16 v[44:45], v5 offset:22592\n"
    "s_or_b64 vcc, vcc, s[64:65]\n"
    "v_cmp_ne_u32 s[64:65], s84, v203\n"
    "s_nop 0\n"
    "s_or_b64 vcc, vcc, s[64:65]\n"
    "v_mfma_f32_16x16x32_bf16 v[120:123], v[24:27], v[168:171], v[120:123]\n"
    "ds_read_b64_tr_b16 v[46:47], v5 offset:24128\n"
    "s_cbranch_vccnz L_retry_%=\n"
    "L_retried_%=:\n"
    "s_waitcnt lgkmcnt(8)\n"
    "v_mfma_f32_16x16x32_bf16 v[124:127], v[28:31], v[164:167], v[124:127]\n"
    "ds_read_b64_tr_b16 v[20:21], v1 offset:8192\n"
    "v_mfma_f32_16x16x32_bf16 v[128:131], v[28:31], v[168:171], v[128:131]\n"
    "v_add_f32_dpp v188, v188, v188 row_ror:8 row_mask:0xf bank_mask:0xf\n"
    "ds_read_b64_tr_b16 v[22:23], v1 offset:12288\n"
    "s_add_u32 m0, s68, s76\n"
    "s_nop 0\n"
    "buffer_load_dwordx4 v11, s[20:23], s71 offen lds\n"
    "s_add_u32 s71, s71, 176128\n"
    "s_sub_u32 s62, s71, 1409024\n"
    "s_cselect_b32 s71, s71, s62\n"
    "s_waitcnt lgkmcnt(8)\n"
    "v_mfma_f32_16x16x32_bf16 v[132:135], v[32:35], v[164:167], v[132:135]\n"
    "v_add_f32_dpp v190, v190, v190 row_ror:8 row_mask:0xf bank_mask:0xf\n"
    "ds_read_b64_tr_b16 v[24:25], v2 offset:8192\n"
    "v_mfma_f32_16x16x32_bf16 v[136:139], v[32:35], v[168:171], v[136:139]\n"
    "v_mov_b32 v192, v188\n"
    "v_mov_b32 v193, v190\n"
    "ds_read_b64_tr_b16 v[26:27], v2 offset:12288\n"
    "s_waitcnt lgkmcnt(8)\n"
    "v_mfma_f32_16x16x32_bf16 v[140:143], v[36:39], v[164:167], v[140:143]\n"
    "ds_read_b64_tr_b16 v[28:29], v1 offset:8704\n"
    "v_mfma_f32_16x16x32_bf16 v[144:147], v[36:39], v[168:171], v[144:147]\n"
    "ds_read_b64_tr_b16 v[30:31], v1 offset:12800\n"
    "s_waitcnt lgkmcnt(8)\n"
    "v_mfma_f32_16x16x32_bf16 v[148:151], v[40:43], v[164:167], v[148:151]\n"
    "v_permlane16_swap_b32 v188, v192\n"
    "ds_read_b64_tr_b16 v[32:33], v2 offset:8704\n"
    "v_mfma_f32_16x16x32_bf16 v[152:155], v[40:43], v[168:171], v[152:155]\n"
    "v_permlane16_swap_b32 v190, v193\n"
    "ds_read_b64_tr_b16 v[34:35], v2 offset:12800\n"
    "s_mov_b32 m0, s78\n"
    "s_nop 0\n"
    "buffer_load_dwordx4 v8, s[8:11], s72 offen lds\n"
    "s_waitcnt lgkmcnt(8)\n"
    "v_mfma_f32_16x16x32_bf16 v[156:159], v[44:47], v[164:167], v[156:159]\n"
    "ds_read_b64_tr_b16 v[36:37], v1 offset:9216\n"
    "v_mfma_f32_16x16x32_bf16 v[160:163], v[44:47], v[168:171], v[160:163]\n"
    "v_add_f32 v188, v188, v192\n"
    "v_add_f32 v189, v190, v193\n"
    "ds_read_b64_tr_b16 v[38:39], v1 offset:13312\n"
    "s_waitcnt lgkmcnt(8)\n"
    "v_mfma_f32_16x16x32_bf16 v[52:55], v[20:23], v[172:175], v[52:55]\n"
    "v_and_b32 v248, 7, v0\n"
    "v_bfe_u32 v249, v0, 5, 1\n"
    "ds_read_b64_tr_b16 v[40:41], v2 offset:9216\n"
    "s_lshl_b32 s64, s7, 6\n"
    "s_add_u32 s64, s64, 161792\n"
    "v_mfma_f32_16x16x32_bf16 v[56:59], v[20:23], v[176:179], v[56:59]\n"
    "ds_read_b64_tr_b16 v[42:43], v2 offset:13312\n"
    "s_waitcnt lgkmcnt(8)\n"
    "v_mfma_f32_16x16x32_bf16 v[60:63], v[24:27], v[172:175], v[60:63]\n"
    "v_lshl_add_u32 v248, v248, 3, s64\n"
    "v_lshl_add_u32 v248, v249, 9, v248\n"
    "ds_read_b64_tr_b16 v[44:45], v1 offset:9728\n"
    "v_mfma_f32_16x16x32_bf16 v[64:67], v[24:27], v[176:179], v[64:67]\n"
    "ds_read_b64_tr_b16 v[46:47], v1 offset:13824\n"
    "ds_write_b64 v248, v[188:189]\n"
    "s_add_u32 m0, s78, 1024\n"
    "s_nop 0\n"
    "buffer_load_dwordx4 v9, s[8:11], s72 offen lds\n"
    "s_waitcnt lgkmcnt(9)\n"
    "v_mfma_f32_16x16x32_bf16 v[68:71], v[28:31], v[172:175], v[68:71]\n"
    "v_sub_f32 v220, 0, v196\n"
    "v_mul_f32 v216, 0x3e0293ee, v198\n"
    "ds_read_b64_tr_b16 v[20:21], v2 offset:9728\n"
    "v_mfma_f32_16x16x32_bf16 v[72:75], v[28:31], v[176:179], v[72:75]\n"
    "ds_read_b64_tr_b16 v[22:23], v2 offset:13824\n"
    "s_waitcnt vmcnt(63)\n"
    "ds_read_b128 v[164:167], v3 offset:0\n"
    "s_waitcnt lgkmcnt(10)\n"
    "v_mfma_f32_16x16x32_bf16 v[76:79], v[32:35], v[172:175], v[76:79]\n"
    "v_mul_f32 v240, 0xbfb8aa3b, v198\n"
    "v_mov_b32 v217, v216\n"
    "ds_read_b64_tr_b16 v[24:25], v5 offset:19456\n"
    "v_mfma_f32_16x16x32_bf16 v[80:83], v[32:35], v[176:179], v[80:83]\n"
    "v_rcp_f32 v242, v198\n"
    "ds_read_b64_tr_b16 v[26:27], v5 offset:20992\n"
    "ds_read_b128 v[168:171], v3 offset:2048\n"
    "s_waitcnt lgkmcnt(11)\n"
    "v_mfma_f32_16x16x32_bf16 v[84:87], v[36:39], v[172:175], v[84:87]\n"
    "ds_read_b64_tr_b16 v[28:29], v5 offset:25600\n"
    "v_mfma_f32_16x16x32_bf16 v[88:91], v[36:39], v[176:179], v[88:91]\n"
    "v_mov_b32 v241, v240\n"
    "v_mov_b32 v243, v242\n"
    "v_xor_b32 v3, 0x10000, v3\n"
    "ds_read_b64_tr_b16 v[30:31], v5 offset:27136\n"
    "s_add_u32 m0, s78, 2048\n"
    "s_nop 0\n"
    "buffer_load_dwordx4 v8, s[8:11], s73 offen lds\n"
    "s_waitcnt lgkmcnt(11)\n"
    "v_mfma_f32_16x16x32_bf16 v[92:95], v[40:43], v[172:175], v[92:95]\n"
    "v_sub_f32 v221, 0, v200\n"
    "v_mul_f32 v218, 0x3e0293ee, v202\n"
    "ds_read_b64_tr_b16 v[32:33], v5 offset:19488\n"
    "v_mfma_f32_16x16x32_bf16 v[96:99], v[40:43], v[176:179], v[96:99]\n"
    "v_mul_f32 v244, 0xbfb8aa3b, v202\n"
    "v_mov_b32 v219, v218\n"
    "ds_read_b64_tr_b16 v[34:35], v5 offset:21024\n"
    "ds_read_b128 v[204:207], v4 offset:0\n"
    "s_waitcnt lgkmcnt(12)\n"
    "v_mfma_f32_16x16x32_bf16 v[100:103], v[44:47], v[172:175], v[100:103]\n"
    "ds_read_b64_tr_b16 v[36:37], v5 offset:25632\n"
    "v_mfma_f32_16x16x32_bf16 v[104:107], v[44:47], v[176:179], v[104:107]\n"
    "v_rcp_f32 v246, v202\n"
    "ds_read_b64_tr_b16 v[38:39], v5 offset:27168\n"
    "ds_read_b128 v[208:211], v4 offset:2048\n"
    "s_waitcnt lgkmcnt(12)\n"
    "v_mfma_f32_16x16x32_bf16 v[108:111], v[20:23], v[172:175], v[108:111]\n"
    "v_mov_b32 v245, v244\n"
    "v_mov_b32 v247, v246\n"
    "ds_read_b64_tr_b16 v[40:41], v5 offset:19520\n"
    "v_mfma_f32_16x16x32_bf16 v[112:115], v[20:23], v[176:179], v[112:115]\n"
    "v_xor_b32 v4, 0x10000, v4\n"
    "ds_read_b64_tr_b16 v[42:43], v5 offset:21056\n"
    "s_add_u32 m0, s78, 3072\n"
    "s_nop 0\n"
    "buffer_load_dwordx4 v9, s[8:11], s73 offen lds\n"
    "s_add_u32 s72, s72, 128\n"
    "s_and_b32 s72, s72, 1023\n"
    "s_add_u32 s73, s72, 16384\n"
    "s_xor_b32 s78, s78, 0x10000\n"
    "s_waitcnt lgkmcnt(11)\n"
    "v_mfma_f32_16x16x32_bf16 v[116:119], v[24:27], v[172:175], v[116:119]\n"
    "ds_read_b64_tr_b16 v[44:45], v5 offset:25664\n"
    "v_mfma_f32_16x16x32_bf16 v[120:123], v[24:27], v[176:179], v[120:123]\n"
    "ds_read_b64_tr_b16 v[46:47], v5 offset:27200\n"
    "s_waitcnt lgkmcnt(10)\n"
    "v_mfma_f32_16x16x32_bf16 v[124:127], v[28:31], v[172:175], v[124:127]\n"
    "s_mov_b32 s68, s67\n"
    "s_add_u32 s67, s67, 0x10000\n"
    "s_cmp_eq_u32 s67, 0x30000\n"
    "s_cselect_b32 s67, 0, s67\n"
    "s_sub_u32 s69, s67, s68\n"
    "v_mfma_f32_16x16x32_bf16 v[128:131], v[28:31], v[176:179], v[128:131]\n"
    "v_add_u32 v1, s69, v1\n"
    "v_add_u32 v2, s69, v2\n"
    "v_bfe_u32 v6, v0, 1, 3\n"
    "s_waitcnt lgkmcnt(8)\n"
    "v_mfma_f32_16x16x32_bf16 v[132:135], v[32:35], v[172:175], v[132:135]\n"
    "v_add_u32 v5, s69, v5\n"
    "v_bfe_u32 v7, v0, 5, 1\n"
    "v_mfma_f32_16x16x32_bf16 v[136:139], v[32:35], v[176:179], v[136:139]\n"
    "v_xor_b32 v6, v6, v7\n"
    "s_waitcnt vmcnt(63)\n"
    "s_waitcnt lgkmcnt(0)\n"
    "s_barrier\n"
    "ds_read_b64_tr_b16 v[20:21], v1 offset:0\n"
    "ds_read_b64_tr_b16 v[22:23], v1 offset:4096\n"
    "v_mfma_f32_16x16x32_bf16 v[140:143], v[36:39], v[172:175], v[140:143]\n"
    "v_lshlrev_b32 v6, 4, v6\n"
    "ds_read_b64_tr_b16 v[24:25], v2 offset:0\n"
    "ds_read_b64_tr_b16 v[26:27], v2 offset:4096\n"
    "v_mfma_f32_16x16x32_bf16 v[144:147], v[36:39], v[176:179], v[144:147]\n"
    "v_and_b32 v7, 0x780, v16\n"
    "ds_read_b128 v[180:183], v19 offset:320\n"
    "ds_read_b64_tr_b16 v[28:29], v1 offset:512\n"
    "ds_read_b64_tr_b16 v[30:31], v1 offset:4608\n"
    "v_mfma_f32_16x16x32_bf16 v[148:151], v[40:43], v[172:175], v[148:151]\n"
    "v_add_u32 v6, v6, v7\n"
    "ds_read_b64_tr_b16 v[32:33], v2 offset:512\n"
    "ds_read_b64_tr_b16 v[34:35], v2 offset:4608\n"
    "v_mfma_f32_16x16x32_bf16 v[152:155], v[40:43], v[176:179], v[152:155]\n"
    "v_bfe_u32 v7, v0, 4, 1\n"
    "s_mov_b32 m0, s88\n"
    "s_nop 0\n"
    "buffer_load_dwordx4 v12, s[24:27], s82 offen lds\n"
    "v_mfma_f32_16x16x32_bf16 v[156:159], v[44:47], v[172:175], v[156:159]\n"
    "v_lshl_add_u32 v6, v7, 3, v6\n"
    "ds_read_b64_tr_b16 v[36:37], v1 offset:1024\n"
    "v_mfma_f32_16x16x32_bf16 v[160:163], v[44:47], v[176:179], v[160:163]\n"
    "ds_read_b64_tr_b16 v[38:39], v1 offset:5120\n"
    "s_waitcnt lgkmcnt(9)\n"
    "v_mfma_f32_16x16x32_bf16 v[52:55], v[20:23], v[164:167], v[52:55]\n"
    "s_waitcnt lgkmcnt(6)\n"
    "v_fmac_f32 v92, v220, v180\n"
    "ds_read_b64_tr_b16 v[40:41], v2 offset:1024\n"
    "v_mfma_f32_16x16x32_bf16 v[56:59], v[20:23], v[168:171], v[56:59]\n"
    "v_fmac_f32 v93, v220, v181\n"
    "ds_read_b64_tr_b16 v[42:43], v2 offset:5120\n"
    "ds_read_b128 v[184:187], v19 offset:832\n"
    "v_mfma_f32_16x16x32_bf16 v[60:63], v[24:27], v[164:167], v[60:63]\n"
    "v_fmac_f32 v94, v220, v182\n"
    "ds_read_b64_tr_b16 v[44:45], v1 offset:1536\n"
    "v_mfma_f32_16x16x32_bf16 v[64:67], v[24:27], v[168:171], v[64:67]\n"
    "v_fmac_f32 v95, v220, v183\n"
    "ds_read_b64_tr_b16 v[46:47], v1 offset:5632\n"
    "s_add_u32 m0, s88, 1024\n"
    "s_nop 0\n"
    "buffer_load_dwordx4 v13, s[24:27], s82 offen lds\n"
    "s_add_u32 s82, s82, 16384\n"
    "s_waitcnt lgkmcnt(9)\n"
    "v_mfma_f32_16x16x32_bf16 v[68:71], v[28:31], v[164:167], v[68:71]\n"
    "v_fmac_f32 v96, v221, v180\n"
    "ds_read_b64_tr_b16 v[20:21], v2 offset:1536\n"
    "v_mfma_f32_16x16x32_bf16 v[72:75], v[28:31], v[168:171], v[72:75]\n"
    "v_fmac_f32 v97, v221, v181\n"
    "ds_read_b64_tr_b16 v[22:23], v2 offset:5632\n"
    "s_waitcnt lgkmcnt(9)\n"
    "v_mfma_f32_16x16x32_bf16 v[76:79], v[32:35], v[164:167], v[76:79]\n"
    "v_fmac_f32 v98, v221, v182\n"
    "s_waitcnt lgkmcnt(4)\n"
    "v_fmac_f32 v156, v220, v184\n"
    "ds_read_b64_tr_b16 v[24:25], v5 offset:16384\n"
    "v_mfma_f32_16x16x32_bf16 v[80:83], v[32:35], v[168:171], v[80:83]\n"
    "v_fmac_f32 v99, v221, v183\n"
    "v_fmac_f32 v157, v220, v185\n"
    "ds_read_b64_tr_b16 v[26:27], v5 offset:17920\n"
    "ds_read_b128 v[180:183], v19 offset:0\n"
    "v_mfma_f32_16x16x32_bf16 v[84:87], v[36:39], v[164:167], v[84:87]\n"
    "v_fmac_f32 v158, v220, v186\n"
    "ds_read_b64_tr_b16 v[28:29], v5 offset:22528\n"
    "v_mfma_f32_16x16x32_bf16 v[88:91], v[36:39], v[168:171], v[88:91]\n"
    "v_fmac_f32 v159, v220, v187\n"
    "ds_read_b64_tr_b16 v[30:31], v5 offset:24064\n"
    "s_add_u32 m0, s88, 16384\n"
    "s_nop 0\n"
    "buffer_load_dwordx4 v14, s[28:31], s83 offen lds\n"
    "v_mfma_f32_16x16x32_bf16 v[92:95], v[40:43], v[164:167], v[92:95]\n"
    "v_fmac_f32 v160, v221, v184\n"
    "ds_read_b64_tr_b16 v[32:33], v5 offset:16416\n"
    "v_mfma_f32_16x16x32_bf16 v[96:99], v[40:43], v[168:171], v[96:99]\n"
    "v_fmac_f32 v161, v221, v185\n"
    "ds_read_b64_tr_b16 v[34:35], v5 offset:17952\n"
    "s_waitcnt lgkmcnt(9)\n"
    "v_mfma_f32_16x16x32_bf16 v[100:103], v[44:47], v[164:167], v[100:103]\n"
    "v_fmac_f32 v162, v221, v186\n"
    "s_waitcnt lgkmcnt(4)\n"
    "v_fmac_f32 v52, v220, v180\n"
    "ds_read_b64_tr_b16 v[36:37], v5 offset:22560\n"
    "v_mfma_f32_16x16x32_bf16 v[104:107], v[44:47], v[168:171], v[104:107]\n"
    "v_fmac_f32 v163, v221, v187\n"
    "v_fmac_f32 v53, v220, v181\n"
    "ds_read_b64_tr_b16 v[38:39], v5 offset:24096\n"
    "ds_read_b128 v[184:187], v19 offset:64\n"
    "v_mfma_f32_16x16x32_bf16 v[108:111], v[20:23], v[164:167], v[108:111]\n"
    "v_fmac_f32 v54, v220, v182\n"
    "ds_read_b64_tr_b16 v[40:41], v5 offset:16448\n"
    "v_mfma_f32_16x16x32_bf16 v[112:115], v[20:23], v[168:171], v[112:115]\n"
    "v_fmac_f32 v55, v220, v183\n"
    "ds_read_b64_tr_b16 v[42:43], v5 offset:17984\n"
    "s_add_u32 s62, s83, 128\n"
    "s_add_u32 m0, s88, 17408\n"
    "s_nop 0\n"
    "buffer_load_dwordx4 v14, s[28:31], s62 offen lds\n"
    "s_add_u32 s83, s83, 16384\n"
    "s_add_u32 s88, s88, 0x8000\n"
    "s_sub_u32 s62, s88, 0x28000\n"
    "s_cselect_b32 s88, s88, s62\n"
    "v_mfma_f32_16x16x32_bf16 v[116:119], v[24:27], v[164:167], v[116:119]\n"
    "v_fmac_f32 v56, v221, v180\n"
    "ds_read_b64_tr_b16 v[44:45], v5 offset:22592\n"
    "v_mfma_f32_16x16x32_bf16 v[120:123], v[24:27], v[168:171], v[120:123]\n"
    "v_fmac_f32 v57, v221, v181\n"
    "ds_read_b64_tr_b16 v[46:47], v5 offset:24128\n"
    "s_waitcnt lgkmcnt(9)\n"
    "v_mfma_f32_16x16x32_bf16 v[124:127], v[28:31], v[164:167], v[124:127]\n"
    "v_fmac_f32 v58, v221, v182\n"
    "s_waitcnt lgkmcnt(4)\n"
    "v_fmac_f32 v60, v220, v184\n"
    "ds_read_b64_tr_b16 v[20:21], v1 offset:8192\n"
    "v_mfma_f32_16x16x32_bf16 v[128:131], v[28:31], v[168:171], v[128:131]\n"
    "v_fmac_f32 v59, v221, v183\n"
    "v_fmac_f32 v61, v220, v185\n"
    "ds_read_b64_tr_b16 v[22:23], v1 offset:12288\n"
    "ds_read_b128 v[180:183], v19 offset:192\n"
    "v_mfma_f32_16x16x32_bf16 v[132:135], v[32:35], v[164:167], v[132:135]\n"
    "v_fmac_f32 v62, v220, v186\n"
    "ds_read_b64_tr_b16 v[24:25], v2 offset:8192\n"
    "v_mfma_f32_16x16x32_bf16 v[136:139], v[32:35], v[168:171], v[136:139]\n"
    "v_fmac_f32 v63, v220, v187\n"
    "ds_read_b64_tr_b16 v[26:27], v2 offset:12288\n"
    "v_mfma_f32_16x16x32_bf16 v[140:143], v[36:39], v[164:167], v[140:143]\n"
    "v_fmac_f32 v64, v221, v184\n"
    "ds_read_b64_tr_b16 v[28:29], v1 offset:8704\n"
    "v_mfma_f32_16x16x32_bf16 v[144:147], v[36:39], v[168:171], v[144:147]\n"
    "v_fmac_f32 v65, v221, v185\n"
    "ds_read_b64_tr_b16 v[30:31], v1 offset:12800\n"
    "s_mov_b32 m0, s88\n"
    "s_nop 0\n"
    "buffer_load_dwordx4 v12, s[24:27], s82 offen lds\n"
    "s_waitcnt lgkmcnt(9)\n"
    "v_mfma_f32_16x16x32_bf16 v[148:151], v[40:43], v[164:167], v[148:151]\n"
    "v_fmac_f32 v66, v221, v186\n"
    "s_waitcnt lgkmcnt(4)\n"
    "v_fmac_f32 v76, v220, v180\n"
    "ds_read_b64_tr_b16 v[32:33], v2 offset:8704\n"
    "v_mfma_f32_16x16x32_bf16 v[152:155], v[40:43], v[168:171], v[152:155]\n"
    "v_fmac_f32 v67, v221, v187\n"
    "v_fmac_f32 v77, v220, v181\n"
    "ds_read_b64_tr_b16 v[34:35], v2 offset:12800\n"
    "ds_read_b128 v[184:187], v19 offset:384\n"
    "v_mfma_f32_16x16x32_bf16 v[156:159], v[44:47], v[164:167], v[156:159]\n"
    "v_fmac_f32 v78, v220, v182\n"
    "ds_read_b64_tr_b16 v[36:37], v1 offset:9216\n"
    "v_mfma_f32_16x16x32_bf16 v[160:163], v[44:47], v[168:171], v[160:163]\n"
    "v_fmac_f32 v79, v220, v183\n"
    "ds_read_b64_tr_b16 v[38:39], v1 offset:13312\n"
    "v_mfma_f32_16x16x32_bf16 v[52:55], v[20:23], v[204:207], v[52:55]\n"
    "v_fmac_f32 v80, v221, v180\n"
    "ds_read_b64_tr_b16 v[40:41], v2 offset:9216\n"
    "v_mfma_f32_16x16x32_bf16 v[56:59], v[20:23], v[208:211], v[56:59]\n"
    "v_fmac_f32 v81, v221, v181\n"
    "ds_read_b64_tr_b16 v[42:43], v2 offset:13312\n"
    "s_add_u32 m0, s88, 1024\n"
    "s_nop 0\n"
    "buffer_load_dwordx4 v13, s[24:27], s82 offen lds\n"
    "s_add_u32 s82, s82, 16384\n"
    "s_waitcnt lgkmcnt(9)\n"
    "v_mfma_f32_16x16x32_bf16 v[60:63], v[24:27], v[204:207], v[60:63]\n"
    "v_fmac_f32 v82, v221, v182\n"
    "s_waitcnt lgkmcnt(4)\n"
    "v_fmac_f32 v100, v220, v184\n"
    "ds_read_b64_tr_b16 v[44:45], v1 offset:9728\n"
    "v_mfma_f32_16x16x32_bf16 v[64:67], v[24:27], v[208:211], v[64:67]\n"
    "v_fmac_f32 v83, v221, v183\n"
    "v_fmac_f32 v101, v220, v185\n"
    "ds_read_b64_tr_b16 v[46:47], v1 offset:13824\n"
    "ds_read_b128 v[180:183], v19 offset:768\n"
    "v_mfma_f32_16x16x32_bf16 v[68:71], v[28:31], v[204:207], v[68:71]\n"
    "v_fmac_f32 v102, v220, v186\n"
    "ds_read_b64_tr_b16 v[20:21], v2 offset:9728\n"
    "v_mfma_f32_16x16x32_bf16 v[72:75], v[28:31], v[208:211], v[72:75]\n"
    "v_fmac_f32 v103, v220, v187\n"
    "ds_read_b64_tr_b16 v[22:23], v2 offset:13824\n"
    "s_waitcnt vmcnt(6)\n"
    "ds_read_b128 v[164:167], v3 offset:0\n"
    "v_mfma_f32_16x16x32_bf16 v[76:79], v[32:35], v[204:207], v[76:79]\n"
    "v_fmac_f32 v104, v221, v184\n"
    "ds_read_b64_tr_b16 v[24:25], v5 offset:19456\n"
    "v_mfma_f32_16x16x32_bf16 v[80:83], v[32:35], v[208:211], v[80:83]\n"
    "v_fmac_f32 v105, v221, v185\n"
    "ds_read_b64_tr_b16 v[26:27], v5 offset:20992\n"
    "s_add_u32 m0, s88, 16384\n"
    "s_nop 0\n"
    "buffer_load_dwordx4 v14, s[28:31], s83 offen lds\n"
    "ds_read_b128 v[168:171], v3 offset:2048\n"
    "s_waitcnt lgkmcnt(11)\n"
    "v_mfma_f32_16x16x32_bf16 v[84:87], v[36:39], v[204:207], v[84:87]\n"
    "v_fmac_f32 v106, v221, v186\n"
    "s_waitcnt lgkmcnt(6)\n"
    "v_fmac_f32 v148, v220, v180\n"
    "ds_read_b64_tr_b16 v[28:29], v5 offset:25600\n"
    "v_mfma_f32_16x16x32_bf16 v[88:91], v[36:39], v[208:211], v[88:91]\n"
    "v_fmac_f32 v107, v221, v187\n"
    "v_fmac_f32 v149, v220, v181\n"
    "v_xor_b32 v3, 0x10000, v3\n"
    "ds_read_b64_tr_b16 v[30:31], v5 offset:27136\n"
    "v_mfma_f32_16x16x32_bf16 v[92:95], v[40:43], v[204:207], v[92:95]\n"
    "v_fmac_f32 v150, v220, v182\n"
    "ds_read_b64_tr_b16 v[32:33], v5 offset:19488\n"
    "v_mfma_f32_16x16x32_bf16 v[96:99], v[40:43], v[208:211], v[96:99]\n"
    "v_fmac_f32 v151, v220, v183\n"
    "ds_read_b64_tr_b16 v[34:35], v5 offset:21024\n"
    "v_mfma_f32_16x16x32_bf16 v[100:103], v[44:47], v[204:207], v[100:103]\n"
    "v_fmac_f32 v152, v221, v180\n"
    "ds_read_b64_tr_b16 v[36:37], v5 offset:25632\n"
    "v_mfma_f32_16x16x32_bf16 v[104:107], v[44:47], v[208:211], v[104:107]\n"
    "v_fmac_f32 v153, v221, v181\n"
    "ds_read_b64_tr_b16 v[38:39], v5 offset:27168\n"
    "s_add_u32 s62, s83, 128\n"
    "s_add_u32 m0, s88, 17408\n"
    "s_nop 0\n"
    "buffer_load_dwordx4 v14, s[28:31], s62 offen lds\n"
    "s_add_u32 s83, s83, 16384\n"
    "s_add_u32 s88, s88, 0x8000\n"
    "s_sub_u32 s62, s88, 0x28000\n"
    "s_cselect_b32 s88, s88, s62\n"
    "s_waitcnt lgkmcnt(10)\n"
    "v_mfma_f32_16x16x32_bf16 v[108:111], v[20:23], v[204:207], v[108:111]\n"
    "v_fmac_f32 v154, v221, v182\n"
    "ds_read_b64_tr_b16 v[40:41], v5 offset:19520\n"
    "v_mfma_f32_16x16x32_bf16 v[112:115], v[20:23], v[208:211], v[112:115]\n"
    "v_fmac_f32 v155, v221, v183\n"
    "ds_read_b64_tr_b16 v[42:43], v5 offset:21056\n"
    "s_waitcnt lgkmcnt(9)\n"
    "v_mfma_f32_16x16x32_bf16 v[116:119], v[24:27], v[204:207], v[116:119]\n"
    "ds_read_b64_tr_b16 v[44:45], v5 offset:25664\n"
    "v_mfma_f32_16x16x32_bf16 v[120:123], v[24:27], v[208:211], v[120:123]\n"
    "ds_read_b64_tr_b16 v[46:47], v5 offset:27200\n"
    "s_waitcnt lgkmcnt(8)\n"
    "v_mfma_f32_16x16x32_bf16 v[124:127], v[28:31], v[204:207], v[124:127]\n"
    "s_mov_b32 s68, s67\n"
    "s_add_u32 s67, s67, 0x10000\n"
    "s_cmp_eq_u32 s67, 0x30000\n"
    "s_cselect_b32 s67, 0, s67\n"
    "s_sub_u32 s69, s67, s68\n"
    "ds_read_b128 v[180:183], v19 offset:128\n"
    "v_mfma_f32_16x16x32_bf16 v[128:131], v[28:31], v[208:211], v[128:131]\n"
    "v_add_u32 v1, s69, v1\n"
    "v_add_u32 v2, s69, v2\n"
    "v_xor_b32 v3, 0, v6\n"
    "s_waitcnt lgkmcnt(7)\n"
    "v_mfma_f32_16x16x32_bf16 v[132:135], v[32:35], v[204:207], v[132:135]\n"
    "v_add_u32 v5, s69, v5\n"
    "v_mfma_f32_16x16x32_bf16 v[136:139], v[32:35], v[208:211], v[136:139]\n"
    "v_xor_b32 v8, 64, v6\n"
    "s_waitcnt vmcnt(63)\n"
    "s_waitcnt lgkmcnt(0)\n"
    "s_barrier\n"
    "ds_read_b64_tr_b16 v[20:21], v1 offset:0\n"
    "ds_read_b64_tr_b16 v[22:23], v1 offset:4096\n"
    "v_mfma_f32_16x16x32_bf16 v[140:143], v[36:39], v[204:207], v[140:143]\n"
    "v_xor_b32 v9, 96, v6\n"
    "ds_read_b64_tr_b16 v[24:25], v2 offset:0\n"
    "ds_read_b64_tr_b16 v[26:27], v2 offset:4096\n"
    "v_mfma_f32_16x16x32_bf16 v[144:147], v[36:39], v[208:211], v[144:147]\n"
    "v_fmac_f32 v68, v220, v180\n"
    "ds_read_b64_tr_b16 v[28:29], v1 offset:512\n"
    "ds_read_b64_tr_b16 v[30:31], v1 offset:4608\n"
    "v_mfma_f32_16x16x32_bf16 v[148:151], v[40:43], v[204:207], v[148:151]\n"
    "v_fmac_f32 v69, v220, v181\n"
    "ds_read_b64_tr_b16 v[32:33], v2 offset:512\n"
    "ds_read_b64_tr_b16 v[34:35], v2 offset:4608\n"
    "ds_read_b128 v[184:187], v19 offset:640\n"
    "v_mfma_f32_16x16x32_bf16 v[152:155], v[40:43], v[208:211], v[152:155]\n"
    "v_fmac_f32 v70, v220, v182\n"
    "s_mov_b32 m0, s88\n"
    "s_nop 0\n"
    "buffer_load_dwordx4 v12, s[24:27], s82 offen lds\n"
    "v_mfma_f32_16x16x32_bf16 v[156:159], v[44:47], v[204:207], v[156:159]\n"
    "v_fmac_f32 v71, v220, v183\n"
    "ds_read_b64_tr_b16 v[36:37], v1 offset:1024\n"
    "v_mfma_f32_16x16x32_bf16 v[160:163], v[44:47], v[208:211], v[160:163]\n"
    "v_fmac_f32 v72, v221, v180\n"
    "ds_read_b64_tr_b16 v[38:39], v1 offset:5120\n"
    "s_waitcnt lgkmcnt(9)\n"
    "v_mfma_f32_16x16x32_bf16 v[52:55], v[20:23], v[164:167], v[52:55]\n"
    "v_fmac_f32 v73, v221, v181\n"
    "ds_read_b64_tr_b16 v[40:41], v2 offset:1024\n"
    "v_mfma_f32_16x16x32_bf16 v[56:59], v[20:23], v[168:171], v[56:59]\n"
    "v_fmac_f32 v74, v221, v182\n"
    "s_waitcnt lgkmcnt(3)\n"
    "v_fmac_f32 v132, v220, v184\n"
    "ds_read_b64_tr_b16 v[42:43], v2 offset:5120\n"
    "ds_read_b128 v[172:175], v4 offset:0\n"
    "v_mfma_f32_16x16x32_bf16 v[60:63], v[24:27], v[164:167], v[60:63]\n"
    "v_fmac_f32 v75, v221, v183\n"
    "v_fmac_f32 v133, v220, v185\n"
    "ds_read_b64_tr_b16 v[44:45], v1 offset:1536\n"
    "ds_read_b128 v[180:183], v19 offset:576\n"
    "v_mfma_f32_16x16x32_bf16 v[64:67], v[24:27], v[168:171], v[64:67]\n"
    "v_fmac_f32 v134, v220, v186\n"
    "ds_read_b64_tr_b16 v[46:47], v1 offset:5632\n"
    "s_add_u32 m0, s88, 1024\n"
    "s_nop 0\n"
    "buffer_load_dwordx4 v13, s[24:27], s82 offen lds\n"
    "s_add_u32 s82, s82, 16384\n"
    "ds_read_b128 v[176:179], v4 offset:2048\n"
    "v_mfma_f32_16x16x32_bf16 v[68:71], v[28:31], v[164:167], v[68:71]\n"
    "v_fmac_f32 v135, v220, v187\n"
    "ds_read_b64_tr_b16 v[20:21], v2 offset:1536\n"
    "v_mfma_f32_16x16x32_bf16 v[72:75], v[28:31], v[168:171], v[72:75]\n"
    "v_fmac_f32 v136, v221, v184\n"
    "v_xor_b32 v4, 0x10000, v4\n"
    "ds_read_b64_tr_b16 v[22:23], v2 offset:5632\n"
    "v_mfma_f32_16x16x32_bf16 v[76:79], v[32:35], v[164:167], v[76:79]\n"
    "v_fmac_f32 v137, v221, v185\n"
    "ds_read_b64_tr_b16 v[24:25], v5 offset:16384\n"
    "v_mfma_f32_16x16x32_bf16 v[80:83], v[32:35], v[168:171], v[80:83]\n"
    "v_fmac_f32 v138, v221, v186\n"
    "s_waitcnt lgkmcnt(5)\n"
    "v_fmac_f32 v124, v220, v180\n"
    "v_xor_b32 v4, 32, v6\n"
    "ds_read_b64_tr_b16 v[26:27], v5 offset:17920\n"
    "v_mfma_f32_16x16x32_bf16 v[84:87], v[36:39], v[164:167], v[84:87]\n"
    "v_fmac_f32 v139, v221, v187\n"
    "v_fmac_f32 v125, v220, v181\n"
    "ds_read_b64_tr_b16 v[28:29], v5 offset:22528\n"
    "ds_read_b128 v[184:187], v19 offset:704\n"
    "v_mfma_f32_16x16x32_bf16 v[88:91], v[36:39], v[168:171], v[88:91]\n"
    "v_fmac_f32 v126, v220, v182\n"
    "ds_read_b64_tr_b16 v[30:31], v5 offset:24064\n"
    "s_add_u32 m0, s88, 16384\n"
    "s_nop 0\n"
    "buffer_load_dwordx4 v14, s[28:31], s83 offen lds\n"
    "v_mfma_f32_16x16x32_bf16 v[92:95], v[40:43], v[164:167], v[92:95]\n"
    "v_fmac_f32 v127, v220, v183\n"
    "ds_read_b64_tr_b16 v[32:33], v5 offset:16416\n"
    "v_mfma_f32_16x16x32_bf16 v[96:99], v[40:43], v[168:171], v[96:99]\n"
    "v_fmac_f32 v128, v221, v180\n"
    "ds_read_b64_tr_b16 v[34:35], v5 offset:17952\n"
    "s_waitcnt lgkmcnt(10)\n"
    "v_mfma_f32_16x16x32_bf16 v[100:103], v[44:47], v[164:167], v[100:103]\n"
    "v_fmac_f32 v129, v221, v181\n"
    "ds_read_b64_tr_b16 v[36:37], v5 offset:22560\n"
    "v_mfma_f32_16x16x32_bf16 v[104:107], v[44:47], v[168:171], v[104:107]\n"
    "v_fmac_f32 v130, v221, v182\n"
    "s_waitcnt lgkmcnt(4)\n"
    "v_fmac_f32 v140, v220, v184\n"
    "ds_read_b64_tr_b16 v[38:39], v5 offset:24096\n"
    "v_mfma_f32_16x16x32_bf16 v[108:111], v[20:23], v[164:167], v[108:111]\n"
    "v_fmac_f32 v131, v221, v183\n"
    "v_fmac_f32 v141, v220, v185\n"
    "ds_read_b64_tr_b16 v[40:41], v5 offset:16448\n"
    "ds_read_b128 v[180:183], v19 offset:256\n"
    "v_mfma_f32_16x16x32_bf16 v[112:115], v[20:23], v[168:171], v[112:115]\n"
    "v_fmac_f32 v142, v220, v186\n"
    "ds_read_b64_tr_b16 v[42:43], v5 offset:17984\n"
    "s_add_u32 s62, s83, 128\n"
    "s_add_u32 m0, s88, 17408\n"
    "s_nop 0\n"
    "buffer_load_dwordx4 v14, s[28:31], s62 offen lds\n"
    "s_add_u32 s83, s83, 16384\n"
    "s_add_u32 s88, s88, 0x8000\n"
    "s_sub_u32 s62, s88, 0x28000\n"
    "s_cselect_b32 s88, s88, s62\n"
    "v_mfma_f32_16x16x32_bf16 v[116:119], v[24:27], v[164:167], v[116:119]\n"
    "v_fmac_f32 v143, v220, v187\n"
    "ds_read_b64_tr_b16 v[44:45], v5 offset:22592\n"
    "v_mfma_f32_16x16x32_bf16 v[120:123], v[24:27], v[168:171], v[120:123]\n"
    "v_fmac_f32 v144, v221, v184\n"
    "ds_read_b64_tr_b16 v[46:47], v5 offset:24128\n"
    "s_waitcnt lgkmcnt(9)\n"
    "v_mfma_f32_16x16x32_bf16 v[124:127], v[28:31], v[164:167], v[124:127]\n"
    "v_fmac_f32 v145, v221, v185\n"
    "ds_read_b64_tr_b16 v[20:21], v1 offset:8192\n"
    "v_mfma_f32_16x16x32_bf16 v[128:131], v[28:31], v[168:171], v[128:131]\n"
    "v_fmac_f32 v146, v221, v186\n"
    "s_waitcnt lgkmcnt(4)\n"
    "v_fmac_f32 v84, v220, v180\n"
    "ds_read_b64_tr_b16 v[22:23], v1 offset:12288\n"
    "s_waitcnt vmcnt(4)\n"
    "s_barrier\n"
    "v_mfma_f32_16x16x32_bf16 v[132:135], v[32:35], v[164:167], v[132:135]\n"
    "v_fmac_f32 v147, v221, v187\n"
    "v_fmac_f32 v85, v220, v181\n"
    "ds_read_b64_tr_b16 v[24:25], v2 offset:8192\n"
    "ds_read_b128 v[184:187], v19 offset:512\n"
    "v_mfma_f32_16x16x32_bf16 v[136:139], v[32:35], v[168:171], v[136:139]\n"
    "v_fmac_f32 v86, v220, v182\n"
    "ds_read_b64_tr_b16 v[26:27], v2 offset:12288\n"
    "v_mfma_f32_16x16x32_bf16 v[140:143], v[36:39], v[164:167], v[140:143]\n"
    "v_fmac_f32 v87, v220, v183\n"
    "ds_read_b64_tr_b16 v[28:29], v1 offset:8704\n"
    "v_mfma_f32_16x16x32_bf16 v[144:147], v[36:39], v[168:171], v[144:147]\n"
    "v_fmac_f32 v88, v221, v180\n"
    "ds_read_b64_tr_b16 v[30:31], v1 offset:12800\n"
    "s_mov_b32 m0, s88\n"
    "s_nop 0\n"
    "buffer_load_dwordx4 v12, s[24:27], s82 offen lds\n"
    "s_waitcnt lgkmcnt(9)\n"
    "v_mfma_f32_16x16x32_bf16 v[148:151], v[40:43], v[164:167], v[148:151]\n"
    "v_fmac_f32 v89, v221, v181\n"
    "ds_read_b64_tr_b16 v[32:33], v2 offset:8704\n"
    "v_mfma_f32_16x16x32_bf16 v[152:155], v[40:43], v[168:171], v[152:155]\n"
    "v_fmac_f32 v90, v221, v182\n"
    "s_waitcnt lgkmcnt(4)\n"
    "v_fmac_f32 v116, v220, v184\n"
    "ds_read_b64_tr_b16 v[34:35], v2 offset:12800\n"
    "v_mfma_f32_16x16x32_bf16 v[156:159], v[44:47], v[164:167], v[156:159]\n"
    "v_fmac_f32 v91, v221, v183\n"
    "v_fmac_f32 v117, v220, v185\n"
    "ds_read_b64_tr_b16 v[36:37], v1 offset:9216\n"
    "ds_read_b128 v[180:183], v19 offset:448\n"
    "v_mfma_f32_16x16x32_bf16 v[160:163], v[44:47], v[168:171], v[160:163]\n"
    "v_fmac_f32 v118, v220, v186\n"
    "ds_read_b64_tr_b16 v[38:39], v1 offset:13312\n"
    "s_add_u32 m0, s88, 1024\n"
    "s_nop 0\n"
    "buffer_load_dwordx4 v13, s[24:27], s82 offen lds\n"
    "s_add_u32 s82, s82, 16384\n"
    "v_mfma_f32_16x16x32_bf16 v[52:55], v[20:23], v[172:175], v[52:55]\n"
    "v_fmac_f32 v119, v220, v187\n"
    "ds_read_b64_tr_b16 v[40:41], v2 offset:9216\n"
    "v_mfma_f32_16x16x32_bf16 v[56:59], v[20:23], v[176:179], v[56:59]\n"
    "v_fmac_f32 v120, v221, v184\n"
    "ds_read_b64_tr_b16 v[42:43], v2 offset:13312\n"
    "s_waitcnt lgkmcnt(9)\n"
    "v_mfma_f32_16x16x32_bf16 v[60:63], v[24:27], v[172:175], v[60:63]\n"
    "v_fmac_f32 v121, v221, v185\n"
    "ds_read_b64_tr_b16 v[44:45], v1 offset:9728\n"
    "v_mfma_f32_16x16x32_bf16 v[64:67], v[24:27], v[176:179], v[64:67]\n"
    "v_fmac_f32 v122, v221, v186\n"
    "s_waitcnt lgkmcnt(4)\n"
    "v_fmac_f32 v108, v220, v180\n"
    "ds_read_b64_tr_b16 v[46:47], v1 offset:13824\n"
    "s_add_u32 m0, s88, 16384\n"
    "s_nop 0\n"
    "buffer_load_dwordx4 v14, s[28:31], s83 offen lds\n"
    "v_mfma_f32_16x16x32_bf16 v[68:71], v[28:31], v[172:175], v[68:71]\n"
    "v_fmac_f32 v123, v221, v187\n"
    "v_fmac_f32 v109, v220, v181\n"
    "ds_read_b64_tr_b16 v[20:21], v2 offset:9728\n"
    "v_mfma_f32_16x16x32_bf16 v[72:75], v[28:31], v[176:179], v[72:75]\n"
    "v_fmac_f32 v110, v220, v182\n"
    "ds_read_b64_tr_b16 v[22:23], v2 offset:13824\n"
    "v_mfma_f32_16x16x32_bf16 v[76:79], v[32:35], v[172:175], v[76:79]\n"
    "v_fmac_f32 v111, v220, v183\n"
    "ds_read_b64_tr_b16 v[24:25], v5 offset:19456\n"
    "v_mfma_f32_16x16x32_bf16 v[80:83], v[32:35], v[176:179], v[80:83]\n"
    "v_fmac_f32 v112, v221, v180\n"
    "ds_read_b64_tr_b16 v[26:27], v5 offset:20992\n"
    "s_add_u32 s62, s83, 128\n"
    "s_add_u32 m0, s88, 17408\n"
    "s_nop 0\n"
    "buffer_load_dwordx4 v14, s[28:31], s62 offen lds\n"
    "s_add_u32 s83, s83, 16384\n"
    "s_add_u32 s88, s88, 0x8000\n"
    "s_sub_u32 s62, s88, 0x28000\n"
    "s_cselect_b32 s88, s88, s62\n"
    "s_waitcnt lgkmcnt(8)\n"
    "v_mfma_f32_16x16x32_bf16 v[84:87], v[36:39], v[172:175], v[84:87]\n"
    "v_fmac_f32 v113, v221, v181\n"
    "ds_read_b64_tr_b16 v[28:29], v5 offset:25600\n"
    "v_mfma_f32_16x16x32_bf16 v[88:91], v[36:39], v[176:179], v[88:91]\n"
    "v_fmac_f32 v114, v221, v182\n"
    "ds_read_b64_tr_b16 v[30:31], v5 offset:27136\n"
    "s_waitcnt lgkmcnt(8)\n"
    "v_mfma_f32_16x16x32_bf16 v[92:95], v[40:43], v[172:175], v[92:95]\n"
    "v_fmac_f32 v115, v221, v183\n"
    "v_pk_mul_f32 v[52:53], v[52:53], v[216:217]\n"
    "v_pk_mul_f32 v[54:55], v[54:55], v[216:217]\n"
    "ds_read_b64_tr_b16 v[32:33], v5 offset:19488\n"
    "v_mfma_f32_16x16x32_bf16 v[96:99], v[40:43], v[176:179], v[96:99]\n"
    "v_cvt_pk_bf16_f32 v180, v52, v53\n"
    "v_cvt_pk_bf16_f32 v181, v54, v55\n"
    "ds_read_b64_tr_b16 v[34:35], v5 offset:21024\n"
    "s_waitcnt lgkmcnt(8)\n"
    "v_mfma_f32_16x16x32_bf16 v[100:103], v[44:47], v[172:175], v[100:103]\n"
    "v_pk_mul_f32 v[60:61], v[60:61], v[216:217]\n"
    "v_pk_mul_f32 v[62:63], v[62:63], v[216:217]\n"
    "ds_read_b64_tr_b16 v[36:37], v5 offset:25632\n"
    "v_mfma_f32_16x16x32_bf16 v[104:107], v[44:47], v[176:179], v[104:107]\n"
    "v_cvt_pk_bf16_f32 v182, v60, v61\n"
    "v_cvt_pk_bf16_f32 v183, v62, v63\n"
    "ds_read_b64_tr_b16 v[38:39], v5 offset:27168\n"
    "s_waitcnt lgkmcnt(8)\n"
    "v_mfma_f32_16x16x32_bf16 v[108:111], v[20:23], v[172:175], v[108:111]\n"
    "v_pk_mul_f32 v[56:57], v[56:57], v[218:219]\n"
    "v_pk_mul_f32 v[58:59], v[58:59], v[218:219]\n"
    "ds_read_b64_tr_b16 v[40:41], v5 offset:19520\n"
    "v_mfma_f32_16x16x32_bf16 v[112:115], v[20:23], v[176:179], v[112:115]\n"
    "v_cvt_pk_bf16_f32 v184, v56, v57\n"
    "v_cvt_pk_bf16_f32 v185, v58, v59\n"
    "ds_read_b64_tr_b16 v[42:43], v5 offset:21056\n"
    "s_waitcnt lgkmcnt(8)\n"
    "v_mfma_f32_16x16x32_bf16 v[116:119], v[24:27], v[172:175], v[116:119]\n"
    "v_pk_mul_f32 v[64:65], v[64:65], v[218:219]\n"
    "v_pk_mul_f32 v[66:67], v[66:67], v[218:219]\n"
    "ds_read_b64_tr_b16 v[44:45], v5 offset:25664\n"
    "v_mfma_f32_16x16x32_bf16 v[120:123], v[24:27], v[176:179], v[120:123]\n"
    "ds_read_b64_tr_b16 v[46:47], v5 offset:27200\n"
    "s_waitcnt lgkmcnt(8)\n"
    "v_mfma_f32_16x16x32_bf16 v[124:127], v[28:31], v[172:175], v[124:127]\n"
    "v_cvt_pk_bf16_f32 v186, v64, v65\n"
    "v_cvt_pk_bf16_f32 v187, v66, v67\n"
    "v_pk_mul_f32 v[68:69], v[68:69], v[216:217]\n"
    "v_pk_mul_f32 v[70:71], v[70:71], v[216:217]\n"
    "v_mfma_f32_16x16x32_bf16 v[128:131], v[28:31], v[176:179], v[128:131]\n"
    "v_cvt_pk_bf16_f32 v188, v68, v69\n"
    "v_cvt_pk_bf16_f32 v189, v70, v71\n"
    "v_pk_mul_f32 v[76:77], v[76:77], v[216:217]\n"
    "v_pk_mul_f32 v[78:79], v[78:79], v[216:217]\n"
    "s_waitcnt lgkmcnt(6)\n"
    "v_mfma_f32_16x16x32_bf16 v[132:135], v[32:35], v[172:175], v[132:135]\n"
    "v_cvt_pk_bf16_f32 v190, v76, v77\n"
    "v_cvt_pk_bf16_f32 v191, v78, v79\n"
    "v_pk_mul_f32 v[72:73], v[72:73], v[218:219]\n"
    "v_pk_mul_f32 v[74:75], v[74:75], v[218:219]\n"
    "v_mfma_f32_16x16x32_bf16 v[136:139], v[32:35], v[176:179], v[136:139]\n"
    "v_cvt_pk_bf16_f32 v192, v72, v73\n"
    "v_cvt_pk_bf16_f32 v193, v74, v75\n"
    "v_pk_mul_f32 v[80:81], v[80:81], v[218:219]\n"
    "v_pk_mul_f32 v[82:83], v[82:83], v[218:219]\n"
    "s_waitcnt lgkmcnt(4)\n"
    "v_mfma_f32_16x16x32_bf16 v[140:143], v[36:39], v[172:175], v[140:143]\n"
    "v_cvt_pk_bf16_f32 v194, v80, v81\n"
    "v_cvt_pk_bf16_f32 v195, v82, v83\n"
    "v_pk_mul_f32 v[84:85], v[84:85], v[216:217]\n"
    "v_pk_mul_f32 v[86:87], v[86:87], v[216:217]\n"
    "v_mfma_f32_16x16x32_bf16 v[144:147], v[36:39], v[176:179], v[144:147]\n"
    "v_cvt_pk_bf16_f32 v196, v84, v85\n"
    "v_cvt_pk_bf16_f32 v197, v86, v87\n"
    "v_pk_mul_f32 v[92:93], v[92:93], v[216:217]\n"
    "v_pk_mul_f32 v[94:95], v[94:95], v[216:217]\n"
    "s_waitcnt lgkmcnt(2)\n"
    "v_mfma_f32_16x16x32_bf16 v[148:151], v[40:43], v[172:175], v[148:151]\n"
    "v_cvt_pk_bf16_f32 v198, v92, v93\n"
    "v_cvt_pk_bf16_f32 v199, v94, v95\n"
    "v_pk_mul_f32 v[88:89], v[88:89], v[218:219]\n"
    "v_pk_mul_f32 v[90:91], v[90:91], v[218:219]\n"
    "v_mfma_f32_16x16x32_bf16 v[152:155], v[40:43], v[176:179], v[152:155]\n"
    "v_cvt_pk_bf16_f32 v200, v88, v89\n"
    "v_cvt_pk_bf16_f32 v201, v90, v91\n"
    "v_pk_mul_f32 v[96:97], v[96:97], v[218:219]\n"
    "v_pk_mul_f32 v[98:99], v[98:99], v[218:219]\n"
    "s_waitcnt lgkmcnt(0)\n"
    "v_mfma_f32_16x16x32_bf16 v[156:159], v[44:47], v[172:175], v[156:159]\n"
    "v_cvt_pk_bf16_f32 v202, v96, v97\n"
    "v_cvt_pk_bf16_f32 v203, v98, v99\n"
    "v_pk_mul_f32 v[100:101], v[100:101], v[216:217]\n"
    "v_pk_mul_f32 v[102:103], v[102:103], v[216:217]\n"
    "v_mfma_f32_16x16x32_bf16 v[160:163], v[44:47], v[176:179], v[160:163]\n"
    "v_cvt_pk_bf16_f32 v204, v100, v101\n"
    "v_cvt_pk_bf16_f32 v205, v102, v103\n"
    "v_pk_mul_f32 v[108:109], v[108:109], v[216:217]\n"
    "v_pk_mul_f32 v[110:111], v[110:111], v[216:217]\n"
    "v_cvt_pk_bf16_f32 v206, v108, v109\n"
    "v_cvt_pk_bf16_f32 v207, v110, v111\n"
    "v_pk_mul_f32 v[104:105], v[104:105], v[218:219]\n"
    "v_pk_mul_f32 v[106:107], v[106:107], v[218:219]\n"
    "v_cvt_pk_bf16_f32 v208, v104, v105\n"
    "v_cvt_pk_bf16_f32 v209, v106, v107\n"
    "v_pk_mul_f32 v[112:113], v[112:113], v[218:219]\n"
    "v_pk_mul_f32 v[114:115], v[114:115], v[218:219]\n"
    "v_cvt_pk_bf16_f32 v210, v112, v113\n"
    "v_cvt_pk_bf16_f32 v211, v114, v115\n"
    "ds_read_b64 v[20:21], v3 offset:0\n"
    "ds_read_b64 v[22:23], v4 offset:0\n"
    "ds_read_b64 v[24:25], v3 offset:4096\n"
    "ds_read_b64 v[26:27], v4 offset:4096\n"
    "ds_read_b64 v[28:29], v8 offset:0\n"
    "ds_read_b64 v[30:31], v9 offset:0\n"
    "ds_read_b64 v[32:33], v8 offset:4096\n"
    "ds_read_b64 v[34:35], v9 offset:4096\n"
    "ds_read_b64 v[36:37], v3 offset:2048\n"
    "ds_read_b64 v[38:39], v4 offset:2048\n"
    "v_add_u32 v1, 16384, v17\n"
    "v_xor_b32 v2, 32, v1\n"
    "s_waitcnt lgkmcnt(8)\n"
    "v_mfma_f32_16x16x32_bf16 v[52:55], v[20:23], v[180:183], v[232:235]\n"
    "v_pk_mul_f32 v[116:117], v[116:117], v[240:241]\n"
    "v_pk_mul_f32 v[118:119], v[118:119], v[240:241]\n"
    "v_exp_f32 v116, v116\n"
    "ds_read_b64 v[40:41], v3 offset:6144\n"
    "v_mfma_f32_16x16x32_bf16 v[56:59], v[20:23], v[184:187], v[236:239]\n"
    "v_exp_f32 v117, v117\n"
    "ds_read_b64 v[42:43], v4 offset:6144\n"
    "s_waitcnt lgkmcnt(8)\n"
    "v_mfma_f32_16x16x32_bf16 v[60:63], v[24:27], v[180:183], v[232:235]\n"
    "v_exp_f32 v118, v118\n"
    "v_exp_f32 v119, v119\n"
    "s_and_b32 s62, s6, 7\n"
    "s_lshr_b32 s63, s6, 3\n"
    "s_lshr_b32 s70, s62, 1\n"
    "s_xor_b32 s70, s70, 2\n"
    "s_and_b32 s64, s62, 1\n"
    "s_lshl_b32 s64, s64, 3\n"
    "s_lshr_b32 s65, s63, 2\n"
    "s_add_u32 s72, s64, s65\n"
    "s_and_b32 s73, s63, 3\n"
    "s_lshl_b32 s75, s7, 11\n"
    "ds_read_b64 v[44:45], v8 offset:2048\n"
    "v_mfma_f32_16x16x32_bf16 v[64:67], v[24:27], v[184:187], v[236:239]\n"
    "v_pk_fma_f32 v[116:117], v[116:117], v[242:243], v[242:243]\n"
    "v_pk_fma_f32 v[118:119], v[118:119], v[242:243], v[242:243]\n"
    "ds_read_b64 v[46:47], v9 offset:2048\n"
    "s_waitcnt lgkmcnt(8)\n"
    "v_mfma_f32_16x16x32_bf16 v[52:55], v[28:31], v[188:191], v[52:55]\n"
    "v_rcp_f32 v116, v116\n"
    "v_rcp_f32 v117, v117\n"
    "s_lshl_b32 s62, s73, 8\n"
    "s_mul_i32 s63, s70, 360448\n"
    "s_add_u32 s63, s63, s62\n"
    "s_add_u32 s12, s54, s63\n"
    "s_addc_u32 s13, s55, 0\n"
    "s_and_b32 s13, s13, 0xffff\n"
    "s_sub_u32 s14, 1409024, s63\n"
    "s_mov_b32 s15, 0x20000\n"
    "ds_read_b64 v[48:49], v8 offset:6144\n"
    "v_mfma_f32_16x16x32_bf16 v[56:59], v[28:31], v[192:195], v[56:59]\n"
    "v_rcp_f32 v118, v118\n"
    "ds_read_b64 v[50:51], v9 offset:6144\n"
    "s_waitcnt lgkmcnt(8)\n"
    "v_mfma_f32_16x16x32_bf16 v[60:63], v[32:35], v[188:191], v[60:63]\n"
    "v_rcp_f32 v119, v119\n"
    "v_pk_mul_f32 v[124:125], v[124:125], v[116:117]\n"
    "v_pk_mul_f32 v[126:127], v[126:127], v[118:119]\n"
    "s_lshl_b32 s63, s70, 20\n"
    "s_add_u32 s63, s63, s62\n"
    "s_add_u32 s8, s48, s63\n"
    "s_addc_u32 s9, s49, 0\n"
    "s_and_b32 s9, s9, 0xffff\n"
    "s_sub_u32 s10, 4194304, s63\n"
    "s_mov_b32 s11, 0x20000\n"
    "ds_read_b64 v[20:21], v3 offset:8192\n"
    "v_mfma_f32_16x16x32_bf16 v[64:67], v[32:35], v[192:195], v[64:67]\n"
    "v_cvt_pk_bf16_f32 v108, v124, v125\n"
    "v_cvt_pk_bf16_f32 v109, v126, v127\n"
    "ds_read_b64 v[22:23], v4 offset:8192\n"
    "s_waitcnt lgkmcnt(8)\n"
    "v_mfma_f32_16x16x32_bf16 v[52:55], v[36:39], v[196:199], v[52:55]\n"
    "v_pk_mul_f32 v[120:121], v[120:121], v[244:245]\n"
    "v_pk_mul_f32 v[122:123], v[122:123], v[244:245]\n"
    "v_exp_f32 v120, v120\n"
    "s_mul_i32 s63, s72, 356352\n"
    "s_add_u32 s20, s58, s63\n"
    "s_addc_u32 s21, s59, 0\n"
    "s_add_u32 s20, s20, 17956864\n"
    "s_addc_u32 s21, s21, 0\n"
    "s_and_b32 s21, s21, 0xffff\n"
    "s_mov_b32 s22, 356352\n"
    "s_mov_b32 s23, 0x20000\n"
    "ds_read_b64 v[24:25], v3 offset:12288\n"
    "v_mfma_f32_16x16x32_bf16 v[56:59], v[36:39], v[200:203], v[56:59]\n"
    "v_exp_f32 v121, v121\n"
    "ds_read_b64 v[26:27], v4 offset:12288\n"
    "s_waitcnt lgkmcnt(8)\n"
    "v_mfma_f32_16x16x32_bf16 v[60:63], v[40:43], v[196:199], v[60:63]\n"
    "v_exp_f32 v122, v122\n"
    "v_exp_f32 v123, v123\n"
    "s_mul_i32 s63, s72, 1056768\n"
    "s_add_u32 s16, s58, s63\n"
    "s_addc_u32 s17, s59, 0\n"
    "s_add_u32 s16, s16, 1048576\n"
    "s_addc_u32 s17, s17, 0\n"
    "s_and_b32 s17, s17, 0xffff\n"
    "s_mov_b32 s18, 1056768\n"
    "s_mov_b32 s19, 0x20000\n"
    "ds_read_b64 v[28:29], v8 offset:8192\n"
    "v_mfma_f32_16x16x32_bf16 v[64:67], v[40:43], v[200:203], v[64:67]\n"
    "v_pk_fma_f32 v[120:121], v[120:121], v[246:247], v[246:247]\n"
    "v_pk_fma_f32 v[122:123], v[122:123], v[246:247], v[246:247]\n"
    "ds_read_b64 v[30:31], v9 offset:8192\n"
    "s_waitcnt lgkmcnt(8)\n"
    "v_mfma_f32_16x16x32_bf16 v[52:55], v[44:47], v[204:207], v[52:55]\n"
    "v_rcp_f32 v120, v120\n"
    "v_rcp_f32 v121, v121\n"
    "ds_read_b64 v[32:33], v8 offset:12288\n"
    "v_mfma_f32_16x16x32_bf16 v[56:59], v[44:47], v[208:211], v[56:59]\n"
    "v_rcp_f32 v122, v122\n"
    "ds_read_b64 v[34:35], v9 offset:12288\n"
    "s_waitcnt lgkmcnt(8)\n"
    "v_mfma_f32_16x16x32_bf16 v[60:63], v[48:51], v[204:207], v[60:63]\n"
    "v_rcp_f32 v123, v123\n"
    "v_pk_mul_f32 v[128:129], v[128:129], v[120:121]\n"
    "v_pk_mul_f32 v[130:131], v[130:131], v[122:123]\n"
    "ds_read_b64 v[36:37], v3 offset:10240\n"
    "v_mfma_f32_16x16x32_bf16 v[64:67], v[48:51], v[208:211], v[64:67]\n"
    "v_cvt_pk_bf16_f32 v110, v128, v129\n"
    "v_cvt_pk_bf16_f32 v111, v130, v131\n"
    "ds_read_b64 v[38:39], v4 offset:10240\n"
    "s_waitcnt lgkmcnt(8)\n"
    "v_mfma_f32_16x16x32_bf16 v[68:71], v[20:23], v[180:183], v[232:235]\n"
    "buffer_store_dwordx4 v[108:111], v15, s[36:39], 0 offen offset:0 sc1\n"
    "ds_read_b64 v[40:41], v3 offset:14336\n"
    "v_mfma_f32_16x16x32_bf16 v[72:75], v[20:23], v[184:187], v[236:239]\n"
    "v_exp_f32 v100, v52\n"
    "v_exp_f32 v101, v53\n"
    "ds_read_b64 v[42:43], v4 offset:14336\n"
    "s_waitcnt lgkmcnt(8)\n"
    "v_mfma_f32_16x16x32_bf16 v[76:79], v[24:27], v[180:183], v[232:235]\n"
    "v_exp_f32 v102, v56\n"
    "v_exp_f32 v103, v57\n"
    "ds_read_b64 v[44:45], v8 offset:10240\n"
    "v_mfma_f32_16x16x32_bf16 v[80:83], v[24:27], v[184:187], v[236:239]\n"
    "v_cvt_pk_bf16_f32 v84, v100, v101\n"
    "ds_read_b64 v[46:47], v9 offset:10240\n"
    "s_waitcnt lgkmcnt(8)\n"
    "v_mfma_f32_16x16x32_bf16 v[68:71], v[28:31], v[188:191], v[68:71]\n"
    "v_exp_f32 v104, v54\n"
    "v_exp_f32 v105, v55\n"
    "ds_read_b64 v[48:49], v8 offset:14336\n"
    "v_mfma_f32_16x16x32_bf16 v[72:75], v[28:31], v[192:195], v[72:75]\n"
    "v_exp_f32 v106, v58\n"
    "v_exp_f32 v107, v59\n"
    "ds_read_b64 v[50:51], v9 offset:14336\n"
    "s_waitcnt lgkmcnt(8)\n"
    "v_mfma_f32_16x16x32_bf16 v[76:79], v[32:35], v[188:191], v[76:79]\n"
    "v_cvt_pk_bf16_f32 v88, v102, v103\n"
    "v_cvt_pk_bf16_f32 v85, v104, v105\n"
    "v_add_u32 v3, 32768, v3\n"
    "v_add_u32 v4, 32768, v4\n"
    "v_mfma_f32_16x16x32_bf16 v[80:83], v[32:35], v[192:195], v[80:83]\n"
    "v_exp_f32 v100, v60\n"
    "v_exp_f32 v101, v61\n"
    "v_add_u32 v8, 32768, v8\n"
    "v_add_u32 v9, 32768, v9\n"
    "s_waitcnt lgkmcnt(6)\n"
    "v_mfma_f32_16x16x32_bf16 v[68:71], v[36:39], v[196:199], v[68:71]\n"
    "v_exp_f32 v102, v64\n"
    "ds_read_b64 v[20:21], v3 offset:0\n"
    "v_mfma_f32_16x16x32_bf16 v[72:75], v[36:39], v[200:203], v[72:75]\n"
    "v_exp_f32 v103, v65\n"
    "v_cvt_pk_bf16_f32 v89, v106, v107\n"
    "v_cvt_pk_bf16_f32 v86, v100, v101\n"
    "ds_read_b64 v[22:23], v4 offset:0\n"
    "s_waitcnt lgkmcnt(6)\n"
    "v_mfma_f32_16x16x32_bf16 v[76:79], v[40:43], v[196:199], v[76:79]\n"
    "v_exp_f32 v104, v62\n"
    "v_exp_f32 v105, v63\n"
    "ds_read_b64 v[24:25], v3 offset:4096\n"
    "v_mfma_f32_16x16x32_bf16 v[80:83], v[40:43], v[200:203], v[80:83]\n"
    "v_exp_f32 v106, v66\n"
    "ds_read_b64 v[26:27], v4 offset:4096\n"
    "s_waitcnt lgkmcnt(6)\n"
    "v_mfma_f32_16x16x32_bf16 v[68:71], v[44:47], v[204:207], v[68:71]\n"
    "v_cvt_pk_bf16_f32 v90, v102, v103\n"
    "v_exp_f32 v107, v67\n"
    "ds_read_b64 v[28:29], v8 offset:0\n"
    "v_mfma_f32_16x16x32_bf16 v[72:75], v[44:47], v[208:211], v[72:75]\n"
    "v_cvt_pk_bf16_f32 v87, v104, v105\n"
    "v_cvt_pk_bf16_f32 v91, v106, v107\n"
    "ds_read_b64 v[30:31], v9 offset:0\n"
    "s_waitcnt lgkmcnt(6)\n"
    "v_mfma_f32_16x16x32_bf16 v[76:79], v[48:51], v[204:207], v[76:79]\n"
    "ds_read_b64 v[32:33], v8 offset:4096\n"
    "s_nop 2\n"
    "v_mfma_f32_16x16x32_bf16 v[80:83], v[48:51], v[208:211], v[80:83]\n"
    "ds_read_b64 v[34:35], v9 offset:4096\n"
    "ds_read_b64 v[36:37], v3 offset:2048\n"
    "ds_read_b64 v[38:39], v4 offset:2048\n"
    "s_nop 0\n"
    "v_mfma_f32_16x16x32_bf16 v[216:219], v[212:215], v[84:87], 0\n"
    "s_nop 3\n"
    "v_mfma_f32_16x16x32_bf16 v[220:223], v[212:215], v[88:91], 0\n"
    "v_pk_mul_f32 v[132:133], v[132:133], v[240:241]\n"
    "v_pk_mul_f32 v[134:135], v[134:135], v[240:241]\n"
    "v_exp_f32 v132, v132\n"
    "v_exp_f32 v133, v133\n"
    "v_exp_f32 v134, v134\n"
    "v_exp_f32 v135, v135\n"
    "v_pk_fma_f32 v[132:133], v[132:133], v[242:243], v[242:243]\n"
    "v_pk_fma_f32 v[134:135], v[134:135], v[242:243], v[242:243]\n"
    "v_max_f32 v248, v216, v220\n"
    "v_cmp_lt_f32 vcc, s80, v248\n"
    "v_min_f32 v248, v216, v220\n"
    "v_cmp_gt_f32 s[64:65], s81, v248\n"
    "s_nop 0\n"
    "s_or_b64 vcc, vcc, s[64:65]\n"
    "s_cbranch_vccnz L_rare_p0_%=\n"
    "L_ret_p0_%=:\n"
    "s_mov_b32 s86, 32768\n"
    "s_mov_b32 s87, 0\n"
    "s_waitcnt lgkmcnt(8)\n"
    "v_mfma_f32_16x16x32_bf16 v[52:55], v[20:23], v[180:183], v[232:235]\n"
    "v_rcp_f32 v132, v132\n"
    "v_rcp_f32 v133, v133\n"
    "ds_read_b64 v[40:41], v3 offset:6144\n"
    "v_mfma_f32_16x16x32_bf16 v[56:59], v[20:23], v[184:187], v[236:239]\n"
    "v_rcp_f32 v134, v134\n"
    "v_rcp_f32 v135, v135\n"
    "ds_read_b64 v[42:43], v4 offset:6144\n"
    "s_waitcnt lgkmcnt(8)\n"
    "v_mfma_f32_16x16x32_bf16 v[60:63], v[24:27], v[180:183], v[232:235]\n"
    "v_pk_mul_f32 v[140:141], v[140:141], v[132:133]\n"
    "v_pk_mul_f32 v[142:143], v[142:143], v[134:135]\n"
    "v_cvt_pk_bf16_f32 v112, v140, v141\n"
    "v_cvt_pk_bf16_f32 v113, v142, v143\n"
    "ds_read_b64 v[44:45], v8 offset:2048\n"
    "v_mfma_f32_16x16x32_bf16 v[64:67], v[24:27], v[184:187], v[236:239]\n"
    "v_pk_mul_f32 v[136:137], v[136:137], v[244:245]\n"
    "v_pk_mul_f32 v[138:139], v[138:139], v[244:245]\n"
    "v_exp_f32 v136, v136\n"
    "ds_read_b64 v[46:47], v9 offset:2048\n"
    "s_waitcnt lgkmcnt(8)\n"
    "v_mfma_f32_16x16x32_bf16 v[52:55], v[28:31], v[188:191], v[52:55]\n"
    "v_exp_f32 v137, v137\n"
    "v_exp_f32 v138, v138\n"
    "ds_read_b64 v[48:49], v8 offset:6144\n"
    "v_mfma_f32_16x16x32_bf16 v[56:59], v[28:31], v[192:195], v[56:59]\n"
    "v_exp_f32 v139, v139\n"
    "v_pk_fma_f32 v[136:137], v[136:137], v[246:247], v[246:247]\n"
    "v_pk_fma_f32 v[138:139], v[138:139], v[246:247], v[246:247]\n"
    "ds_read_b64 v[50:51], v9 offset:6144\n"
    "s_waitcnt lgkmcnt(8)\n"
    "v_mfma_f32_16x16x32_bf16 v[60:63], v[32:35], v[188:191], v[60:63]\n"
    "v_rcp_f32 v136, v136\n"
    "v_rcp_f32 v137, v137\n"
    "ds_read_b64_tr_b16 v[20:21], v1 offset:0\n"
    "v_mfma_f32_16x16x32_bf16 v[64:67], v[32:35], v[192:195], v[64:67]\n"
    "v_rcp_f32 v138, v138\n"
    "v_rcp_f32 v139, v139\n"
    "ds_read_b64_tr_b16 v[22:23], v1 offset:4096\n"
    "s_waitcnt lgkmcnt(8)\n"
    "v_mfma_f32_16x16x32_bf16 v[52:55], v[36:39], v[196:199], v[52:55]\n"
    "v_pk_mul_f32 v[144:145], v[144:145], v[136:137]\n"
    "v_pk_mul_f32 v[146:147], v[146:147], v[138:139]\n"
    "v_cvt_pk_bf16_f32 v114, v144, v145\n"
    "v_cvt_pk_bf16_f32 v115, v146, v147\n"
    "v_pk_mul_f32 v[148:149], v[148:149], v[240:241]\n"
    "v_pk_mul_f32 v[150:151], v[150:151], v[240:241]\n"
    "v_exp_f32 v148, v148\n"
    "ds_read_b64_tr_b16 v[24:25], v2 offset:0\n"
    "v_mfma_f32_16x16x32_bf16 v[56:59], v[36:39], v[200:203], v[56:59]\n"
    "v_exp_f32 v149, v149\n"
    "v_exp_f32 v150, v150\n"
    "ds_read_b64_tr_b16 v[26:27], v2 offset:4096\n"
    "buffer_store_dwordx4 v[112:115], v15, s[36:39], 0 offen offset:1024 sc1\n"
    "s_waitcnt lgkmcnt(8)\n"
    "v_mfma_f32_16x16x32_bf16 v[60:63], v[40:43], v[196:199], v[60:63]\n"
    "v_exp_f32 v151, v151\n"
    "ds_read_b64_tr_b16 v[28:29], v1 offset:512\n"
    "v_mfma_f32_16x16x32_bf16 v[64:67], v[40:43], v[200:203], v[64:67]\n"
    "v_pk_fma_f32 v[148:149], v[148:149], v[242:243], v[242:243]\n"
    "v_pk_fma_f32 v[150:151], v[150:151], v[242:243], v[242:243]\n"
    "v_rcp_f32 v148, v148\n"
    "ds_read_b64_tr_b16 v[30:31], v1 offset:4608\n"
    "s_waitcnt lgkmcnt(8)\n"
    "v_mfma_f32_16x16x32_bf16 v[52:55], v[44:47], v[204:207], v[52:55]\n"
    "v_rcp_f32 v149, v149\n"
    "v_rcp_f32 v150, v150\n"
    "ds_read_b64_tr_b16 v[32:33], v2 offset:512\n"
    "v_mfma_f32_16x16x32_bf16 v[56:59], v[44:47], v[208:211], v[56:59]\n"
    "v_rcp_f32 v151, v151\n"
    "ds_read_b64_tr_b16 v[34:35], v2 offset:4608\n"
    "s_waitcnt lgkmcnt(8)\n"
    "v_mfma_f32_16x16x32_bf16 v[60:63], v[48:51], v[204:207], v[60:63]\n"
    "v_exp_f32 v100, v68\n"
    "v_exp_f32 v101, v69\n"
    "v_pk_mul_f32 v[156:157], v[156:157], v[148:149]\n"
    "v_pk_mul_f32 v[158:159], v[158:159], v[150:151]\n"
    "v_cvt_pk_bf16_f32 v252, v156, v157\n"
    "v_cvt_pk_bf16_f32 v253, v158, v159\n"
    "ds_read_b64_tr_b16 v[36:37], v1 offset:1024\n"
    "v_mfma_f32_16x16x32_bf16 v[64:67], v[48:51], v[208:211], v[64:67]\n"
    "v_exp_f32 v102, v72\n"
    "v_exp_f32 v103, v73\n"
    "v_pk_mul_f32 v[152:153], v[152:153], v[244:245]\n"
    "v_pk_mul_f32 v[154:155], v[154:155], v[244:245]\n"
    "v_exp_f32 v152, v152\n"
    "ds_read_b64_tr_b16 v[38:39], v1 offset:5120\n"
    "s_waitcnt lgkmcnt(8)\n"
    "v_mfma_f32_16x16x32_bf16 v[116:119], v[20:23], v[84:87], 0\n"
    "v_cvt_pk_bf16_f32 v92, v100, v101\n"
    "v_exp_f32 v153, v153\n"
    "ds_read_b64_tr_b16 v[40:41], v2 offset:1024\n"
    "v_mfma_f32_16x16x32_bf16 v[120:123], v[20:23], v[88:91], 0\n"
    "v_exp_f32 v104, v70\n"
    "v_exp_f32 v105, v71\n"
    "v_exp_f32 v154, v154\n"
    "v_exp_f32 v155, v155\n"
    "ds_read_b64_tr_b16 v[42:43], v2 offset:5120\n"
    "s_waitcnt lgkmcnt(8)\n"
    "v_mfma_f32_16x16x32_bf16 v[124:127], v[24:27], v[84:87], 0\n"
    "v_exp_f32 v106, v74\n"
    "v_exp_f32 v107, v75\n"
    "v_pk_fma_f32 v[152:153], v[152:153], v[246:247], v[246:247]\n"
    "v_pk_fma_f32 v[154:155], v[154:155], v[246:247], v[246:247]\n"
    "v_rcp_f32 v152, v152\n"
    "ds_read_b64_tr_b16 v[44:45], v1 offset:1536\n"
    "v_mfma_f32_16x16x32_bf16 v[128:131], v[24:27], v[88:91], 0\n"
    "v_cvt_pk_bf16_f32 v96, v102, v103\n"
    "v_cvt_pk_bf16_f32 v93, v104, v105\n"
    "v_rcp_f32 v153, v153\n"
    "ds_read_b64_tr_b16 v[46:47], v1 offset:5632\n"
    "s_waitcnt lgkmcnt(8)\n"
    "v_mfma_f32_16x16x32_bf16 v[132:135], v[28:31], v[84:87], 0\n"
    "v_exp_f32 v100, v76\n"
    "v_exp_f32 v101, v77\n"
    "v_rcp_f32 v154, v154\n"
    "v_rcp_f32 v155, v155\n"
    "ds_read_b64_tr_b16 v[48:49], v2 offset:1536\n"
    "v_mfma_f32_16x16x32_bf16 v[136:139], v[28:31], v[88:91], 0\n"
    "v_exp_f32 v102, v80\n"
    "v_pk_mul_f32 v[160:161], v[160:161], v[152:153]\n"
    "v_pk_mul_f32 v[162:163], v[162:163], v[154:155]\n"
    "v_cvt_pk_bf16_f32 v254, v160, v161\n"
    "v_cvt_pk_bf16_f32 v255, v162, v163\n"
    "ds_read_b64_tr_b16 v[50:51], v2 offset:5632\n"
    "s_waitcnt lgkmcnt(8)\n"
    "v_mfma_f32_16x16x32_bf16 v[140:143], v[32:35], v[84:87], 0\n"
    "v_exp_f32 v103, v81\n"
    "v_cvt_pk_bf16_f32 v97, v106, v107\n"
    "v_cvt_pk_bf16_f32 v94, v100, v101\n"
    "ds_read_b64 v[20:21], v3 offset:8192\n"
    "buffer_store_dwordx4 v[252:255], v15, s[36:39], s79 offen offset:0 sc1\n"
    "v_mfma_f32_16x16x32_bf16 v[144:147], v[32:35], v[88:91], 0\n"
    "v_exp_f32 v104, v78\n"
    "v_exp_f32 v105, v79\n"
    "ds_read_b64 v[22:23], v4 offset:8192\n"
    "s_waitcnt lgkmcnt(8)\n"
    "v_mfma_f32_16x16x32_bf16 v[148:151], v[36:39], v[84:87], 0\n"
    "v_exp_f32 v106, v82\n"
    "ds_read_b64 v[24:25], v3 offset:12288\n"
    "v_mfma_f32_16x16x32_bf16 v[152:155], v[36:39], v[88:91], 0\n"
    "v_cvt_pk_bf16_f32 v98, v102, v103\n"
    "v_exp_f32 v107, v83\n"
    "ds_read_b64 v[26:27], v4 offset:12288\n"
    "s_waitcnt lgkmcnt(8)\n"
    "v_mfma_f32_16x16x32_bf16 v[156:159], v[40:43], v[84:87], 0\n"
    "v_cvt_pk_bf16_f32 v95, v104, v105\n"
    "v_cvt_pk_bf16_f32 v99, v106, v107\n"
    "ds_read_b64 v[28:29], v8 offset:8192\n"
    "v_mfma_f32_16x16x32_bf16 v[224:227], v[212:215], v[92:95], v[216:219]\n"
    "s_nop 3\n"
    "v_mfma_f32_16x16x32_bf16 v[228:231], v[212:215], v[96:99], v[220:223]\n"
    "s_nop 3\n"
    "v_mfma_f32_16x16x32_bf16 v[160:163], v[40:43], v[88:91], 0\n"
    "ds_read_b64 v[30:31], v9 offset:8192\n"
    "s_waitcnt lgkmcnt(8)\n"
    "s_nop 1\n"
    "v_mfma_f32_16x16x32_bf16 v[164:167], v[44:47], v[84:87], 0\n"
    "ds_read_b64 v[32:33], v8 offset:12288\n"
    "s_nop 2\n"
    "v_mfma_f32_16x16x32_bf16 v[168:171], v[44:47], v[88:91], 0\n"
    "ds_read_b64 v[34:35], v9 offset:12288\n"
    "s_waitcnt lgkmcnt(8)\n"
    "s_nop 1\n"
    "v_mfma_f32_16x16x32_bf16 v[172:175], v[48:51], v[84:87], 0\n"
    "v_max_f32 v248, v224, v228\n"
    "v_cmp_lt_f32 vcc, s80, v248\n"
    "ds_read_b64 v[36:37], v3 offset:10240\n"
    "v_mfma_f32_16x16x32_bf16 v[176:179], v[48:51], v[88:91], 0\n"
    "ds_read_b64 v[38:39], v4 offset:10240\n"
    "s_cbranch_vccnz L_rare_od0_%=\n"
    "L_ret_od0_%=:\n"
    "s_waitcnt lgkmcnt(8)\n"
    "v_mfma_f32_16x16x32_bf16 v[68:71], v[20:23], v[180:183], v[232:235]\n"
    "v_exp_f32 v100, v52\n"
    "ds_read_b64 v[40:41], v3 offset:14336\n"
    "v_mfma_f32_16x16x32_bf16 v[72:75], v[20:23], v[184:187], v[236:239]\n"
    "v_exp_f32 v101, v53\n"
    "ds_read_b64 v[42:43], v4 offset:14336\n"
    "s_waitcnt lgkmcnt(8)\n"
    "v_mfma_f32_16x16x32_bf16 v[76:79], v[24:27], v[180:183], v[232:235]\n"
    "v_exp_f32 v102, v56\n"
    "v_and_b32 v7, 31, v0\n"
    "ds_read_b64 v[44:45], v8 offset:10240\n"
    "s_lshr_b32 s64, s72, 1\n"
    "s_lshl_b32 s64, s64, 8\n"
    "v_lshl_add_u32 v7, v7, 3, s64\n"
    "v_mfma_f32_16x16x32_bf16 v[80:83], v[24:27], v[184:187], v[236:239]\n"
    "v_exp_f32 v103, v57\n"
    "ds_read_b64 v[46:47], v9 offset:10240\n"
    "s_waitcnt lgkmcnt(8)\n"
    "v_mfma_f32_16x16x32_bf16 v[68:71], v[28:31], v[188:191], v[68:71]\n"
    "v_cvt_pk_bf16_f32 v84, v100, v101\n"
    "v_and_b32 v5, 3, v0\n"
    "ds_read_b64 v[48:49], v8 offset:14336\n"
    "s_lshl_b32 s65, s70, 6\n"
    "s_add_u32 s64, s64, s65\n"
    "s_and_b32 s65, s7, 1\n"
    "s_lshl_b32 s65, s65, 5\n"
    "v_mfma_f32_16x16x32_bf16 v[72:75], v[28:31], v[192:195], v[72:75]\n"
    "ds_read_b64 v[50:51], v9 offset:14336\n"
    "s_waitcnt lgkmcnt(8)\n"
    "s_nop 1\n"
    "v_mfma_f32_16x16x32_bf16 v[76:79], v[32:35], v[188:191], v[76:79]\n"
    "v_exp_f32 v104, v54\n"
    "ds_read_b64_tr_b16 v[20:21], v1 offset:8192\n"
    "s_add_u32 s64, s64, s65\n"
    "s_add_u32 s64, s64, 4096\n"
    "v_lshl_add_u32 v5, v5, 3, s64\n"
    "v_mfma_f32_16x16x32_bf16 v[80:83], v[32:35], v[192:195], v[80:83]\n"
    "v_exp_f32 v105, v55\n"
    "ds_read_b64_tr_b16 v[22:23], v1 offset:12288\n"
    "s_waitcnt lgkmcnt(8)\n"
    "v_mfma_f32_16x16x32_bf16 v[68:71], v[36:39], v[196:199], v[68:71]\n"
    "v_exp_f32 v106, v58\n"
    "ds_read_b64_tr_b16 v[24:25], v2 offset:8192\n"
    "v_mfma_f32_16x16x32_bf16 v[72:75], v[36:39], v[200:203], v[72:75]\n"
    "v_exp_f32 v107, v59\n"
    "ds_read_b64_tr_b16 v[26:27], v2 offset:12288\n"
    "s_waitcnt lgkmcnt(8)\n"
    "v_mfma_f32_16x16x32_bf16 v[76:79], v[40:43], v[196:199], v[76:79]\n"
    "ds_read_b64_tr_b16 v[28:29], v1 offset:8704\n"
    "s_add_u32 s68, s86, 0x8000\n"
    "s_cmp_eq_u32 s68, 0x28000\n"
    "s_cselect_b32 s68, 0, s68\n"
    "v_mfma_f32_16x16x32_bf16 v[80:83], v[40:43], v[200:203], v[80:83]\n"
    "v_cvt_pk_bf16_f32 v88, v102, v103\n"
    "v_cvt_pk_bf16_f32 v85, v104, v105\n"
    "ds_read_b64_tr_b16 v[30:31], v1 offset:12800\n"
    "s_sub_u32 s69, s68, s86\n"
    "s_mov_b32 s86, s68\n"
    "s_waitcnt lgkmcnt(8)\n"
    "v_mfma_f32_16x16x32_bf16 v[68:71], v[44:47], v[204:207], v[68:71]\n"
    "v_exp_f32 v100, v60\n"
    "v_lshlrev_b32 v6, 3, v15\n"
    "ds_read_b64_tr_b16 v[32:33], v2 offset:8704\n"
    "s_lshl_b32 s65, s73, 8\n"
    "v_mfma_f32_16x16x32_bf16 v[72:75], v[44:47], v[208:211], v[72:75]\n"
    "v_exp_f32 v101, v61\n"
    "ds_read_b64_tr_b16 v[34:35], v2 offset:12800\n"
    "s_waitcnt lgkmcnt(8)\n"
    "v_mfma_f32_16x16x32_bf16 v[76:79], v[48:51], v[204:207], v[76:79]\n"
    "v_exp_f32 v102, v64\n"
    "v_add_u32 v6, 1024, v6\n"
    "v_subrev_u32 v6, s65, v6\n"
    "ds_read_b64_tr_b16 v[36:37], v1 offset:9216\n"
    "v_mfma_f32_16x16x32_bf16 v[80:83], v[48:51], v[208:211], v[80:83]\n"
    "v_add_u32 v3, s69, v3\n"
    "v_add_u32 v4, s69, v4\n"
    "ds_read_b64_tr_b16 v[38:39], v1 offset:13312\n"
    "s_waitcnt lgkmcnt(8)\n"
    "v_mfma_f32_16x16x32_bf16 v[116:119], v[20:23], v[92:95], v[116:119]\n"
    "v_exp_f32 v103, v65\n"
    "v_mov_b32 v250, 0x40000000\n"
    "v_cmp_gt_u32 vcc, 256, v15\n"
    "ds_read_b64_tr_b16 v[40:41], v2 offset:9216\n"
    "v_mfma_f32_16x16x32_bf16 v[120:123], v[20:23], v[96:99], v[120:123]\n"
    "v_cvt_pk_bf16_f32 v89, v106, v107\n"
    "v_cvt_pk_bf16_f32 v86, v100, v101\n"
    "v_add_u32 v8, s69, v8\n"
    "v_add_u32 v9, s69, v9\n"
    "ds_read_b64_tr_b16 v[42:43], v2 offset:13312\n"
    "s_waitcnt lgkmcnt(8)\n"
    "v_mfma_f32_16x16x32_bf16 v[124:127], v[24:27], v[92:95], v[124:127]\n"
    "v_exp_f32 v104, v62\n"
    "v_cndmask_b32 v249, v250, v6, vcc\n"
    "v_cmp_gt_u32 vcc, 128, v15\n"
    "ds_read_b64_tr_b16 v[44:45], v1 offset:9728\n"
    "v_mfma_f32_16x16x32_bf16 v[128:131], v[24:27], v[96:99], v[128:131]\n"
    "v_exp_f32 v105, v63\n"
    "ds_read_b64_tr_b16 v[46:47], v1 offset:13824\n"
    "s_waitcnt lgkmcnt(8)\n"
    "v_mfma_f32_16x16x32_bf16 v[132:135], v[28:31], v[92:95], v[132:135]\n"
    "v_cndmask_b32 v6, v250, v6, vcc\n"
    "ds_read_b64_tr_b16 v[48:49], v2 offset:9728\n"
    "s_lshr_b32 s63, s6, 3\n"
    "s_lshl_b32 s64, s63, 15\n"
    "s_lshl_b32 s65, s7, 12\n"
    "s_add_u32 s78, s64, s65\n"
    "s_sub_u32 s78, s78, 1024\n"
    "s_cselect_b32 s78, 0, s78\n"
    "v_mfma_f32_16x16x32_bf16 v[136:139], v[28:31], v[96:99], v[136:139]\n"
    "v_exp_f32 v106, v66\n"
    "ds_read_b64_tr_b16 v[50:51], v2 offset:13824\n"
    "s_waitcnt lgkmcnt(8)\n"
    "v_mfma_f32_16x16x32_bf16 v[140:143], v[32:35], v[92:95], v[140:143]\n"
    "v_cvt_pk_bf16_f32 v90, v102, v103\n"
    "s_lshr_b32 s63, s6, 3\n"
    "s_mul_i32 s64, s63, 11\n"
    "s_mul_i32 s65, s7, 11\n"
    "s_lshr_b32 s65, s65, 3\n"
    "s_add_u32 s64, s64, s65\n"
    "s_lshl_b32 s64, s64, 10\n"
    "s_sub_u32 s64, s64, 1024\n"
    "s_cselect_b32 s64, 0, s64\n"
    "buffer_load_dword v19, v249, s[12:15], s64 offen\n"
    "v_mfma_f32_16x16x32_bf16 v[144:147], v[32:35], v[96:99], v[144:147]\n"
    "v_exp_f32 v107, v67\n"
    "s_waitcnt lgkmcnt(6)\n"
    "v_mfma_f32_16x16x32_bf16 v[148:151], v[36:39], v[92:95], v[148:151]\n"
    "v_cvt_pk_bf16_f32 v87, v104, v105\n"
    "v_cvt_pk_bf16_f32 v91, v106, v107\n"
    "s_add_u32 s68, s87, 0x8000\n"
    "s_cmp_eq_u32 s68, 0x28000\n"
    "s_cselect_b32 s68, 0, s68\n"
    "v_mfma_f32_16x16x32_bf16 v[152:155], v[36:39], v[96:99], v[152:155]\n"
    "s_waitcnt vmcnt(10)\n"
    "s_waitcnt lgkmcnt(0)\n"
    "s_barrier\n"
    "ds_read_b64 v[20:21], v3 offset:0\n"
    "ds_read_b64 v[22:23], v4 offset:0\n"
    "v_mfma_f32_16x16x32_bf16 v[156:159], v[40:43], v[92:95], v[156:159]\n"
    "ds_read_b64 v[24:25], v3 offset:4096\n"
    "ds_read_b64 v[26:27], v4 offset:4096\n"
    "s_nop 1\n"
    "v_mfma_f32_16x16x32_bf16 v[216:219], v[212:215], v[84:87], v[224:227]\n"
    "s_nop 3\n"
    "v_mfma_f32_16x16x32_bf16 v[220:223], v[212:215], v[88:91], v[228:231]\n"
    "s_nop 3\n"
    "v_mfma_f32_16x16x32_bf16 v[160:163], v[40:43], v[96:99], v[160:163]\n"
    "ds_read_b64 v[28:29], v8 offset:0\n"
    "ds_read_b64 v[30:31], v9 offset:0\n"
    "s_nop 1\n"
    "v_mfma_f32_16x16x32_bf16 v[164:167], v[44:47], v[92:95], v[164:167]\n"
    "ds_read_b64 v[32:33], v8 offset:4096\n"
    "s_sub_u32 s69, s68, s87\n"
    "s_mov_b32 s87, s68\n"
    "s_nop 0\n"
    "v_mfma_f32_16x16x32_bf16 v[168:171], v[44:47], v[96:99], v[168:171]\n"
    "ds_read_b64 v[34:35], v9 offset:4096\n"
    "ds_read_b64 v[36:37], v3 offset:2048\n"
    "s_cmp_eq_u32 s7, 0\n"
    "s_cselect_b32 s67, 4, 99\n"
    "v_mfma_f32_16x16x32_bf16 v[172:175], v[48:51], v[92:95], v[172:175]\n"
    "v_max_f32 v248, v216, v220\n"
    "v_cmp_lt_f32 vcc, s80, v248\n"
    "v_add_u32 v1, s69, v1\n"
    "v_add_u32 v2, s69, v2\n"
    "ds_read_b64 v[38:39], v4 offset:2048\n"
    "v_mfma_f32_16x16x32_bf16 v[176:179], v[48:51], v[96:99], v[176:179]\n"
    "s_cbranch_vccnz L_rare_ev0_%=\n"
    "L_ret_ev0_%=:\n"
    "s_mov_b32 s66, 4\n"
    "L_att_%=:\n"
    "s_waitcnt lgkmcnt(8)\n"
    "v_mfma_f32_16x16x32_bf16 v[52:55], v[20:23], v[180:183], v[232:235]\n"
    "v_exp_f32 v100, v68\n"
    "ds_read_b64 v[40:41], v3 offset:6144\n"
    "v_mfma_f32_16x16x32_bf16 v[56:59], v[20:23], v[184:187], v[236:239]\n"
    "v_exp_f32 v101, v69\n"
    "ds_read_b64 v[42:43], v4 offset:6144\n"
    "s_waitcnt lgkmcnt(8)\n"
    "v_mfma_f32_16x16x32_bf16 v[60:63], v[24:27], v[180:183], v[232:235]\n"
    "v_exp_f32 v102, v72\n"
    "ds_read_b64 v[44:45], v8 offset:2048\n"
    "v_mfma_f32_16x16x32_bf16 v[64:67], v[24:27], v[184:187], v[236:239]\n"
    "v_exp_f32 v103, v73\n"
    "ds_read_b64 v[46:47], v9 offset:2048\n"
    "s_waitcnt lgkmcnt(8)\n"
    "v_mfma_f32_16x16x32_bf16 v[52:55], v[28:31], v[188:191], v[52:55]\n"
    "v_cvt_pk_bf16_f32 v92, v100, v101\n"
    "ds_read_b64 v[48:49], v8 offset:6144\n"
    "s_nop 0\n"
    "v_mfma_f32_16x16x32_bf16 v[56:59], v[28:31], v[192:195], v[56:59]\n"
    "ds_read_b64 v[50:51], v9 offset:6144\n"
    "s_mov_b32 m0, s88\n"
    "s_nop 0\n"
    "buffer_load_dwordx4 v12, s[24:27], s82 offen lds\n"
    "s_waitcnt lgkmcnt(8)\n"
    "v_mfma_f32_16x16x32_bf16 v[60:63], v[32:35], v[188:191], v[60:63]\n"
    "v_exp_f32 v104, v70\n"
    "ds_read_b64_tr_b16 v[20:21], v1 offset:0\n"
    "v_mfma_f32_16x16x32_bf16 v[64:67], v[32:35], v[192:195], v[64:67]\n"
    "v_exp_f32 v105, v71\n"
    "ds_read_b64_tr_b16 v[22:23], v1 offset:4096\n"
    "s_waitcnt lgkmcnt(8)\n"
    "v_mfma_f32_16x16x32_bf16 v[52:55], v[36:39], v[196:199], v[52:55]\n"
    "v_exp_f32 v106, v74\n"
    "ds_read_b64_tr_b16 v[24:25], v2 offset:0\n"
    "v_mfma_f32_16x16x32_bf16 v[56:59], v[36:39], v[200:203], v[56:59]\n"
    "v_exp_f32 v107, v75\n"
    "ds_read_b64_tr_b16 v[26:27], v2 offset:4096\n"
    "s_waitcnt lgkmcnt(8)\n"
    "v_mfma_f32_16x16x32_bf16 v[60:63], v[40:43], v[196:199], v[60:63]\n"
    "ds_read_b64_tr_b16 v[28:29], v1 offset:512\n"
    "s_add_u32 m0, s88, 1024\n"
    "s_nop 0\n"
    "buffer_load_dwordx4 v13, s[24:27], s82 offen lds\n"
    "s_add_u32 s82, s82, 16384\n"
    "v_mfma_f32_16x16x32_bf16 v[64:67], v[40:43], v[200:203], v[64:67]\n"
    "v_cvt_pk_bf16_f32 v96, v102, v103\n"
    "v_cvt_pk_bf16_f32 v93, v104, v105\n"
    "ds_read_b64_tr_b16 v[30:31], v1 offset:4608\n"
    "s_waitcnt lgkmcnt(8)\n"
    "v_mfma_f32_16x16x32_bf16 v[52:55], v[44:47], v[204:207], v[52:55]\n"
    "v_exp_f32 v100, v76\n"
    "ds_read_b64_tr_b16 v[32:33], v2 offset:512\n"
    "v_mfma_f32_16x16x32_bf16 v[56:59], v[44:47], v[208:211], v[56:59]\n"
    "v_exp_f32 v101, v77\n"
    "ds_read_b64_tr_b16 v[34:35], v2 offset:4608\n"
    "s_waitcnt lgkmcnt(8)\n"
    "v_mfma_f32_16x16x32_bf16 v[60:63], v[48:51], v[204:207], v[60:63]\n"
    "v_exp_f32 v102, v80\n"
    "ds_read_b64_tr_b16 v[36:37], v1 offset:1024\n"
    "v_mfma_f32_16x16x32_bf16 v[64:67], v[48:51], v[208:211], v[64:67]\n"
    "ds_read_b64_tr_b16 v[38:39], v1 offset:5120\n"
    "s_waitcnt lgkmcnt(8)\n"
    "s_nop 1\n"
    "v_mfma_f32_16x16x32_bf16 v[116:119], v[20:23], v[84:87], v[116:119]\n"
    "v_exp_f32 v103, v81\n"
    "ds_read_b64_tr_b16 v[40:41], v2 offset:1024\n"
    "v_mfma_f32_16x16x32_bf16 v[120:123], v[20:23], v[88:91], v[120:123]\n"
    "v_cvt_pk_bf16_f32 v97, v106, v107\n"
    "v_cvt_pk_bf16_f32 v94, v100, v101\n"
    "ds_read_b64_tr_b16 v[42:43], v2 offset:5120\n"
    "s_waitcnt lgkmcnt(8)\n"
    "v_mfma_f32_16x16x32_bf16 v[124:127], v[24:27], v[84:87], v[124:127]\n"
    "v_exp_f32 v104, v78\n"
    "ds_read_b64_tr_b16 v[44:45], v1 offset:1536\n"
    "v_mfma_f32_16x16x32_bf16 v[128:131], v[24:27], v[88:91], v[128:131]\n"
    "v_exp_f32 v105, v79\n"
    "ds_read_b64_tr_b16 v[46:47], v1 offset:5632\n"
    "s_waitcnt lgkmcnt(8)\n"
    "v_mfma_f32_16x16x32_bf16 v[132:135], v[28:31], v[84:87], v[132:135]\n"
    "ds_read_b64_tr_b16 v[48:49], v2 offset:1536\n"
    "s_add_u32 m0, s88, 16384\n"
    "s_nop 0\n"
    "buffer_load_dwordx4 v14, s[28:31], s83 offen lds\n"
    "v_mfma_f32_16x16x32_bf16 v[136:139], v[28:31], v[88:91], v[136:139]\n"
    "v_exp_f32 v106, v82\n"
    "ds_read_b64_tr_b16 v[50:51], v2 offset:5632\n"
    "s_waitcnt lgkmcnt(8)\n"
    "v_mfma_f32_16x16x32_bf16 v[140:143], v[32:35], v[84:87], v[140:143]\n"
    "v_cvt_pk_bf16_f32 v98, v102, v103\n"
    "ds_read_b64 v[20:21], v3 offset:8192\n"
    "s_nop 0\n"
    "v_mfma_f32_16x16x32_bf16 v[144:147], v[32:35], v[88:91], v[144:147]\n"
    "v_exp_f32 v107, v83\n"
    "ds_read_b64 v[22:23], v4 offset:8192\n"
    "s_waitcnt lgkmcnt(8)\n"
    "v_mfma_f32_16x16x32_bf16 v[148:151], v[36:39], v[84:87], v[148:151]\n"
    "v_cvt_pk_bf16_f32 v95, v104, v105\n"
    "v_cvt_pk_bf16_f32 v99, v106, v107\n"
    "ds_read_b64 v[24:25], v3 offset:12288\n"
    "v_mfma_f32_16x16x32_bf16 v[152:155], v[36:39], v[88:91], v[152:155]\n"
    "ds_read_b64 v[26:27], v4 offset:12288\n"
    "s_waitcnt lgkmcnt(8)\n"
    "s_nop 1\n"
    "v_mfma_f32_16x16x32_bf16 v[156:159], v[40:43], v[84:87], v[156:159]\n"
    "ds_read_b64 v[28:29], v8 offset:8192\n"
    "s_add_u32 s62, s83, 128\n"
    "s_add_u32 m0, s88, 17408\n"
    "s_nop 0\n"
    "buffer_load_dwordx4 v14, s[28:31], s62 offen lds\n"
    "v_mfma_f32_16x16x32_bf16 v[224:227], v[212:215], v[92:95], v[216:219]\n"
    "s_nop 3\n"
    "v_mfma_f32_16x16x32_bf16 v[228:231], v[212:215], v[96:99], v[220:223]\n"
    "s_nop 3\n"
    "v_mfma_f32_16x16x32_bf16 v[160:163], v[40:43], v[88:91], v[160:163]\n"
    "ds_read_b64 v[30:31], v9 offset:8192\n"
    "s_waitcnt lgkmcnt(8)\n"
    "s_nop 1\n"
    "v_mfma_f32_16x16x32_bf16 v[164:167], v[44:47], v[84:87], v[164:167]\n"
    "ds_read_b64 v[32:33], v8 offset:12288\n"
    "s_add_u32 s83, s83, 16384\n"
    "s_add_u32 s88, s88, 0x8000\n"
    "s_nop 0\n"
    "v_mfma_f32_16x16x32_bf16 v[168:171], v[44:47], v[88:91], v[168:171]\n"
    "ds_read_b64 v[34:35], v9 offset:12288\n"
    "s_waitcnt lgkmcnt(8)\n"
    "s_nop 1\n"
    "v_mfma_f32_16x16x32_bf16 v[172:175], v[48:51], v[84:87], v[172:175]\n"
    "v_max_f32 v248, v224, v228\n"
    "v_cmp_lt_f32 vcc, s80, v248\n"
    "ds_read_b64 v[36:37], v3 offset:10240\n"
    "s_sub_u32 s62, s88, 0x28000\n"
    "s_cselect_b32 s88, s88, s62\n"
    "v_mfma_f32_16x16x32_bf16 v[176:179], v[48:51], v[88:91], v[176:179]\n"
    "ds_read_b64 v[38:39], v4 offset:10240\n"
    "s_cbranch_vccnz L_rare_oda_%=\n"
    "L_ret_oda_%=:\n"
    "s_waitcnt lgkmcnt(8)\n"
    "v_mfma_f32_16x16x32_bf16 v[68:71], v[20:23], v[180:183], v[232:235]\n"
    "v_exp_f32 v100, v52\n"
    "ds_read_b64 v[40:41], v3 offset:14336\n"
    "v_mfma_f32_16x16x32_bf16 v[72:75], v[20:23], v[184:187], v[236:239]\n"
    "v_exp_f32 v101, v53\n"
    "ds_read_b64 v[42:43], v4 offset:14336\n"
    "s_waitcnt lgkmcnt(8)\n"
    "v_mfma_f32_16x16x32_bf16 v[76:79], v[24:27], v[180:183], v[232:235]\n"
    "v_exp_f32 v102, v56\n"
    "ds_read_b64 v[44:45], v8 offset:10240\n"
    "v_mfma_f32_16x16x32_bf16 v[80:83], v[24:27], v[184:187], v[236:239]\n"
    "v_exp_f32 v103, v57\n"
    "ds_read_b64 v[46:47], v9 offset:10240\n"
    "s_waitcnt lgkmcnt(8)\n"
    "v_mfma_f32_16x16x32_bf16 v[68:71], v[28:31], v[188:191], v[68:71]\n"
    "v_cvt_pk_bf16_f32 v84, v100, v101\n"
    "ds_read_b64 v[48:49], v8 offset:14336\n"
    "s_nop 0\n"
    "v_mfma_f32_16x16x32_bf16 v[72:75], v[28:31], v[192:195], v[72:75]\n"
    "ds_read_b64 v[50:51], v9 offset:14336\n"
    "s_waitcnt lgkmcnt(8)\n"
    "s_nop 1\n"
    "v_mfma_f32_16x16x32_bf16 v[76:79], v[32:35], v[188:191], v[76:79]\n"
    "v_exp_f32 v104, v54\n"
    "ds_read_b64_tr_b16 v[20:21], v1 offset:8192\n"
    "v_mfma_f32_16x16x32_bf16 v[80:83], v[32:35], v[192:195], v[80:83]\n"
    "v_exp_f32 v105, v55\n"
    "ds_read_b64_tr_b16 v[22:23], v1 offset:12288\n"
    "s_waitcnt lgkmcnt(8)\n"
    "v_mfma_f32_16x16x32_bf16 v[68:71], v[36:39], v[196:199], v[68:71]\n"
    "v_exp_f32 v106, v58\n"
    "ds_read_b64_tr_b16 v[24:25], v2 offset:8192\n"
    "v_mfma_f32_16x16x32_bf16 v[72:75], v[36:39], v[200:203], v[72:75]\n"
    "v_exp_f32 v107, v59\n"
    "ds_read_b64_tr_b16 v[26:27], v2 offset:12288\n"
    "s_waitcnt lgkmcnt(8)\n"
    "v_mfma_f32_16x16x32_bf16 v[76:79], v[40:43], v[196:199], v[76:79]\n"
    "ds_read_b64_tr_b16 v[28:29], v1 offset:8704\n"
    "s_add_u32 s68, s86, 0x8000\n"
    "s_cmp_eq_u32 s68, 0x28000\n"
    "s_cselect_b32 s68, 0, s68\n"
    "v_mfma_f32_16x16x32_bf16 v[80:83], v[40:43], v[200:203], v[80:83]\n"
    "v_cvt_pk_bf16_f32 v88, v102, v103\n"
    "v_cvt_pk_bf16_f32 v85, v104, v105\n"
    "ds_read_b64_tr_b16 v[30:31], v1 offset:12800\n"
    "s_sub_u32 s69, s68, s86\n"
    "s_mov_b32 s86, s68\n"
    "s_waitcnt lgkmcnt(8)\n"
    "v_mfma_f32_16x16x32_bf16 v[68:71], v[44:47], v[204:207], v[68:71]\n"
    "v_exp_f32 v100, v60\n"
    "ds_read_b64_tr_b16 v[32:33], v2 offset:8704\n"
    "v_mfma_f32_16x16x32_bf16 v[72:75], v[44:47], v[208:211], v[72:75]\n"
    "v_exp_f32 v101, v61\n"
    "ds_read_b64_tr_b16 v[34:35], v2 offset:12800\n"
    "s_waitcnt lgkmcnt(8)\n"
    "v_mfma_f32_16x16x32_bf16 v[76:79], v[48:51], v[204:207], v[76:79]\n"
    "v_exp_f32 v102, v64\n"
    "ds_read_b64_tr_b16 v[36:37], v1 offset:9216\n"
    "v_mfma_f32_16x16x32_bf16 v[80:83], v[48:51], v[208:211], v[80:83]\n"
    "v_add_u32 v3, s69, v3\n"
    "v_add_u32 v4, s69, v4\n"
    "ds_read_b64_tr_b16 v[38:39], v1 offset:13312\n"
    "s_waitcnt lgkmcnt(8)\n"
    "v_mfma_f32_16x16x32_bf16 v[116:119], v[20:23], v[92:95], v[116:119]\n"
    "v_exp_f32 v103, v65\n"
    "ds_read_b64_tr_b16 v[40:41], v2 offset:9216\n"
    "v_mfma_f32_16x16x32_bf16 v[120:123], v[20:23], v[96:99], v[120:123]\n"
    "v_cvt_pk_bf16_f32 v89, v106, v107\n"
    "v_cvt_pk_bf16_f32 v86, v100, v101\n"
    "ds_read_b64_tr_b16 v[42:43], v2 offset:13312\n"
    "s_waitcnt lgkmcnt(8)\n"
    "v_mfma_f32_16x16x32_bf16 v[124:127], v[24:27], v[92:95], v[124:127]\n"
    "v_exp_f32 v104, v62\n"
    "ds_read_b64_tr_b16 v[44:45], v1 offset:9728\n"
    "v_mfma_f32_16x16x32_bf16 v[128:131], v[24:27], v[96:99], v[128:131]\n"
    "v_exp_f32 v105, v63\n"
    "ds_read_b64_tr_b16 v[46:47], v1 offset:13824\n"
    "s_waitcnt lgkmcnt(8)\n"
    "v_mfma_f32_16x16x32_bf16 v[132:135], v[28:31], v[92:95], v[132:135]\n"
    "v_add_u32 v8, s69, v8\n"
    "v_add_u32 v9, s69, v9\n"
    "ds_read_b64_tr_b16 v[48:49], v2 offset:9728\n"
    "buffer_load_dword v19, v6, s[8:11], s78 offen\n"
    "s_add_u32 s78, s78, 1024\n"
    "v_mfma_f32_16x16x32_bf16 v[136:139], v[28:31], v[96:99], v[136:139]\n"
    "v_exp_f32 v106, v66\n"
    "ds_read_b64_tr_b16 v[50:51], v2 offset:13824\n"
    "s_waitcnt lgkmcnt(8)\n"
    "v_mfma_f32_16x16x32_bf16 v[140:143], v[32:35], v[92:95], v[140:143]\n"
    "v_cvt_pk_bf16_f32 v90, v102, v103\n"
    "s_add_u32 s68, s87, 0x8000\n"
    "s_cmp_eq_u32 s68, 0x28000\n"
    "s_cselect_b32 s68, 0, s68\n"
    "v_mfma_f32_16x16x32_bf16 v[144:147], v[32:35], v[96:99], v[144:147]\n"
    "v_exp_f32 v107, v67\n"
    "s_waitcnt lgkmcnt(6)\n"
    "v_mfma_f32_16x16x32_bf16 v[148:151], v[36:39], v[92:95], v[148:151]\n"
    "v_cvt_pk_bf16_f32 v87, v104, v105\n"
    "v_cvt_pk_bf16_f32 v91, v106, v107\n"
    "s_nop 0\n"
    "v_mfma_f32_16x16x32_bf16 v[152:155], v[36:39], v[96:99], v[152:155]\n"
    "s_waitcnt vmcnt(6)\n"
    "s_waitcnt lgkmcnt(0)\n"
    "s_barrier\n"
    "ds_read_b64 v[20:21], v3 offset:0\n"
    "ds_read_b64 v[22:23], v4 offset:0\n"
    "v_mfma_f32_16x16x32_bf16 v[156:159], v[40:43], v[92:95], v[156:159]\n"
    "ds_read_b64 v[24:25], v3 offset:4096\n"
    "ds_read_b64 v[26:27], v4 offset:4096\n"
    "s_nop 1\n"
    "v_mfma_f32_16x16x32_bf16 v[216:219], v[212:215], v[84:87], v[224:227]\n"
    "s_nop 3\n"
    "v_mfma_f32_16x16x32_bf16 v[220:223], v[212:215], v[88:91], v[228:231]\n"
    "s_nop 3\n"
    "v_mfma_f32_16x16x32_bf16 v[160:163], v[40:43], v[96:99], v[160:163]\n"
    "ds_read_b64 v[28:29], v8 offset:0\n"
    "ds_read_b64 v[30:31], v9 offset:0\n"
    "s_nop 1\n"
    "v_mfma_f32_16x16x32_bf16 v[164:167], v[44:47], v[92:95], v[164:167]\n"
    "ds_read_b64 v[32:33], v8 offset:4096\n"
    "s_sub_u32 s69, s68, s87\n"
    "s_mov_b32 s87, s68\n"
    "s_nop 0\n"
    "v_mfma_f32_16x16x32_bf16 v[168:171], v[44:47], v[96:99], v[168:171]\n"
    "ds_read_b64 v[34:35], v9 offset:4096\n"
    "ds_read_b64 v[36:37], v3 offset:2048\n"
    "s_cmp_lg_u32 s66, s67\n"
    "s_cbranch_scc1 L_nf_h_%=\n"
    "s_lshl_b32 s64, s61, 5\n"
    "s_add_u32 s64, s64, s60\n"
    "s_lshl_b32 s64, s64, 3\n"
    "v_mov_b32 v249, s64\n"
    "v_mov_b32 v250, s84\n"
    "v_mov_b32 v251, s85\n"
    "s_mov_b64 exec, 1\n"
    "global_store_dwordx2 v249, v[250:251], s[58:59] offset:1024 sc1\n"
    "s_mov_b64 exec, -1\n"
    "L_nf_h_%=:\n"
    "v_mfma_f32_16x16x32_bf16 v[172:175], v[48:51], v[92:95], v[172:175]\n"
    "v_max_f32 v248, v216, v220\n"
    "v_cmp_lt_f32 vcc, s80, v248\n"
    "v_add_u32 v1, s69, v1\n"
    "v_add_u32 v2, s69, v2\n"
    "ds_read_b64 v[38:39], v4 offset:2048\n"
    "s_sub_u32 s66, s66, 1\n"
    "v_mfma_f32_16x16x32_bf16 v[176:179], v[48:51], v[96:99], v[176:179]\n"
    "s_cbranch_vccnz L_rare_eva_%=\n"
    "L_ret_eva_%=:\n"
    "s_cmp_lg_u32 s66, 0\n"
    "s_cbranch_scc1 L_att_%=\n"
    "s_waitcnt lgkmcnt(8)\n"
    "v_mfma_f32_16x16x32_bf16 v[52:55], v[20:23], v[180:183], v[232:235]\n"
    "v_exp_f32 v100, v68\n"
    "ds_read_b64 v[40:41], v3 offset:6144\n"
    "v_mfma_f32_16x16x32_bf16 v[56:59], v[20:23], v[184:187], v[236:239]\n"
    "v_exp_f32 v101, v69\n"
    "ds_read_b64 v[42:43], v4 offset:6144\n"
    "global_load_dwordx2 v[108:109], v7, s[58:59] offset:1024 sc1\n"
    "s_waitcnt lgkmcnt(8)\n"
    "v_mfma_f32_16x16x32_bf16 v[60:63], v[24:27], v[180:183], v[232:235]\n"
    "v_exp_f32 v102, v72\n"
    "ds_read_b64 v[44:45], v8 offset:2048\n"
    "s_cmp_lt_u32 s7, 4\n"
    "s_cselect_b32 s36, s12, s20\n"
    "s_cselect_b32 s37, s13, s21\n"
    "s_cselect_b32 s38, s14, s22\n"
    "s_cselect_b32 s39, s15, s23\n"
    "v_mfma_f32_16x16x32_bf16 v[64:67], v[24:27], v[184:187], v[236:239]\n"
    "v_exp_f32 v103, v73\n"
    "ds_read_b64 v[46:47], v9 offset:2048\n"
    "s_lshl_b32 s4, s72, 17\n"
    "s_lshr_b32 s5, s7, 1\n"
    "s_lshl_b32 s5, s5, 15\n"
    "s_add_u32 s4, s4, s5\n"
    "s_waitcnt lgkmcnt(8)\n"
    "v_mfma_f32_16x16x32_bf16 v[52:55], v[28:31], v[188:191], v[52:55]\n"
    "v_cvt_pk_bf16_f32 v92, v100, v101\n"
    "ds_read_b64 v[48:49], v8 offset:6144\n"
    "s_movk_i32 s74, 2048\n"
    "s_movk_i32 s71, 1024\n"
    "s_cmp_lt_u32 s7, 4\n"
    "s_cselect_b32 s74, 32768, s74\n"
    "s_cselect_b32 s71, 128, s71\n"
    "v_mfma_f32_16x16x32_bf16 v[56:59], v[28:31], v[192:195], v[56:59]\n"
    "ds_read_b64 v[50:51], v9 offset:6144\n"
    "s_lshl_b32 s5, s73, 8\n"
    "s_add_u32 s4, s4, s5\n"
    "s_and_b32 s5, s7, 1\n"
    "s_lshl_b32 s5, s5, 7\n"
    "s_waitcnt lgkmcnt(8)\n"
    "v_mfma_f32_16x16x32_bf16 v[60:63], v[32:35], v[188:191], v[60:63]\n"
    "v_exp_f32 v104, v70\n"
    "ds_read_b64_tr_b16 v[20:21], v1 offset:0\n"
    "s_cmp_lt_u32 s7, 4\n"
    "s_cselect_b64 vcc, -1, 0\n"
    "v_cndmask_b32 v10, v15, v18, vcc\n"
    "s_lshl_b32 s64, s7, 13\n"
    "s_and_b32 s65, s7, 3\n"
    "v_mfma_f32_16x16x32_bf16 v[64:67], v[32:35], v[192:195], v[64:67]\n"
    "v_exp_f32 v105, v71\n"
    "ds_read_b64_tr_b16 v[22:23], v1 offset:4096\n"
    "s_add_u32 s4, s4, s5\n"
    "s_lshl_b32 s5, s70, 5\n"
    "s_add_u32 s4, s4, s5\n"
    "s_waitcnt lgkmcnt(8)\n"
    "v_mfma_f32_16x16x32_bf16 v[52:55], v[36:39], v[196:199], v[52:55]\n"
    "v_exp_f32 v106, v74\n"
    "ds_read_b64_tr_b16 v[24:25], v2 offset:0\n"
    "s_mul_i32 s65, s65, 87\n"
    "s_mul_i32 s79, s70, 22\n"
    "s_add_u32 s65, s65, s79\n"
    "s_lshl_b32 s65, s65, 10\n"
    "v_mfma_f32_16x16x32_bf16 v[56:59], v[36:39], v[200:203], v[56:59]\n"
    "v_exp_f32 v107, v75\n"
    "ds_read_b64_tr_b16 v[26:27], v2 offset:4096\n"
    "s_add_u32 s24, s40, s4\n"
    "s_addc_u32 s25, s41, 0\n"
    "s_waitcnt lgkmcnt(8)\n"
    "v_mfma_f32_16x16x32_bf16 v[60:63], v[40:43], v[196:199], v[60:63]\n"
    "ds_read_b64_tr_b16 v[28:29], v1 offset:512\n"
    "s_cmp_lt_u32 s7, 4\n"
    "s_cselect_b32 s79, s64, s65\n"
    "s_and_b32 s25, s25, 0xffff\n"
    "s_mov_b32 s26, 31776\n"
    "s_mov_b32 s27, 0x20000\n"
    "v_mfma_f32_16x16x32_bf16 v[64:67], v[40:43], v[200:203], v[64:67]\n"
    "v_cvt_pk_bf16_f32 v96, v102, v103\n"
    "v_cvt_pk_bf16_f32 v93, v104, v105\n"
    "ds_read_b64_tr_b16 v[30:31], v1 offset:4608\n"
    "s_add_u32 s28, s56, s4\n"
    "s_addc_u32 s29, s57, 0\n"
    "s_waitcnt lgkmcnt(8)\n"
    "v_mfma_f32_16x16x32_bf16 v[52:55], v[44:47], v[204:207], v[52:55]\n"
    "v_exp_f32 v100, v76\n"
    "ds_read_b64_tr_b16 v[32:33], v2 offset:512\n"
    "v_mfma_f32_16x16x32_bf16 v[56:59], v[44:47], v[208:211], v[56:59]\n"
    "v_exp_f32 v101, v77\n"
    "ds_read_b64_tr_b16 v[34:35], v2 offset:4608\n"
    "s_waitcnt lgkmcnt(8)\n"
    "v_mfma_f32_16x16x32_bf16 v[60:63], v[48:51], v[204:207], v[60:63]\n"
    "v_exp_f32 v102, v80\n"
    "ds_read_b64_tr_b16 v[36:37], v1 offset:1024\n"
    "v_mfma_f32_16x16x32_bf16 v[64:67], v[48:51], v[208:211], v[64:67]\n"
    "ds_read_b64_tr_b16 v[38:39], v1 offset:5120\n"
    "s_and_b32 s29, s29, 0xffff\n"
    "s_mov_b32 s30, 31776\n"
    "s_mov_b32 s31, 0x20000\n"
    "s_waitcnt lgkmcnt(8)\n"
    "v_mfma_f32_16x16x32_bf16 v[116:119], v[20:23], v[84:87], v[116:119]\n"
    "v_exp_f32 v103, v81\n"
    "ds_read_b64_tr_b16 v[40:41], v2 offset:1024\n"
    "s_lshl_b32 s51, s72, 2\n"
    "s_add_u32 s51, s51, s73\n"
    "s_lshl_b32 s52, s51, 17\n"
    "s_lshl_b32 s53, s7, 10\n"
    "v_mfma_f32_16x16x32_bf16 v[120:123], v[20:23], v[88:91], v[120:123]\n"
    "v_cvt_pk_bf16_f32 v97, v106, v107\n"
    "v_cvt_pk_bf16_f32 v94, v100, v101\n"
    "ds_read_b64_tr_b16 v[42:43], v2 offset:5120\n"
    "s_waitcnt lgkmcnt(8)\n"
    "v_mfma_f32_16x16x32_bf16 v[124:127], v[24:27], v[84:87], v[124:127]\n"
    "v_exp_f32 v104, v78\n"
    "ds_read_b64_tr_b16 v[44:45], v1 offset:1536\n"
    "s_waitcnt vmcnt(0)\n"
    "v_cmp_ne_u64 vcc, s[84:85], v[108:109]\n"
    "s_nop 0\n"
    "s_cbranch_vccnz L_hretry_%=\n"
    "L_hok_%=:\n"
    "v_mfma_f32_16x16x32_bf16 v[128:131], v[24:27], v[88:91], v[128:131]\n"
    "v_exp_f32 v105, v79\n"
    "ds_read_b64_tr_b16 v[46:47], v1 offset:5632\n"
    "s_waitcnt lgkmcnt(8)\n"
    "v_mfma_f32_16x16x32_bf16 v[132:135], v[28:31], v[84:87], v[132:135]\n"
    "ds_read_b64_tr_b16 v[48:49], v2 offset:1536\n"
    "s_add_u32 m0, s75, 98304\n"
    "s_nop 0\n"
    "buffer_load_dwordx4 v10, s[36:39], s79 offen sc1 lds\n"
    "s_add_u32 s52, s52, s53\n"
    "s_add_u32 s52, s52, 25165824\n"
    "s_lshl_b32 s51, s51, 5\n"
    "s_add_u32 s51, s51, 40960\n"
    "v_mfma_f32_16x16x32_bf16 v[136:139], v[28:31], v[88:91], v[136:139]\n"
    "v_exp_f32 v106, v82\n"
    "ds_read_b64_tr_b16 v[50:51], v2 offset:5632\n"
    "s_waitcnt lgkmcnt(8)\n"
    "v_mfma_f32_16x16x32_bf16 v[140:143], v[32:35], v[84:87], v[140:143]\n"
    "v_cvt_pk_bf16_f32 v98, v102, v103\n"
    "ds_read_b64 v[20:21], v3 offset:8192\n"
    "s_add_u32 s40, s58, s52\n"
    "s_addc_u32 s41, s59, 0\n"
    "v_mfma_f32_16x16x32_bf16 v[144:147], v[32:35], v[88:91], v[144:147]\n"
    "v_exp_f32 v107, v83\n"
    "ds_read_b64 v[22:23], v4 offset:8192\n"
    "s_waitcnt lgkmcnt(8)\n"
    "v_mfma_f32_16x16x32_bf16 v[148:151], v[36:39], v[84:87], v[148:151]\n"
    "v_cvt_pk_bf16_f32 v95, v104, v105\n"
    "v_cvt_pk_bf16_f32 v99, v106, v107\n"
    "ds_read_b64 v[24:25], v3 offset:12288\n"
    "s_and_b32 s41, s41, 0xffff\n"
    "s_mov_b32 s42, 8519680\n"
    "s_mov_b32 s43, 0x20000\n"
    "v_mfma_f32_16x16x32_bf16 v[152:155], v[36:39], v[88:91], v[152:155]\n"
    "v_and_b32 v11, 3, v0\n"
    "v_lshl_add_u32 v11, v11, 3, s51\n"
    "ds_read_b64 v[26:27], v4 offset:12288\n"
    "s_waitcnt lgkmcnt(8)\n"
    "v_mfma_f32_16x16x32_bf16 v[156:159], v[40:43], v[84:87], v[156:159]\n"
    "ds_read_b64 v[28:29], v8 offset:8192\n"
    "s_add_u32 s62, s79, s71\n"
    "s_add_u32 m0, s75, 99328\n"
    "s_nop 0\n"
    "buffer_load_dwordx4 v10, s[36:39], s62 offen sc1 lds\n"
    "s_add_u32 s79, s79, s74\n"
    "v_mfma_f32_16x16x32_bf16 v[224:227], v[212:215], v[92:95], v[216:219]\n"
    "s_nop 3\n"
    "v_mfma_f32_16x16x32_bf16 v[228:231], v[212:215], v[96:99], v[220:223]\n"
    "s_nop 3\n"
    "v_mfma_f32_16x16x32_bf16 v[160:163], v[40:43], v[88:91], v[160:163]\n"
    "ds_read_b64 v[30:31], v9 offset:8192\n"
    "s_add_u32 s52, s70, 1\n"
    "s_and_b32 s52, s52, 3\n"
    "s_lshl_b32 s53, s52, 15\n"
    "s_lshl_b32 s50, s70, 13\n"
    "s_waitcnt lgkmcnt(8)\n"
    "v_mfma_f32_16x16x32_bf16 v[164:167], v[44:47], v[84:87], v[164:167]\n"
    "ds_read_b64 v[32:33], v8 offset:12288\n"
    "s_add_u32 m0, s75, 114688\n"
    "s_nop 0\n"
    "buffer_load_dwordx4 v10, s[36:39], s79 offen sc1 lds\n"
    "v_mfma_f32_16x16x32_bf16 v[168:171], v[44:47], v[88:91], v[168:171]\n"
    "ds_read_b64 v[34:35], v9 offset:12288\n"
    "s_add_u32 s44, s50, s53\n"
    "s_lshl_b32 s53, s52, 13\n"
    "s_lshl_b32 s50, s70, 15\n"
    "s_add_u32 s47, s50, s53\n"
    "s_waitcnt lgkmcnt(8)\n"
    "v_mfma_f32_16x16x32_bf16 v[172:175], v[48:51], v[84:87], v[172:175]\n"
    "v_max_f32 v248, v224, v228\n"
    "v_cmp_lt_f32 vcc, s80, v248\n"
    "ds_read_b64 v[36:37], v3 offset:10240\n"
    "s_add_u32 s62, s79, s71\n"
    "s_add_u32 m0, s75, 115712\n"
    "s_nop 0\n"
    "buffer_load_dwordx4 v10, s[36:39], s62 offen sc1 lds\n"
    "s_add_u32 s79, s79, s74\n"
    "v_mfma_f32_16x16x32_bf16 v[176:179], v[48:51], v[88:91], v[176:179]\n"
    "ds_read_b64 v[38:39], v4 offset:10240\n"
    "s_cbranch_vccnz L_rare_od5_%=\n"
    "L_ret_od5_%=:\n"
    "s_waitcnt lgkmcnt(8)\n"
    "v_mfma_f32_16x16x32_bf16 v[68:71], v[20:23], v[180:183], v[232:235]\n"
    "v_exp_f32 v100, v52\n"
    "ds_read_b64 v[40:41], v3 offset:14336\n"
    "s_add_u32 s52, s70, 2\n"
    "s_and_b32 s52, s52, 3\n"
    "s_lshl_b32 s53, s52, 15\n"
    "s_lshl_b32 s50, s70, 13\n"
    "v_mfma_f32_16x16x32_bf16 v[72:75], v[20:23], v[184:187], v[236:239]\n"
    "v_exp_f32 v101, v53\n"
    "ds_read_b64 v[42:43], v4 offset:14336\n"
    "s_waitcnt lgkmcnt(8)\n"
    "v_mfma_f32_16x16x32_bf16 v[76:79], v[24:27], v[180:183], v[232:235]\n"
    "v_exp_f32 v102, v56\n"
    "ds_read_b64 v[44:45], v8 offset:10240\n"
    "s_add_u32 s45, s50, s53\n"
    "s_lshl_b32 s53, s52, 13\n"
    "s_lshl_b32 s50, s70, 15\n"
    "s_add_u32 s48, s50, s53\n"
    "v_mfma_f32_16x16x32_bf16 v[80:83], v[24:27], v[184:187], v[236:239]\n"
    "v_exp_f32 v103, v57\n"
    "ds_read_b64 v[46:47], v9 offset:10240\n"
    "s_waitcnt lgkmcnt(8)\n"
    "v_mfma_f32_16x16x32_bf16 v[68:71], v[28:31], v[188:191], v[68:71]\n"
    "v_cvt_pk_bf16_f32 v84, v100, v101\n"
    "ds_read_b64 v[48:49], v8 offset:14336\n"
    "s_add_u32 s52, s70, 3\n"
    "s_and_b32 s52, s52, 3\n"
    "s_lshl_b32 s53, s52, 15\n"
    "s_lshl_b32 s50, s70, 13\n"
    "v_mfma_f32_16x16x32_bf16 v[72:75], v[28:31], v[192:195], v[72:75]\n"
    "ds_read_b64 v[50:51], v9 offset:14336\n"
    "s_add_u32 m0, s75, 131072\n"
    "s_nop 0\n"
    "buffer_load_dwordx4 v10, s[36:39], s79 offen sc1 lds\n"
    "s_add_u32 s46, s50, s53\n"
    "s_lshl_b32 s53, s52, 13\n"
    "s_lshl_b32 s50, s70, 15\n"
    "s_add_u32 s49, s50, s53\n"
    "s_waitcnt lgkmcnt(8)\n"
    "v_mfma_f32_16x16x32_bf16 v[76:79], v[32:35], v[188:191], v[76:79]\n"
    "v_exp_f32 v104, v54\n"
    "ds_read_b64_tr_b16 v[20:21], v1 offset:8192\n"
    "s_add_u32 s62, s79, s71\n"
    "s_add_u32 m0, s75, 132096\n"
    "s_nop 0\n"
    "buffer_load_dwordx4 v10, s[36:39], s62 offen sc1 lds\n"
    "s_add_u32 s79, s79, s74\n"
    "v_mfma_f32_16x16x32_bf16 v[80:83], v[32:35], v[192:195], v[80:83]\n"
    "v_exp_f32 v105, v55\n"
    "ds_read_b64_tr_b16 v[22:23], v1 offset:12288\n"
    "s_add_u32 s68, s86, 0x8000\n"
    "s_cmp_eq_u32 s68, 0x28000\n"
    "s_cselect_b32 s68, 0, s68\n"
    "s_waitcnt lgkmcnt(8)\n"
    "v_mfma_f32_16x16x32_bf16 v[68:71], v[36:39], v[196:199], v[68:71]\n"
    "v_exp_f32 v106, v58\n"
    "ds_read_b64_tr_b16 v[24:25], v2 offset:8192\n"
    "s_and_b32 s51, s7, 6\n"
    "s_mul_i32 s77, s51, 132096\n"
    "s_lshl_b32 s51, s70, 15\n"
    "s_add_u32 s77, s77, s51\n"
    "v_mfma_f32_16x16x32_bf16 v[72:75], v[36:39], v[200:203], v[72:75]\n"
    "v_exp_f32 v107, v59\n"
    "ds_read_b64_tr_b16 v[26:27], v2 offset:12288\n"
    "s_sub_u32 s69, s68, s86\n"
    "s_mov_b32 s86, s68\n"
    "s_waitcnt lgkmcnt(8)\n"
    "v_mfma_f32_16x16x32_bf16 v[76:79], v[40:43], v[196:199], v[76:79]\n"
    "ds_read_b64_tr_b16 v[28:29], v1 offset:8704\n"
    "s_add_u32 m0, s75, 147456\n"
    "s_nop 0\n"
    "buffer_load_dwordx4 v10, s[36:39], s79 offen sc1 lds\n"
    "s_and_b32 s53, s7, 1\n"
    "s_lshl_b32 s51, s53, 14\n"
    "s_add_u32 s77, s77, s51\n"
    "s_lshl_b32 s53, s53, 10\n"
    "v_mfma_f32_16x16x32_bf16 v[80:83], v[40:43], v[200:203], v[80:83]\n"
    "v_cvt_pk_bf16_f32 v88, v102, v103\n"
    "v_cvt_pk_bf16_f32 v85, v104, v105\n"
    "ds_read_b64_tr_b16 v[30:31], v1 offset:12800\n"
    "s_add_u32 s50, s70, 0\n"
    "s_and_b32 s51, s50, 1\n"
    "s_lshl_b32 s51, s51, 5\n"
    "s_and_b32 s50, s50, 2\n"
    "s_waitcnt lgkmcnt(8)\n"
    "v_mfma_f32_16x16x32_bf16 v[68:71], v[44:47], v[204:207], v[68:71]\n"
    "v_exp_f32 v100, v60\n"
    "ds_read_b64_tr_b16 v[32:33], v2 offset:8704\n"
    "s_lshl_b32 s50, s50, 8\n"
    "s_add_u32 s50, s50, s53\n"
    "s_add_u32 s50, s50, 98304\n"
    "v_mfma_f32_16x16x32_bf16 v[72:75], v[44:47], v[208:211], v[72:75]\n"
    "v_exp_f32 v101, v61\n"
    "ds_read_b64_tr_b16 v[34:35], v2 offset:12800\n"
    "s_waitcnt lgkmcnt(8)\n"
    "v_mfma_f32_16x16x32_bf16 v[76:79], v[48:51], v[204:207], v[76:79]\n"
    "v_exp_f32 v102, v64\n"
    "v_xor_b32 v240, s51, v17\n"
    "v_add_u32 v240, s50, v240\n"
    "ds_read_b64_tr_b16 v[36:37], v1 offset:9216\n"
    "v_mfma_f32_16x16x32_bf16 v[80:83], v[48:51], v[208:211], v[80:83]\n"
    "ds_read_b64_tr_b16 v[38:39], v1 offset:13312\n"
    "s_add_u32 s62, s79, s71\n"
    "s_add_u32 m0, s75, 148480\n"
    "s_nop 0\n"
    "buffer_load_dwordx4 v10, s[36:39], s62 offen sc1 lds\n"
    "s_add_u32 s79, s79, s74\n"
    "s_waitcnt lgkmcnt(8)\n"
    "v_mfma_f32_16x16x32_bf16 v[116:119], v[20:23], v[92:95], v[116:119]\n"
    "v_exp_f32 v103, v65\n"
    "v_add_u32 v3, s69, v3\n"
    "v_add_u32 v4, s69, v4\n"
    "ds_read_b64_tr_b16 v[40:41], v2 offset:9216\n"
    "v_mfma_f32_16x16x32_bf16 v[120:123], v[20:23], v[96:99], v[120:123]\n"
    "v_cvt_pk_bf16_f32 v89, v106, v107\n"
    "v_cvt_pk_bf16_f32 v86, v100, v101\n"
    "ds_read_b64_tr_b16 v[42:43], v2 offset:13312\n"
    "s_waitcnt lgkmcnt(8)\n"
    "v_mfma_f32_16x16x32_bf16 v[124:127], v[24:27], v[92:95], v[124:127]\n"
    "v_exp_f32 v104, v62\n"
    "ds_read_b64_tr_b16 v[44:45], v1 offset:9728\n"
    "v_mfma_f32_16x16x32_bf16 v[128:131], v[24:27], v[96:99], v[128:131]\n"
    "v_exp_f32 v105, v63\n"
    "ds_read_b64_tr_b16 v[46:47], v1 offset:13824\n"
    "s_waitcnt lgkmcnt(8)\n"
    "v_mfma_f32_16x16x32_bf16 v[132:135], v[28:31], v[92:95], v[132:135]\n"
    "v_add_u32 v8, s69, v8\n"
    "v_add_u32 v9, s69, v9\n"
    "ds_read_b64_tr_b16 v[48:49], v2 offset:9728\n"
    "s_add_u32 s50, s70, 1\n"
    "s_and_b32 s51, s50, 1\n"
    "s_lshl_b32 s51, s51, 5\n"
    "s_and_b32 s50, s50, 2\n"
    "v_mfma_f32_16x16x32_bf16 v[136:139], v[28:31], v[96:99], v[136:139]\n"
    "v_exp_f32 v106, v66\n"
    "ds_read_b64_tr_b16 v[50:51], v2 offset:13824\n"
    "s_waitcnt lgkmcnt(8)\n"
    "v_mfma_f32_16x16x32_bf16 v[140:143], v[32:35], v[92:95], v[140:143]\n"
    "v_cvt_pk_bf16_f32 v90, v102, v103\n"
    "s_lshl_b32 s50, s50, 8\n"
    "s_add_u32 s50, s50, s53\n"
    "s_add_u32 s50, s50, 98304\n"
    "s_add_u32 s68, s87, 0x8000\n"
    "s_cmp_eq_u32 s68, 0x28000\n"
    "s_cselect_b32 s68, 0, s68\n"
    "v_mfma_f32_16x16x32_bf16 v[144:147], v[32:35], v[96:99], v[144:147]\n"
    "v_exp_f32 v107, v67\n"
    "s_waitcnt lgkmcnt(6)\n"
    "v_mfma_f32_16x16x32_bf16 v[148:151], v[36:39], v[92:95], v[148:151]\n"
    "v_cvt_pk_bf16_f32 v87, v104, v105\n"
    "v_cvt_pk_bf16_f32 v91, v106, v107\n"
    "v_xor_b32 v241, s51, v17\n"
    "v_add_u32 v241, s50, v241\n"
    "v_mfma_f32_16x16x32_bf16 v[152:155], v[36:39], v[96:99], v[152:155]\n"
    "s_waitcnt vmcnt(0)\n"
    "s_waitcnt lgkmcnt(0)\n"
    "s_barrier\n"
    "ds_read_b64 v[20:21], v3 offset:0\n"
    "ds_read_b64 v[22:23], v4 offset:0\n"
    "v_mfma_f32_16x16x32_bf16 v[156:159], v[40:43], v[92:95], v[156:159]\n"
    "ds_read_b64 v[24:25], v3 offset:4096\n"
    "ds_read_b64 v[26:27], v4 offset:4096\n"
    "s_nop 1\n"
    "v_mfma_f32_16x16x32_bf16 v[216:219], v[212:215], v[84:87], v[224:227]\n"
    "s_nop 3\n"
    "v_mfma_f32_16x16x32_bf16 v[220:223], v[212:215], v[88:91], v[228:231]\n"
    "s_nop 3\n"
    "v_mfma_f32_16x16x32_bf16 v[160:163], v[40:43], v[96:99], v[160:163]\n"
    "ds_read_b64 v[28:29], v8 offset:0\n"
    "ds_read_b64 v[30:31], v9 offset:0\n"
    "s_nop 1\n"
    "v_mfma_f32_16x16x32_bf16 v[164:167], v[44:47], v[92:95], v[164:167]\n"
    "ds_read_b64 v[32:33], v8 offset:4096\n"
    "s_sub_u32 s69, s68, s87\n"
    "s_mov_b32 s87, s68\n"
    "s_nop 0\n"
    "v_mfma_f32_16x16x32_bf16 v[168:171], v[44:47], v[96:99], v[168:171]\n"
    "ds_read_b64 v[34:35], v9 offset:4096\n"
    "ds_read_b64 v[36:37], v3 offset:2048\n"
    "s_nop 1\n"
    "v_mfma_f32_16x16x32_bf16 v[172:175], v[48:51], v[92:95], v[172:175]\n"
    "v_max_f32 v248, v216, v220\n"
    "v_cmp_lt_f32 vcc, s80, v248\n"
    "v_add_u32 v1, s69, v1\n"
    "v_add_u32 v2, s69, v2\n"
    "ds_read_b64 v[38:39], v4 offset:2048\n"
    "v_mfma_f32_16x16x32_bf16 v[176:179], v[48:51], v[96:99], v[176:179]\n"
    "s_cbranch_vccnz L_rare_ev5_%=\n"
    "L_ret_ev5_%=:\n"
    "s_cmp_ge_u32 s7, 4\n"
    "s_cbranch_scc1 L_pr2_%=\n"
    "s_setprio 2\n"
    "L_pr2_%=:\n"
    "s_waitcnt lgkmcnt(8)\n"
    "v_mfma_f32_16x16x32_bf16 v[52:55], v[20:23], v[180:183], v[232:235]\n"
    "v_exp_f32 v100, v68\n"
    "ds_read_b64 v[40:41], v3 offset:6144\n"
    "v_mfma_f32_16x16x32_bf16 v[56:59], v[20:23], v[184:187], v[236:239]\n"
    "v_exp_f32 v101, v69\n"
    "ds_read_b64 v[42:43], v4 offset:6144\n"
    "s_waitcnt lgkmcnt(8)\n"
    "v_mfma_f32_16x16x32_bf16 v[60:63], v[24:27], v[180:183], v[232:235]\n"
    "v_exp_f32 v102, v72\n"
    "ds_read_b64 v[44:45], v8 offset:2048\n"
    "v_mfma_f32_16x16x32_bf16 v[64:67], v[24:27], v[184:187], v[236:239]\n"
    "v_exp_f32 v103, v73\n"
    "ds_read_b64 v[46:47], v9 offset:2048\n"
    "s_waitcnt lgkmcnt(8)\n"
    "v_mfma_f32_16x16x32_bf16 v[52:55], v[28:31], v[188:191], v[52:55]\n"
    "v_cvt_pk_bf16_f32 v92, v100, v101\n"
    "ds_read_b64 v[48:49], v8 offset:6144\n"
    "s_add_u32 s50, s70, 2\n"
    "s_and_b32 s51, s50, 1\n"
    "s_lshl_b32 s51, s51, 5\n"
    "s_and_b32 s50, s50, 2\n"
    "v_mfma_f32_16x16x32_bf16 v[56:59], v[28:31], v[192:195], v[56:59]\n"
    "ds_read_b64 v[50:51], v9 offset:6144\n"
    "s_add_u32 m0, s75, 0\n"
    "s_nop 0\n"
    "buffer_load_dwordx4 v10, s[36:39], s79 offen sc1 lds\n"
    "s_lshl_b32 s50, s50, 8\n"
    "s_add_u32 s50, s50, s53\n"
    "s_add_u32 s50, s50, 98304\n"
    "s_waitcnt lgkmcnt(8)\n"
    "v_mfma_f32_16x16x32_bf16 v[60:63], v[32:35], v[188:191], v[60:63]\n"
    "v_exp_f32 v104, v70\n"
    "v_xor_b32 v242, s51, v17\n"
    "v_add_u32 v242, s50, v242\n"
    "ds_read_b64_tr_b16 v[20:21], v1 offset:0\n"
    "v_mfma_f32_16x16x32_bf16 v[64:67], v[32:35], v[192:195], v[64:67]\n"
    "v_exp_f32 v105, v71\n"
    "ds_read_b64_tr_b16 v[22:23], v1 offset:4096\n"
    "s_waitcnt lgkmcnt(8)\n"
    "v_mfma_f32_16x16x32_bf16 v[52:55], v[36:39], v[196:199], v[52:55]\n"
    "v_exp_f32 v106, v74\n"
    "ds_read_b64_tr_b16 v[24:25], v2 offset:0\n"
    "s_add_u32 s50, s70, 3\n"
    "s_and_b32 s51, s50, 1\n"
    "s_lshl_b32 s51, s51, 5\n"
    "s_and_b32 s50, s50, 2\n"
    "v_mfma_f32_16x16x32_bf16 v[56:59], v[36:39], v[200:203], v[56:59]\n"
    "v_exp_f32 v107, v75\n"
    "ds_read_b64_tr_b16 v[26:27], v2 offset:4096\n"
    "s_waitcnt lgkmcnt(8)\n"
    "v_mfma_f32_16x16x32_bf16 v[60:63], v[40:43], v[196:199], v[60:63]\n"
    "ds_read_b64_tr_b16 v[28:29], v1 offset:512\n"
    "s_add_u32 s62, s79, s71\n"
    "s_add_u32 m0, s75, 1024\n"
    "s_nop 0\n"
    "buffer_load_dwordx4 v10, s[36:39], s62 offen sc1 lds\n"
    "s_add_u32 s79, s79, s74\n"
    "v_mfma_f32_16x16x32_bf16 v[64:67], v[40:43], v[200:203], v[64:67]\n"
    "v_cvt_pk_bf16_f32 v96, v102, v103\n"
    "v_cvt_pk_bf16_f32 v93, v104, v105\n"
    "ds_read_b64_tr_b16 v[30:31], v1 offset:4608\n"
    "s_waitcnt lgkmcnt(8)\n"
    "v_mfma_f32_16x16x32_bf16 v[52:55], v[44:47], v[204:207], v[52:55]\n"
    "v_exp_f32 v100, v76\n"
    "ds_read_b64_tr_b16 v[32:33], v2 offset:512\n"
    "v_mfma_f32_16x16x32_bf16 v[56:59], v[44:47], v[208:211], v[56:59]\n"
    "v_exp_f32 v101, v77\n"
    "ds_read_b64_tr_b16 v[34:35], v2 offset:4608\n"
    "s_waitcnt lgkmcnt(8)\n"
    "v_mfma_f32_16x16x32_bf16 v[60:63], v[48:51], v[204:207], v[60:63]\n"
    "v_exp_f32 v102, v80\n"
    "ds_read_b64_tr_b16 v[36:37], v1 offset:1024\n"
    "v_mfma_f32_16x16x32_bf16 v[64:67], v[48:51], v[208:211], v[64:67]\n"
    "ds_read_b64_tr_b16 v[38:39], v1 offset:5120\n"
    "s_add_u32 m0, s75, 16384\n"
    "s_nop 0\n"
    "buffer_load_dwordx4 v10, s[36:39], s79 offen sc1 lds\n"
    "s_waitcnt lgkmcnt(8)\n"
    "v_mfma_f32_16x16x32_bf16 v[116:119], v[20:23], v[84:87], v[116:119]\n"
    "v_exp_f32 v103, v81\n"
    "ds_read_b64_tr_b16 v[40:41], v2 offset:1024\n"
    "v_mfma_f32_16x16x32_bf16 v[120:123], v[20:23], v[88:91], v[120:123]\n"
    "v_cvt_pk_bf16_f32 v97, v106, v107\n"
    "v_cvt_pk_bf16_f32 v94, v100, v101\n"
    "ds_read_b64_tr_b16 v[42:43], v2 offset:5120\n"
    "s_waitcnt lgkmcnt(8)\n"
    "v_mfma_f32_16x16x32_bf16 v[124:127], v[24:27], v[84:87], v[124:127]\n"
    "v_exp_f32 v104, v78\n"
    "ds_read_b64_tr_b16 v[44:45], v1 offset:1536\n"
    "v_mfma_f32_16x16x32_bf16 v[128:131], v[24:27], v[88:91], v[128:131]\n"
    "v_exp_f32 v105, v79\n"
    "ds_read_b64_tr_b16 v[46:47], v1 offset:5632\n"
    "s_waitcnt lgkmcnt(8)\n"
    "v_mfma_f32_16x16x32_bf16 v[132:135], v[28:31], v[84:87], v[132:135]\n"
    "ds_read_b64_tr_b16 v[48:49], v2 offset:1536\n"
    "s_add_u32 s62, s79, s71\n"
    "s_add_u32 m0, s75, 17408\n"
    "s_nop 0\n"
    "buffer_load_dwordx4 v10, s[36:39], s62 offen sc1 lds\n"
    "s_add_u32 s79, s79, s74\n"
    "v_mfma_f32_16x16x32_bf16 v[136:139], v[28:31], v[88:91], v[136:139]\n"
    "v_exp_f32 v106, v82\n"
    "ds_read_b64_tr_b16 v[50:51], v2 offset:5632\n"
    "s_waitcnt lgkmcnt(8)\n"
    "v_mfma_f32_16x16x32_bf16 v[140:143], v[32:35], v[84:87], v[140:143]\n"
    "v_cvt_pk_bf16_f32 v98, v102, v103\n"
    "ds_read_b64 v[20:21], v3 offset:8192\n"
    "s_lshl_b32 s50, s50, 8\n"
    "s_add_u32 s50, s50, s53\n"
    "s_add_u32 s50, s50, 98304\n"
    "v_mfma_f32_16x16x32_bf16 v[144:147], v[32:35], v[88:91], v[144:147]\n"
    "v_exp_f32 v107, v83\n"
    "ds_read_b64 v[22:23], v4 offset:8192\n"
    "s_waitcnt lgkmcnt(8)\n"
    "v_mfma_f32_16x16x32_bf16 v[148:151], v[36:39], v[84:87], v[148:151]\n"
    "v_cvt_pk_bf16_f32 v95, v104, v105\n"
    "v_cvt_pk_bf16_f32 v99, v106, v107\n"
    "ds_read_b64 v[24:25], v3 offset:12288\n"
    "v_mfma_f32_16x16x32_bf16 v[152:155], v[36:39], v[88:91], v[152:155]\n"
    "ds_read_b64 v[26:27], v4 offset:12288\n"
    "s_add_u32 m0, s75, 32768\n"
    "s_nop 0\n"
    "buffer_load_dwordx4 v10, s[36:39], s79 offen sc1 lds\n"
    "s_waitcnt lgkmcnt(8)\n"
    "v_mfma_f32_16x16x32_bf16 v[156:159], v[40:43], v[84:87], v[156:159]\n"
    "ds_read_b64 v[28:29], v8 offset:8192\n"
    "s_add_u32 s62, s79, s71\n"
    "s_add_u32 m0, s75, 33792\n"
    "s_nop 0\n"
    "buffer_load_dwordx4 v10, s[36:39], s62 offen sc1 lds\n"
    "s_add_u32 s79, s79, s74\n"
    "v_mfma_f32_16x16x32_bf16 v[224:227], v[212:215], v[92:95], v[216:219]\n"
    "v_mfma_f32_16x16x32_bf16 v[228:231], v[212:215], v[96:99], v[220:223]\n"
    "v_mfma_f32_16x16x32_bf16 v[160:163], v[40:43], v[88:91], v[160:163]\n"
    "v_xor_b32 v243, s51, v17\n"
    "v_add_u32 v243, s50, v243\n"
    "ds_read_b64 v[30:31], v9 offset:8192\n"
    "s_waitcnt lgkmcnt(8)\n"
    "v_mfma_f32_16x16x32_bf16 v[164:167], v[44:47], v[84:87], v[164:167]\n"
    "ds_read_b64 v[32:33], v8 offset:12288\n"
    "s_lshr_b32 s50, s7, 1\n"
    "s_lshl_b32 s50, s50, 11\n"
    "s_add_u32 s50, s50, 106496\n"
    "v_mfma_f32_16x16x32_bf16 v[168:171], v[44:47], v[88:91], v[168:171]\n"
    "ds_read_b64 v[34:35], v9 offset:12288\n"
    "s_waitcnt lgkmcnt(8)\n"
    "v_mfma_f32_16x16x32_bf16 v[172:175], v[48:51], v[84:87], v[172:175]\n"
    "v_max_f32 v248, v224, v228\n"
    "v_cmp_lt_f32 vcc, s80, v248\n"
    "v_add_u32 v14, s50, v15\n"
    "v_and_b32 v16, 15, v0\n"
    "ds_read_b64 v[36:37], v3 offset:10240\n"
    "v_mfma_f32_16x16x32_bf16 v[176:179], v[48:51], v[88:91], v[176:179]\n"
    "ds_read_b64 v[38:39], v4 offset:10240\n"
    "s_cbranch_vccnz L_rare_od6_%=\n"
    "L_ret_od6_%=:\n"
    "s_waitcnt lgkmcnt(8)\n"
    "v_mfma_f32_16x16x32_bf16 v[68:71], v[20:23], v[180:183], v[232:235]\n"
    "v_exp_f32 v100, v52\n"
    "ds_read_b64 v[40:41], v3 offset:14336\n"
    "v_mfma_f32_16x16x32_bf16 v[72:75], v[20:23], v[184:187], v[236:239]\n"
    "v_exp_f32 v101, v53\n"
    "ds_read_b64 v[42:43], v4 offset:14336\n"
    "s_waitcnt lgkmcnt(8)\n"
    "v_mfma_f32_16x16x32_bf16 v[76:79], v[24:27], v[180:183], v[232:235]\n"
    "v_exp_f32 v102, v56\n"
    "ds_read_b64 v[44:45], v8 offset:10240\n"
    "v_mfma_f32_16x16x32_bf16 v[80:83], v[24:27], v[184:187], v[236:239]\n"
    "v_exp_f32 v103, v57\n"
    "ds_read_b64 v[46:47], v9 offset:10240\n"
    "s_waitcnt lgkmcnt(8)\n"
    "v_mfma_f32_16x16x32_bf16 v[68:71], v[28:31], v[188:191], v[68:71]\n"
    "v_cvt_pk_bf16_f32 v84, v100, v101\n"
    "ds_read_b64 v[48:49], v8 offset:14336\n"
    "v_mfma_f32_16x16x32_bf16 v[72:75], v[28:31], v[192:195], v[72:75]\n"
    "v_bfe_u32 v12, v0, 4, 2\n"
    "v_lshlrev_b32 v12, 3, v12\n"
    "ds_read_b64 v[50:51], v9 offset:14336\n"
    "s_waitcnt lgkmcnt(8)\n"
    "v_mfma_f32_16x16x32_bf16 v[76:79], v[32:35], v[188:191], v[76:79]\n"
    "v_exp_f32 v104, v54\n"
    "ds_read_b64_tr_b16 v[20:21], v1 offset:8192\n"
    "v_mfma_f32_16x16x32_bf16 v[80:83], v[32:35], v[192:195], v[80:83]\n"
    "v_exp_f32 v105, v55\n"
    "ds_read_b64_tr_b16 v[22:23], v1 offset:12288\n"
    "s_waitcnt lgkmcnt(8)\n"
    "v_mfma_f32_16x16x32_bf16 v[68:71], v[36:39], v[196:199], v[68:71]\n"
    "v_exp_f32 v106, v58\n"
    "ds_read_b64_tr_b16 v[24:25], v2 offset:8192\n"
    "v_mfma_f32_16x16x32_bf16 v[72:75], v[36:39], v[200:203], v[72:75]\n"
    "v_exp_f32 v107, v59\n"
    "ds_read_b64_tr_b16 v[26:27], v2 offset:12288\n"
    "s_waitcnt lgkmcnt(8)\n"
    "v_mfma_f32_16x16x32_bf16 v[76:79], v[40:43], v[196:199], v[76:79]\n"
    "v_lshl_add_u32 v16, v16, 10, v12\n"
    "ds_read_b64_tr_b16 v[28:29], v1 offset:8704\n"
    "s_movk_i32 s78, 0x4000\n"
    "v_mfma_f32_16x16x32_bf16 v[80:83], v[40:43], v[200:203], v[80:83]\n"
    "v_cvt_pk_bf16_f32 v88, v102, v103\n"
    "v_cvt_pk_bf16_f32 v85, v104, v105\n"
    "ds_read_b64_tr_b16 v[30:31], v1 offset:12800\n"
    "s_waitcnt lgkmcnt(8)\n"
    "v_mfma_f32_16x16x32_bf16 v[68:71], v[44:47], v[204:207], v[68:71]\n"
    "v_exp_f32 v100, v60\n"
    "ds_read_b64_tr_b16 v[32:33], v2 offset:8704\n"
    "v_mfma_f32_16x16x32_bf16 v[72:75], v[44:47], v[208:211], v[72:75]\n"
    "v_exp_f32 v101, v61\n"
    "ds_read_b64_tr_b16 v[34:35], v2 offset:12800\n"
    "s_waitcnt lgkmcnt(8)\n"
    "v_mfma_f32_16x16x32_bf16 v[76:79], v[48:51], v[204:207], v[76:79]\n"
    "v_exp_f32 v102, v64\n"
    "ds_read_b64_tr_b16 v[36:37], v1 offset:9216\n"
    "v_mfma_f32_16x16x32_bf16 v[80:83], v[48:51], v[208:211], v[80:83]\n"
    "ds_read_b64_tr_b16 v[38:39], v1 offset:13312\n"
    "s_lshr_b32 s50, s7, 2\n"
    "s_lshl_b32 s50, s50, 19\n"
    "s_and_b32 s51, s7, 3\n"
    "s_lshl_b32 s51, s51, 13\n"
    "s_waitcnt lgkmcnt(8)\n"
    "v_mfma_f32_16x16x32_bf16 v[116:119], v[20:23], v[92:95], v[116:119]\n"
    "v_exp_f32 v103, v65\n"
    "ds_read_b64_tr_b16 v[40:41], v2 offset:9216\n"
    "v_mfma_f32_16x16x32_bf16 v[120:123], v[20:23], v[96:99], v[120:123]\n"
    "v_cvt_pk_bf16_f32 v89, v106, v107\n"
    "v_cvt_pk_bf16_f32 v86, v100, v101\n"
    "ds_read_b64_tr_b16 v[42:43], v2 offset:13312\n"
    "s_waitcnt lgkmcnt(8)\n"
    "v_mfma_f32_16x16x32_bf16 v[124:127], v[24:27], v[92:95], v[124:127]\n"
    "v_exp_f32 v104, v62\n"
    "ds_read_b64_tr_b16 v[44:45], v1 offset:9728\n"
    "v_mfma_f32_16x16x32_bf16 v[128:131], v[24:27], v[96:99], v[128:131]\n"
    "v_exp_f32 v105, v63\n"
    "ds_read_b64_tr_b16 v[46:47], v1 offset:13824\n"
    "s_waitcnt lgkmcnt(8)\n"
    "v_mfma_f32_16x16x32_bf16 v[132:135], v[28:31], v[92:95], v[132:135]\n"
    "ds_read_b64_tr_b16 v[48:49], v2 offset:9728\n"
    "s_add_u32 s76, s50, s51\n"
    "s_lshl_b32 s50, s7, 13\n"
    "s_add_u32 s51, s50, 49152\n"
    "s_xor_b32 s50, s50, 8192\n"
    "v_mfma_f32_16x16x32_bf16 v[136:139], v[28:31], v[96:99], v[136:139]\n"
    "v_exp_f32 v106, v66\n"
    "ds_read_b64_tr_b16 v[50:51], v2 offset:13824\n"
    "s_waitcnt lgkmcnt(8)\n"
    "v_mfma_f32_16x16x32_bf16 v[140:143], v[32:35], v[92:95], v[140:143]\n"
    "v_cvt_pk_bf16_f32 v90, v102, v103\n"
    "v_add_u32 v12, s51, v15\n"
    "s_add_u32 s50, s50, 49152\n"
    "v_add_u32 v13, s50, v15\n"
    "v_mfma_f32_16x16x32_bf16 v[144:147], v[32:35], v[96:99], v[144:147]\n"
    "v_exp_f32 v107, v67\n"
    "s_waitcnt lgkmcnt(6)\n"
    "v_mfma_f32_16x16x32_bf16 v[148:151], v[36:39], v[92:95], v[148:151]\n"
    "v_cvt_pk_bf16_f32 v87, v104, v105\n"
    "v_cvt_pk_bf16_f32 v91, v106, v107\n"
    "s_add_u32 s68, s87, 0x8000\n"
    "s_cmp_eq_u32 s68, 0x28000\n"
    "s_cselect_b32 s68, 0, s68\n"
    "v_mfma_f32_16x16x32_bf16 v[152:155], v[36:39], v[96:99], v[152:155]\n"
    "s_waitcnt lgkmcnt(4)\n"
    "v_mfma_f32_16x16x32_bf16 v[156:159], v[40:43], v[92:95], v[156:159]\n"
    "s_sub_u32 s69, s68, s87\n"
    "s_mov_b32 s87, s68\n"
    "v_mfma_f32_16x16x32_bf16 v[216:219], v[212:215], v[84:87], v[224:227]\n"
    "v_mfma_f32_16x16x32_bf16 v[220:223], v[212:215], v[88:91], v[228:231]\n"
    "v_mfma_f32_16x16x32_bf16 v[160:163], v[40:43], v[96:99], v[160:163]\n"
    "s_waitcnt lgkmcnt(2)\n"
    "v_mfma_f32_16x16x32_bf16 v[164:167], v[44:47], v[92:95], v[164:167]\n"
    "v_add_u32 v1, s69, v1\n"
    "v_add_u32 v2, s69, v2\n"
    "v_mfma_f32_16x16x32_bf16 v[168:171], v[44:47], v[96:99], v[168:171]\n"
    "s_waitcnt lgkmcnt(0)\n"
    "v_mfma_f32_16x16x32_bf16 v[172:175], v[48:51], v[92:95], v[172:175]\n"
    "v_max_f32 v248, v216, v220\n"
    "v_cmp_lt_f32 vcc, s80, v248\n"
    "v_mfma_f32_16x16x32_bf16 v[176:179], v[48:51], v[96:99], v[176:179]\n"
    "s_cbranch_vccnz L_rare_ev6_%=\n"
    "L_ret_ev6_%=:\n"
    "ds_read_b64_tr_b16 v[20:21], v1 offset:0\n"
    "ds_read_b64_tr_b16 v[22:23], v1 offset:4096\n"
    "ds_read_b64_tr_b16 v[24:25], v2 offset:0\n"
    "ds_read_b64_tr_b16 v[26:27], v2 offset:4096\n"
    "ds_read_b64_tr_b16 v[28:29], v1 offset:512\n"
    "ds_read_b64_tr_b16 v[30:31], v1 offset:4608\n"
    "ds_read_b64_tr_b16 v[32:33], v2 offset:512\n"
    "ds_read_b64_tr_b16 v[34:35], v2 offset:4608\n"
    "ds_read_b64_tr_b16 v[36:37], v1 offset:1024\n"
    "ds_read_b64_tr_b16 v[38:39], v1 offset:5120\n"
    "v_exp_f32 v100, v68\n"
    "v_exp_f32 v101, v69\n"
    "v_exp_f32 v102, v72\n"
    "v_exp_f32 v103, v73\n"
    "v_cvt_pk_bf16_f32 v92, v100, v101\n"
    "v_exp_f32 v104, v70\n"
    "v_exp_f32 v105, v71\n"
    "v_exp_f32 v106, v74\n"
    "s_waitcnt lgkmcnt(8)\n"
    "v_mfma_f32_16x16x32_bf16 v[116:119], v[20:23], v[84:87], v[116:119]\n"
    "v_exp_f32 v107, v75\n"
    "v_cvt_pk_bf16_f32 v96, v102, v103\n"
    "v_cvt_pk_bf16_f32 v93, v104, v105\n"
    "ds_read_b64_tr_b16 v[40:41], v2 offset:1024\n"
    "v_mfma_f32_16x16x32_bf16 v[120:123], v[20:23], v[88:91], v[120:123]\n"
    "v_exp_f32 v100, v76\n"
    "ds_read_b64_tr_b16 v[42:43], v2 offset:5120\n"
    "s_waitcnt lgkmcnt(8)\n"
    "v_mfma_f32_16x16x32_bf16 v[124:127], v[24:27], v[84:87], v[124:127]\n"
    "v_exp_f32 v101, v77\n"
    "ds_read_b64_tr_b16 v[44:45], v1 offset:1536\n"
    "v_mfma_f32_16x16x32_bf16 v[128:131], v[24:27], v[88:91], v[128:131]\n"
    "v_exp_f32 v102, v80\n"
    "ds_read_b64_tr_b16 v[46:47], v1 offset:5632\n"
    "s_waitcnt lgkmcnt(8)\n"
    "v_mfma_f32_16x16x32_bf16 v[132:135], v[28:31], v[84:87], v[132:135]\n"
    "v_exp_f32 v103, v81\n"
    "ds_read_b64_tr_b16 v[48:49], v2 offset:1536\n"
    "v_mfma_f32_16x16x32_bf16 v[136:139], v[28:31], v[88:91], v[136:139]\n"
    "v_cvt_pk_bf16_f32 v97, v106, v107\n"
    "v_cvt_pk_bf16_f32 v94, v100, v101\n"
    "ds_read_b64_tr_b16 v[50:51], v2 offset:5632\n"
    "s_waitcnt lgkmcnt(8)\n"
    "v_mfma_f32_16x16x32_bf16 v[140:143], v[32:35], v[84:87], v[140:143]\n"
    "v_exp_f32 v104, v78\n"
    "ds_read_b64_tr_b16 v[20:21], v1 offset:8192\n"
    "v_mfma_f32_16x16x32_bf16 v[144:147], v[32:35], v[88:91], v[144:147]\n"
    "v_exp_f32 v105, v79\n"
    "ds_read_b64_tr_b16 v[22:23], v1 offset:12288\n"
    "s_waitcnt lgkmcnt(8)\n"
    "v_mfma_f32_16x16x32_bf16 v[148:151], v[36:39], v[84:87], v[148:151]\n"
    "v_exp_f32 v106, v82\n"
    "ds_read_b64_tr_b16 v[24:25], v2 offset:8192\n"
    "v_mfma_f32_16x16x32_bf16 v[152:155], v[36:39], v[88:91], v[152:155]\n"
    "v_cvt_pk_bf16_f32 v98, v102, v103\n"
    "ds_read_b64_tr_b16 v[26:27], v2 offset:12288\n"
    "s_waitcnt lgkmcnt(8)\n"
    "v_mfma_f32_16x16x32_bf16 v[156:159], v[40:43], v[84:87], v[156:159]\n"
    "v_exp_f32 v107, v83\n"
    "ds_read_b64_tr_b16 v[28:29], v1 offset:8704\n"
    "v_mfma_f32_16x16x32_bf16 v[160:163], v[40:43], v[88:91], v[160:163]\n"
    "v_cvt_pk_bf16_f32 v95, v104, v105\n"
    "v_cvt_pk_bf16_f32 v99, v106, v107\n"
    "ds_read_b64_tr_b16 v[30:31], v1 offset:12800\n"
    "s_waitcnt lgkmcnt(8)\n"
    "v_mfma_f32_16x16x32_bf16 v[164:167], v[44:47], v[84:87], v[164:167]\n"
    "v_mfma_f32_16x16x32_bf16 v[224:227], v[212:215], v[92:95], v[216:219]\n"
    "v_mfma_f32_16x16x32_bf16 v[228:231], v[212:215], v[96:99], v[220:223]\n"
    "ds_read_b64_tr_b16 v[32:33], v2 offset:8704\n"
    "v_mfma_f32_16x16x32_bf16 v[168:171], v[44:47], v[88:91], v[168:171]\n"
    "ds_read_b64_tr_b16 v[34:35], v2 offset:12800\n"
    "s_waitcnt lgkmcnt(8)\n"
    "v_mfma_f32_16x16x32_bf16 v[172:175], v[48:51], v[84:87], v[172:175]\n"
    "ds_read_b64_tr_b16 v[36:37], v1 offset:9216\n"
    "v_mfma_f32_16x16x32_bf16 v[176:179], v[48:51], v[88:91], v[176:179]\n"
    "ds_read_b64_tr_b16 v[38:39], v1 offset:13312\n"
    "v_max_f32 v248, v224, v228\n"
    "v_cmp_lt_f32 vcc, s80, v248\n"
    "s_nop 0\n"
    "s_cbranch_vccnz L_rare_la_%=\n"
    "L_ret_la_%=:\n"
    "s_waitcnt lgkmcnt(8)\n"
    "v_mfma_f32_16x16x32_bf16 v[116:119], v[20:23], v[92:95], v[116:119]\n"
    "ds_read_b64_tr_b16 v[40:41], v2 offset:9216\n"
    "v_mfma_f32_16x16x32_bf16 v[120:123], v[20:23], v[96:99], v[120:123]\n"
    "ds_read_b64_tr_b16 v[42:43], v2 offset:13312\n"
    "s_waitcnt lgkmcnt(8)\n"
    "v_mfma_f32_16x16x32_bf16 v[124:127], v[24:27], v[92:95], v[124:127]\n"
    "ds_read_b64_tr_b16 v[44:45], v1 offset:9728\n"
    "v_mfma_f32_16x16x32_bf16 v[128:131], v[24:27], v[96:99], v[128:131]\n"
    "ds_read_b64_tr_b16 v[46:47], v1 offset:13824\n"
    "s_waitcnt lgkmcnt(8)\n"
    "v_mfma_f32_16x16x32_bf16 v[132:135], v[28:31], v[92:95], v[132:135]\n"
    "ds_read_b64_tr_b16 v[48:49], v2 offset:9728\n"
    "v_mfma_f32_16x16x32_bf16 v[136:139], v[28:31], v[96:99], v[136:139]\n"
    "ds_read_b64_tr_b16 v[50:51], v2 offset:13824\n"
    "s_waitcnt lgkmcnt(8)\n"
    "v_mfma_f32_16x16x32_bf16 v[140:143], v[32:35], v[92:95], v[140:143]\n"
    "v_mfma_f32_16x16x32_bf16 v[144:147], v[32:35], v[96:99], v[144:147]\n"
    "s_waitcnt lgkmcnt(6)\n"
    "v_mfma_f32_16x16x32_bf16 v[148:151], v[36:39], v[92:95], v[148:151]\n"
    "v_mfma_f32_16x16x32_bf16 v[152:155], v[36:39], v[96:99], v[152:155]\n"
    "s_cmp_ge_u32 s7, 4\n"
    "s_cbranch_scc1 L_grpB_%=\n"
    "s_waitcnt lgkmcnt(4)\n"
    "v_mfma_f32_16x16x32_bf16 v[156:159], v[40:43], v[92:95], v[156:159]\n"
    "v_mfma_f32_16x16x32_bf16 v[160:163], v[40:43], v[96:99], v[160:163]\n"
    "s_waitcnt lgkmcnt(2)\n"
    "v_mfma_f32_16x16x32_bf16 v[164:167], v[44:47], v[92:95], v[164:167]\n"
    "v_mfma_f32_16x16x32_bf16 v[168:171], v[44:47], v[96:99], v[168:171]\n"
    "s_waitcnt lgkmcnt(0)\n"
    "v_mfma_f32_16x16x32_bf16 v[172:175], v[48:51], v[92:95], v[172:175]\n"
    "v_mfma_f32_16x16x32_bf16 v[176:179], v[48:51], v[96:99], v[176:179]\n"
    "v_rcp_f32 v248, v224\n"
    "v_rcp_f32 v249, v224\n"
    "v_rcp_f32 v250, v228\n"
    "v_rcp_f32 v251, v228\n"
    "s_mov_b32 s69, 132096\n"
    "v_pk_mul_f32 v[116:117], v[116:117], v[248:249]\n"
    "v_pk_mul_f32 v[118:119], v[118:119], v[248:249]\n"
    "v_cvt_pk_bf16_f32 v180, v116, v117\n"
    "v_cvt_pk_bf16_f32 v181, v118, v119\n"
    "v_pk_mul_f32 v[124:125], v[124:125], v[248:249]\n"
    "v_pk_mul_f32 v[126:127], v[126:127], v[248:249]\n"
    "v_cvt_pk_bf16_f32 v182, v124, v125\n"
    "v_cvt_pk_bf16_f32 v183, v126, v127\n"
    "buffer_store_dwordx4 v[180:183], v15, s[32:35], 0 offen offset:0 sc1\n"
    "v_pk_mul_f32 v[120:121], v[120:121], v[250:251]\n"
    "v_pk_mul_f32 v[122:123], v[122:123], v[250:251]\n"
    "v_cvt_pk_bf16_f32 v184, v120, v121\n"
    "v_cvt_pk_bf16_f32 v185, v122, v123\n"
    "v_pk_mul_f32 v[128:129], v[128:129], v[250:251]\n"
    "v_pk_mul_f32 v[130:131], v[130:131], v[250:251]\n"
    "v_cvt_pk_bf16_f32 v186, v128, v129\n"
    "v_cvt_pk_bf16_f32 v187, v130, v131\n"
    "buffer_store_dwordx4 v[184:187], v15, s[32:35], s69 offen offset:0 sc1\n"
    "v_pk_mul_f32 v[132:133], v[132:133], v[248:249]\n"
    "v_pk_mul_f32 v[134:135], v[134:135], v[248:249]\n"
    "v_cvt_pk_bf16_f32 v188, v132, v133\n"
    "v_cvt_pk_bf16_f32 v189, v134, v135\n"
    "v_pk_mul_f32 v[140:141], v[140:141], v[248:249]\n"
    "v_pk_mul_f32 v[142:143], v[142:143], v[248:249]\n"
    "v_cvt_pk_bf16_f32 v190, v140, v141\n"
    "v_cvt_pk_bf16_f32 v191, v142, v143\n"
    "buffer_store_dwordx4 v[188:191], v15, s[32:35], 0 offen offset:1024 sc1\n"
    "v_pk_mul_f32 v[136:137], v[136:137], v[250:251]\n"
    "v_pk_mul_f32 v[138:139], v[138:139], v[250:251]\n"
    "v_cvt_pk_bf16_f32 v192, v136, v137\n"
    "v_cvt_pk_bf16_f32 v193, v138, v139\n"
    "v_pk_mul_f32 v[144:145], v[144:145], v[250:251]\n"
    "v_pk_mul_f32 v[146:147], v[146:147], v[250:251]\n"
    "v_cvt_pk_bf16_f32 v194, v144, v145\n"
    "v_cvt_pk_bf16_f32 v195, v146, v147\n"
    "buffer_store_dwordx4 v[192:195], v15, s[32:35], s69 offen offset:1024 sc1\n"
    "v_pk_mul_f32 v[148:149], v[148:149], v[248:249]\n"
    "v_pk_mul_f32 v[150:151], v[150:151], v[248:249]\n"
    "v_cvt_pk_bf16_f32 v196, v148, v149\n"
    "v_cvt_pk_bf16_f32 v197, v150, v151\n"
    "v_pk_mul_f32 v[156:157], v[156:157], v[248:249]\n"
    "v_pk_mul_f32 v[158:159], v[158:159], v[248:249]\n"
    "v_cvt_pk_bf16_f32 v198, v156, v157\n"
    "v_cvt_pk_bf16_f32 v199, v158, v159\n"
    "buffer_store_dwordx4 v[196:199], v15, s[32:35], 0 offen offset:2048 sc1\n"
    "v_pk_mul_f32 v[152:153], v[152:153], v[250:251]\n"
    "v_pk_mul_f32 v[154:155], v[154:155], v[250:251]\n"
    "v_cvt_pk_bf16_f32 v200, v152, v153\n"
    "v_cvt_pk_bf16_f32 v201, v154, v155\n"
    "v_pk_mul_f32 v[160:161], v[160:161], v[250:251]\n"
    "v_pk_mul_f32 v[162:163], v[162:163], v[250:251]\n"
    "v_cvt_pk_bf16_f32 v202, v160, v161\n"
    "v_cvt_pk_bf16_f32 v203, v162, v163\n"
    "buffer_store_dwordx4 v[200:203], v15, s[32:35], s69 offen offset:2048 sc1\n"
    "v_pk_mul_f32 v[164:165], v[164:165], v[248:249]\n"
    "v_pk_mul_f32 v[166:167], v[166:167], v[248:249]\n"
    "v_cvt_pk_bf16_f32 v204, v164, v165\n"
    "v_cvt_pk_bf16_f32 v205, v166, v167\n"
    "v_pk_mul_f32 v[172:173], v[172:173], v[248:249]\n"
    "v_pk_mul_f32 v[174:175], v[174:175], v[248:249]\n"
    "v_cvt_pk_bf16_f32 v206, v172, v173\n"
    "v_cvt_pk_bf16_f32 v207, v174, v175\n"
    "buffer_store_dwordx4 v[204:207], v15, s[32:35], 0 offen offset:3072 sc1\n"
    "v_pk_mul_f32 v[168:169], v[168:169], v[250:251]\n"
    "v_pk_mul_f32 v[170:171], v[170:171], v[250:251]\n"
    "v_cvt_pk_bf16_f32 v208, v168, v169\n"
    "v_cvt_pk_bf16_f32 v209, v170, v171\n"
    "v_pk_mul_f32 v[176:177], v[176:177], v[250:251]\n"
    "v_pk_mul_f32 v[178:179], v[178:179], v[250:251]\n"
    "v_cvt_pk_bf16_f32 v210, v176, v177\n"
    "v_cvt_pk_bf16_f32 v211, v178, v179\n"
    "buffer_store_dwordx4 v[208:211], v15, s[32:35], s69 offen offset:3072 sc1\n"
    "s_setprio 0\n"
    "ds_read_b64_tr_b16 v[20:21], v240 offset:0\n"
    "ds_read_b64_tr_b16 v[22:23], v240 offset:4096\n"
    "ds_read_b128 v[52:55], v14 offset:0\n"
    "ds_read_b128 v[56:59], v14 offset:1024\n"
    "ds_read_b64_tr_b16 v[24:25], v241 offset:0\n"
    "ds_read_b64_tr_b16 v[26:27], v241 offset:4096\n"
    "ds_read_b64_tr_b16 v[28:29], v242 offset:0\n"
    "ds_read_b64_tr_b16 v[30:31], v242 offset:4096\n"
    "ds_read_b64_tr_b16 v[32:33], v243 offset:0\n"
    "ds_read_b64_tr_b16 v[34:35], v243 offset:4096\n"
    "ds_read_b64_tr_b16 v[36:37], v240 offset:16384\n"
    "ds_read_b64_tr_b16 v[38:39], v240 offset:20480\n"
    "ds_read_b128 v[60:63], v14 offset:16384\n"
    "ds_read_b128 v[64:67], v14 offset:17408\n"
    "s_waitcnt lgkmcnt(10)\n"
    "v_swap_b32 v54, v56\n"
    "v_swap_b32 v55, v57\n"
    "s_nop 1\n"
    "v_mfma_f32_16x16x32_bf16 v[84:87], v[20:23], v[52:55], 0\n"
    "ds_read_b64_tr_b16 v[40:41], v241 offset:16384\n"
    "v_mfma_f32_16x16x32_bf16 v[88:91], v[20:23], v[56:59], 0\n"
    "ds_read_b64_tr_b16 v[42:43], v241 offset:20480\n"
    "s_waitcnt lgkmcnt(10)\n"
    "v_mfma_f32_16x16x32_bf16 v[92:95], v[24:27], v[52:55], 0\n"
    "ds_read_b64_tr_b16 v[44:45], v242 offset:16384\n"
    "v_mfma_f32_16x16x32_bf16 v[96:99], v[24:27], v[56:59], 0\n"
    "ds_read_b64_tr_b16 v[46:47], v242 offset:20480\n"
    "s_waitcnt lgkmcnt(10)\n"
    "v_mfma_f32_16x16x32_bf16 v[100:103], v[28:31], v[52:55], 0\n"
    "ds_read_b64_tr_b16 v[48:49], v243 offset:16384\n"
    "v_mfma_f32_16x16x32_bf16 v[104:107], v[28:31], v[56:59], 0\n"
    "ds_read_b64_tr_b16 v[50:51], v243 offset:20480\n"
    "s_waitcnt lgkmcnt(10)\n"
    "v_mfma_f32_16x16x32_bf16 v[108:111], v[32:35], v[52:55], 0\n"
    "ds_read_b128 v[68:71], v14 offset:32768\n"
    "ds_read_b64_tr_b16 v[20:21], v240 offset:32768\n"
    "v_mfma_f32_16x16x32_bf16 v[112:115], v[32:35], v[56:59], 0\n"
    "ds_read_b128 v[72:75], v14 offset:33792\n"
    "ds_read_b64_tr_b16 v[22:23], v240 offset:36864\n"
    "s_waitcnt lgkmcnt(10)\n"
    "v_swap_b32 v62, v64\n"
    "v_swap_b32 v63, v65\n"
    "s_nop 1\n"
    "v_mfma_f32_16x16x32_bf16 v[84:87], v[36:39], v[60:63], v[84:87]\n"
    "ds_read_b64_tr_b16 v[24:25], v241 offset:32768\n"
    "v_mfma_f32_16x16x32_bf16 v[88:91], v[36:39], v[64:67], v[88:91]\n"
    "ds_read_b64_tr_b16 v[26:27], v241 offset:36864\n"
    "s_waitcnt lgkmcnt(10)\n"
    "v_mfma_f32_16x16x32_bf16 v[92:95], v[40:43], v[60:63], v[92:95]\n"
    "ds_read_b64_tr_b16 v[28:29], v242 offset:32768\n"
    "v_mfma_f32_16x16x32_bf16 v[96:99], v[40:43], v[64:67], v[96:99]\n"
    "ds_read_b64_tr_b16 v[30:31], v242 offset:36864\n"
    "s_waitcnt lgkmcnt(10)\n"
    "v_mfma_f32_16x16x32_bf16 v[100:103], v[44:47], v[60:63], v[100:103]\n"
    "ds_read_b64_tr_b16 v[32:33], v243 offset:32768\n"
    "v_mfma_f32_16x16x32_bf16 v[104:107], v[44:47], v[64:67], v[104:107]\n"
    "ds_read_b64_tr_b16 v[34:35], v243 offset:36864\n"
    "s_waitcnt lgkmcnt(10)\n"
    "v_mfma_f32_16x16x32_bf16 v[108:111], v[48:51], v[60:63], v[108:111]\n"
    "ds_read_b128 v[52:55], v14 offset:49152\n"
    "ds_read_b64_tr_b16 v[36:37], v240 offset:49152\n"
    "v_mfma_f32_16x16x32_bf16 v[112:115], v[48:51], v[64:67], v[112:115]\n"
    "ds_read_b128 v[56:59], v14 offset:50176\n"
    "ds_read_b64_tr_b16 v[38:39], v240 offset:53248\n"
    "s_waitcnt lgkmcnt(11)\n"
    "v_swap_b32 v70, v72\n"
    "v_swap_b32 v71, v73\n"
    "s_waitcnt lgkmcnt(10)\n"
    "s_nop 0\n"
    "v_mfma_f32_16x16x32_bf16 v[84:87], v[20:23], v[68:71], v[84:87]\n"
    "ds_read_b64_tr_b16 v[40:41], v241 offset:49152\n"
    "v_mfma_f32_16x16x32_bf16 v[88:91], v[20:23], v[72:75], v[88:91]\n"
    "ds_read_b64_tr_b16 v[42:43], v241 offset:53248\n"
    "s_waitcnt lgkmcnt(10)\n"
    "v_mfma_f32_16x16x32_bf16 v[92:95], v[24:27], v[68:71], v[92:95]\n"
    "ds_read_b64_tr_b16 v[44:45], v242 offset:49152\n"
    "v_mfma_f32_16x16x32_bf16 v[96:99], v[24:27], v[72:75], v[96:99]\n"
    "ds_read_b64_tr_b16 v[46:47], v242 offset:53248\n"
    "s_waitcnt lgkmcnt(10)\n"
    "v_mfma_f32_16x16x32_bf16 v[100:103], v[28:31], v[68:71], v[100:103]\n"
    "ds_read_b64_tr_b16 v[48:49], v243 offset:49152\n"
    "v_mfma_f32_16x16x32_bf16 v[104:107], v[28:31], v[72:75], v[104:107]\n"
    "ds_read_b64_tr_b16 v[50:51], v243 offset:53248\n"
    "s_waitcnt lgkmcnt(10)\n"
    "v_mfma_f32_16x16x32_bf16 v[108:111], v[32:35], v[68:71], v[108:111]\n"
    "v_mfma_f32_16x16x32_bf16 v[112:115], v[32:35], v[72:75], v[112:115]\n"
    "s_waitcnt lgkmcnt(7)\n"
    "v_swap_b32 v54, v56\n"
    "v_swap_b32 v55, v57\n"
    "s_waitcnt lgkmcnt(6)\n"
    "s_nop 0\n"
    "v_mfma_f32_16x16x32_bf16 v[84:87], v[36:39], v[52:55], v[84:87]\n"
    "v_mfma_f32_16x16x32_bf16 v[88:91], v[36:39], v[56:59], v[88:91]\n"
    "s_waitcnt lgkmcnt(4)\n"
    "v_mfma_f32_16x16x32_bf16 v[92:95], v[40:43], v[52:55], v[92:95]\n"
    "v_mfma_f32_16x16x32_bf16 v[96:99], v[40:43], v[56:59], v[96:99]\n"
    "s_waitcnt lgkmcnt(2)\n"
    "v_mfma_f32_16x16x32_bf16 v[100:103], v[44:47], v[52:55], v[100:103]\n"
    "v_mfma_f32_16x16x32_bf16 v[104:107], v[44:47], v[56:59], v[104:107]\n"
    "s_waitcnt lgkmcnt(0)\n"
    "v_mfma_f32_16x16x32_bf16 v[108:111], v[48:51], v[52:55], v[108:111]\n"
    "v_mfma_f32_16x16x32_bf16 v[112:115], v[48:51], v[56:59], v[112:115]\n"
    "s_waitcnt vmcnt(8)\n"
    "s_waitcnt lgkmcnt(0)\n"
    "s_barrier\n"
    "s_add_u32 m0, s75, 49152\n"
    "s_nop 0\n"
    "buffer_load_dwordx4 v10, s[36:39], s79 offen sc1 lds\n"
    "s_add_u32 s62, s79, s71\n"
    "s_add_u32 m0, s75, 50176\n"
    "s_nop 0\n"
    "buffer_load_dwordx4 v10, s[36:39], s62 offen sc1 lds\n"
    "s_add_u32 s79, s79, s74\n"
    "s_add_u32 m0, s75, 65536\n"
    "s_nop 0\n"
    "buffer_load_dwordx4 v10, s[36:39], s79 offen sc1 lds\n"
    "s_add_u32 s62, s79, s71\n"
    "s_add_u32 m0, s75, 66560\n"
    "s_nop 0\n"
    "buffer_load_dwordx4 v10, s[36:39], s62 offen sc1 lds\n"
    "s_add_u32 s79, s79, s74\n"
    "s_add_u32 m0, s75, 81920\n"
    "s_nop 0\n"
    "buffer_load_dwordx4 v10, s[36:39], s79 offen sc1 lds\n"
    "s_add_u32 s62, s79, s71\n"
    "s_add_u32 m0, s75, 82944\n"
    "s_nop 0\n"
    "buffer_load_dwordx4 v10, s[36:39], s62 offen sc1 lds\n"
    "s_add_u32 s79, s79, s74\n"
    "v_subrev_u32 v240, 98304, v240\n"
    "v_subrev_u32 v241, 98304, v241\n"
    "v_subrev_u32 v242, 98304, v242\n"
    "v_subrev_u32 v243, 98304, v243\n"
    "v_subrev_u32 v14, 98304, v14\n"
    "ds_read_b64_tr_b16 v[20:21], v240 offset:0\n"
    "ds_read_b64_tr_b16 v[22:23], v240 offset:4096\n"
    "ds_read_b128 v[60:63], v14 offset:0\n"
    "ds_read_b128 v[64:67], v14 offset:1024\n"
    "ds_read_b64_tr_b16 v[24:25], v241 offset:0\n"
    "ds_read_b64_tr_b16 v[26:27], v241 offset:4096\n"
    "ds_read_b64_tr_b16 v[28:29], v242 offset:0\n"
    "ds_read_b64_tr_b16 v[30:31], v242 offset:4096\n"
    "ds_read_b64_tr_b16 v[32:33], v243 offset:0\n"
    "ds_read_b64_tr_b16 v[34:35], v243 offset:4096\n"
    "ds_read_b64_tr_b16 v[36:37], v240 offset:16384\n"
    "ds_read_b64_tr_b16 v[38:39], v240 offset:20480\n"
    "ds_read_b128 v[68:71], v14 offset:16384\n"
    "ds_read_b128 v[72:75], v14 offset:17408\n"
    "s_waitcnt lgkmcnt(10)\n"
    "v_swap_b32 v62, v64\n"
    "v_swap_b32 v63, v65\n"
    "s_nop 1\n"
    "v_mfma_f32_16x16x32_bf16 v[84:87], v[20:23], v[60:63], v[84:87]\n"
    "ds_read_b64_tr_b16 v[40:41], v241 offset:16384\n"
    "v_mfma_f32_16x16x32_bf16 v[88:91], v[20:23], v[64:67], v[88:91]\n"
    "ds_read_b64_tr_b16 v[42:43], v241 offset:20480\n"
    "s_waitcnt lgkmcnt(10)\n"
    "v_mfma_f32_16x16x32_bf16 v[92:95], v[24:27], v[60:63], v[92:95]\n"
    "ds_read_b64_tr_b16 v[44:45], v242 offset:16384\n"
    "v_mfma_f32_16x16x32_bf16 v[96:99], v[24:27], v[64:67], v[96:99]\n"
    "ds_read_b64_tr_b16 v[46:47], v242 offset:20480\n"
    "s_waitcnt lgkmcnt(10)\n"
    "v_mfma_f32_16x16x32_bf16 v[100:103], v[28:31], v[60:63], v[100:103]\n"
    "ds_read_b64_tr_b16 v[48:49], v243 offset:16384\n"
    "v_mfma_f32_16x16x32_bf16 v[104:107], v[28:31], v[64:67], v[104:107]\n"
    "ds_read_b64_tr_b16 v[50:51], v243 offset:20480\n"
    "s_waitcnt lgkmcnt(10)\n"
    "v_mfma_f32_16x16x32_bf16 v[108:111], v[32:35], v[60:63], v[108:111]\n"
    "ds_read_b128 v[52:55], v14 offset:32768\n"
    "ds_read_b64_tr_b16 v[20:21], v240 offset:32768\n"
    "v_mfma_f32_16x16x32_bf16 v[112:115], v[32:35], v[64:67], v[112:115]\n"
    "ds_read_b128 v[56:59], v14 offset:33792\n"
    "ds_read_b64_tr_b16 v[22:23], v240 offset:36864\n"
    "s_waitcnt lgkmcnt(10)\n"
    "v_swap_b32 v70, v72\n"
    "v_swap_b32 v71, v73\n"
    "s_nop 1\n"
    "v_mfma_f32_16x16x32_bf16 v[84:87], v[36:39], v[68:71], v[84:87]\n"
    "ds_read_b64_tr_b16 v[24:25], v241 offset:32768\n"
    "v_mfma_f32_16x16x32_bf16 v[88:91], v[36:39], v[72:75], v[88:91]\n"
    "ds_read_b64_tr_b16 v[26:27], v241 offset:36864\n"
    "s_waitcnt lgkmcnt(10)\n"
    "v_mfma_f32_16x16x32_bf16 v[92:95], v[40:43], v[68:71], v[92:95]\n"
    "ds_read_b64_tr_b16 v[28:29], v242 offset:32768\n"
    "v_mfma_f32_16x16x32_bf16 v[96:99], v[40:43], v[72:75], v[96:99]\n"
    "ds_read_b64_tr_b16 v[30:31], v242 offset:36864\n"
    "s_waitcnt lgkmcnt(10)\n"
    "v_mfma_f32_16x16x32_bf16 v[100:103], v[44:47], v[68:71], v[100:103]\n"
    "ds_read_b64_tr_b16 v[32:33], v243 offset:32768\n"
    "v_mfma_f32_16x16x32_bf16 v[104:107], v[44:47], v[72:75], v[104:107]\n"
    "ds_read_b64_tr_b16 v[34:35], v243 offset:36864\n"
    "s_waitcnt lgkmcnt(10)\n"
    "v_mfma_f32_16x16x32_bf16 v[108:111], v[48:51], v[68:71], v[108:111]\n"
    "v_mfma_f32_16x16x32_bf16 v[112:115], v[48:51], v[72:75], v[112:115]\n"
    "s_waitcnt lgkmcnt(7)\n"
    "v_swap_b32 v54, v56\n"
    "v_swap_b32 v55, v57\n"
    "s_waitcnt lgkmcnt(6)\n"
    "s_nop 0\n"
    "v_mfma_f32_16x16x32_bf16 v[84:87], v[20:23], v[52:55], v[84:87]\n"
    "v_mfma_f32_16x16x32_bf16 v[88:91], v[20:23], v[56:59], v[88:91]\n"
    "s_waitcnt lgkmcnt(4)\n"
    "v_mfma_f32_16x16x32_bf16 v[92:95], v[24:27], v[52:55], v[92:95]\n"
    "v_mfma_f32_16x16x32_bf16 v[96:99], v[24:27], v[56:59], v[96:99]\n"
    "s_waitcnt lgkmcnt(2)\n"
    "v_mfma_f32_16x16x32_bf16 v[100:103], v[28:31], v[52:55], v[100:103]\n"
    "v_mfma_f32_16x16x32_bf16 v[104:107], v[28:31], v[56:59], v[104:107]\n"
    "s_waitcnt lgkmcnt(0)\n"
    "v_mfma_f32_16x16x32_bf16 v[108:111], v[32:35], v[52:55], v[108:111]\n"
    "v_mfma_f32_16x16x32_bf16 v[112:115], v[32:35], v[56:59], v[112:115]\n"
    "s_branch L_join_%=\n"
    "L_grpB_%=:\n"
    "s_waitcnt vmcnt(0)\n"
    "s_waitcnt lgkmcnt(0)\n"
    "s_barrier\n"
    "s_add_u32 m0, s75, 49152\n"
    "s_nop 0\n"
    "buffer_load_dwordx4 v10, s[36:39], s79 offen sc1 lds\n"
    "s_add_u32 s62, s79, s71\n"
    "s_add_u32 m0, s75, 50176\n"
    "s_nop 0\n"
    "buffer_load_dwordx4 v10, s[36:39], s62 offen sc1 lds\n"
    "s_add_u32 s79, s79, s74\n"
    "s_add_u32 m0, s75, 65536\n"
    "s_nop 0\n"
    "buffer_load_dwordx4 v10, s[36:39], s79 offen sc1 lds\n"
    "s_add_u32 s62, s79, s71\n"
    "s_add_u32 m0, s75, 66560\n"
    "s_nop 0\n"
    "buffer_load_dwordx4 v10, s[36:39], s62 offen sc1 lds\n"
    "s_add_u32 s79, s79, s74\n"
    "s_add_u32 m0, s75, 81920\n"
    "s_nop 0\n"
    "buffer_load_dwordx4 v10, s[36:39], s79 offen sc1 lds\n"
    "s_add_u32 s62, s79, s71\n"
    "s_add_u32 m0, s75, 82944\n"
    "s_nop 0\n"
    "buffer_load_dwordx4 v10, s[36:39], s62 offen sc1 lds\n"
    "s_add_u32 s79, s79, s74\n"
    "v_mfma_f32_16x16x32_bf16 v[156:159], v[40:43], v[92:95], v[156:159]\n"
    "v_mfma_f32_16x16x32_bf16 v[160:163], v[40:43], v[96:99], v[160:163]\n"
    "v_mfma_f32_16x16x32_bf16 v[164:167], v[44:47], v[92:95], v[164:167]\n"
    "v_mfma_f32_16x16x32_bf16 v[168:171], v[44:47], v[96:99], v[168:171]\n"
    "v_mfma_f32_16x16x32_bf16 v[172:175], v[48:51], v[92:95], v[172:175]\n"
    "v_mfma_f32_16x16x32_bf16 v[176:179], v[48:51], v[96:99], v[176:179]\n"
    "v_rcp_f32 v248, v224\n"
    "v_rcp_f32 v249, v224\n"
    "v_rcp_f32 v250, v228\n"
    "v_rcp_f32 v251, v228\n"
    "s_mov_b32 s69, 132096\n"
    "v_pk_mul_f32 v[116:117], v[116:117], v[248:249]\n"
    "v_pk_mul_f32 v[118:119], v[118:119], v[248:249]\n"
    "v_cvt_pk_bf16_f32 v180, v116, v117\n"
    "v_cvt_pk_bf16_f32 v181, v118, v119\n"
    "v_pk_mul_f32 v[124:125], v[124:125], v[248:249]\n"
    "v_pk_mul_f32 v[126:127], v[126:127], v[248:249]\n"
    "v_cvt_pk_bf16_f32 v182, v124, v125\n"
    "v_cvt_pk_bf16_f32 v183, v126, v127\n"
    "buffer_store_dwordx4 v[180:183], v15, s[32:35], 0 offen offset:0 sc1\n"
    "v_pk_mul_f32 v[120:121], v[120:121], v[250:251]\n"
    "v_pk_mul_f32 v[122:123], v[122:123], v[250:251]\n"
    "v_cvt_pk_bf16_f32 v184, v120, v121\n"
    "v_cvt_pk_bf16_f32 v185, v122, v123\n"
    "v_pk_mul_f32 v[128:129], v[128:129], v[250:251]\n"
    "v_pk_mul_f32 v[130:131], v[130:131], v[250:251]\n"
    "v_cvt_pk_bf16_f32 v186, v128, v129\n"
    "v_cvt_pk_bf16_f32 v187, v130, v131\n"
    "buffer_store_dwordx4 v[184:187], v15, s[32:35], s69 offen offset:0 sc1\n"
    "v_pk_mul_f32 v[132:133], v[132:133], v[248:249]\n"
    "v_pk_mul_f32 v[134:135], v[134:135], v[248:249]\n"
    "v_cvt_pk_bf16_f32 v188, v132, v133\n"
    "v_cvt_pk_bf16_f32 v189, v134, v135\n"
    "v_pk_mul_f32 v[140:141], v[140:141], v[248:249]\n"
    "v_pk_mul_f32 v[142:143], v[142:143], v[248:249]\n"
    "v_cvt_pk_bf16_f32 v190, v140, v141\n"
    "v_cvt_pk_bf16_f32 v191, v142, v143\n"
    "buffer_store_dwordx4 v[188:191], v15, s[32:35], 0 offen offset:1024 sc1\n"
    "v_pk_mul_f32 v[136:137], v[136:137], v[250:251]\n"
    "v_pk_mul_f32 v[138:139], v[138:139], v[250:251]\n"
    "v_cvt_pk_bf16_f32 v192, v136, v137\n"
    "v_cvt_pk_bf16_f32 v193, v138, v139\n"
    "v_pk_mul_f32 v[144:145], v[144:145], v[250:251]\n"
    "v_pk_mul_f32 v[146:147], v[146:147], v[250:251]\n"
    "v_cvt_pk_bf16_f32 v194, v144, v145\n"
    "v_cvt_pk_bf16_f32 v195, v146, v147\n"
    "buffer_store_dwordx4 v[192:195], v15, s[32:35], s69 offen offset:1024 sc1\n"
    "v_pk_mul_f32 v[148:149], v[148:149], v[248:249]\n"
    "v_pk_mul_f32 v[150:151], v[150:151], v[248:249]\n"
    "v_cvt_pk_bf16_f32 v196, v148, v149\n"
    "v_cvt_pk_bf16_f32 v197, v150, v151\n"
    "v_pk_mul_f32 v[156:157], v[156:157], v[248:249]\n"
    "v_pk_mul_f32 v[158:159], v[158:159], v[248:249]\n"
    "v_cvt_pk_bf16_f32 v198, v156, v157\n"
    "v_cvt_pk_bf16_f32 v199, v158, v159\n"
    "buffer_store_dwordx4 v[196:199], v15, s[32:35], 0 offen offset:2048 sc1\n"
    "v_pk_mul_f32 v[152:153], v[152:153], v[250:251]\n"
    "v_pk_mul_f32 v[154:155], v[154:155], v[250:251]\n"
    "v_cvt_pk_bf16_f32 v200, v152, v153\n"
    "v_cvt_pk_bf16_f32 v201, v154, v155\n"
    "v_pk_mul_f32 v[160:161], v[160:161], v[250:251]\n"
    "v_pk_mul_f32 v[162:163], v[162:163], v[250:251]\n"
    "v_cvt_pk_bf16_f32 v202, v160, v161\n"
    "v_cvt_pk_bf16_f32 v203, v162, v163\n"
    "buffer_store_dwordx4 v[200:203], v15, s[32:35], s69 offen offset:2048 sc1\n"
    "v_pk_mul_f32 v[164:165], v[164:165], v[248:249]\n"
    "v_pk_mul_f32 v[166:167], v[166:167], v[248:249]\n"
    "v_cvt_pk_bf16_f32 v204, v164, v165\n"
    "v_cvt_pk_bf16_f32 v205, v166, v167\n"
    "v_pk_mul_f32 v[172:173], v[172:173], v[248:249]\n"
    "v_pk_mul_f32 v[174:175], v[174:175], v[248:249]\n"
    "v_cvt_pk_bf16_f32 v206, v172, v173\n"
    "v_cvt_pk_bf16_f32 v207, v174, v175\n"
    "buffer_store_dwordx4 v[204:207], v15, s[32:35], 0 offen offset:3072 sc1\n"
    "v_pk_mul_f32 v[168:169], v[168:169], v[250:251]\n"
    "v_pk_mul_f32 v[170:171], v[170:171], v[250:251]\n"
    "v_cvt_pk_bf16_f32 v208, v168, v169\n"
    "v_cvt_pk_bf16_f32 v209, v170, v171\n"
    "v_pk_mul_f32 v[176:177], v[176:177], v[250:251]\n"
    "v_pk_mul_f32 v[178:179], v[178:179], v[250:251]\n"
    "v_cvt_pk_bf16_f32 v210, v176, v177\n"
    "v_cvt_pk_bf16_f32 v211, v178, v179\n"
    "buffer_store_dwordx4 v[208:211], v15, s[32:35], s69 offen offset:3072 sc1\n"
    "ds_read_b64_tr_b16 v[20:21], v240 offset:0\n"
    "ds_read_b64_tr_b16 v[22:23], v240 offset:4096\n"
    "ds_read_b128 v[52:55], v14 offset:0\n"
    "ds_read_b128 v[56:59], v14 offset:1024\n"
    "ds_read_b64_tr_b16 v[24:25], v241 offset:0\n"
    "ds_read_b64_tr_b16 v[26:27], v241 offset:4096\n"
    "ds_read_b64_tr_b16 v[28:29], v242 offset:0\n"
    "ds_read_b64_tr_b16 v[30:31], v242 offset:4096\n"
    "ds_read_b64_tr_b16 v[32:33], v243 offset:0\n"
    "ds_read_b64_tr_b16 v[34:35], v243 offset:4096\n"
    "ds_read_b64_tr_b16 v[36:37], v240 offset:16384\n"
    "ds_read_b64_tr_b16 v[38:39], v240 offset:20480\n"
    "ds_read_b128 v[60:63], v14 offset:16384\n"
    "ds_read_b128 v[64:67], v14 offset:17408\n"
    "s_waitcnt lgkmcnt(10)\n"
    "v_swap_b32 v54, v56\n"
    "v_swap_b32 v55, v57\n"
    "s_nop 1\n"
    "v_mfma_f32_16x16x32_bf16 v[84:87], v[20:23], v[52:55], 0\n"
    "ds_read_b64_tr_b16 v[40:41], v241 offset:16384\n"
    "v_mfma_f32_16x16x32_bf16 v[88:91], v[20:23], v[56:59], 0\n"
    "ds_read_b64_tr_b16 v[42:43], v241 offset:20480\n"
    "s_waitcnt lgkmcnt(10)\n"
    "v_mfma_f32_16x16x32_bf16 v[92:95], v[24:27], v[52:55], 0\n"
    "ds_read_b64_tr_b16 v[44:45], v242 offset:16384\n"
    "v_mfma_f32_16x16x32_bf16 v[96:99], v[24:27], v[56:59], 0\n"
    "ds_read_b64_tr_b16 v[46:47], v242 offset:20480\n"
    "s_waitcnt lgkmcnt(10)\n"
    "v_mfma_f32_16x16x32_bf16 v[100:103], v[28:31], v[52:55], 0\n"
    "ds_read_b64_tr_b16 v[48:49], v243 offset:16384\n"
    "v_mfma_f32_16x16x32_bf16 v[104:107], v[28:31], v[56:59], 0\n"
    "ds_read_b64_tr_b16 v[50:51], v243 offset:20480\n"
    "s_waitcnt lgkmcnt(10)\n"
    "v_mfma_f32_16x16x32_bf16 v[108:111], v[32:35], v[52:55], 0\n"
    "ds_read_b128 v[68:71], v14 offset:32768\n"
    "ds_read_b64_tr_b16 v[20:21], v240 offset:32768\n"
    "v_mfma_f32_16x16x32_bf16 v[112:115], v[32:35], v[56:59], 0\n"
    "ds_read_b128 v[72:75], v14 offset:33792\n"
    "ds_read_b64_tr_b16 v[22:23], v240 offset:36864\n"
    "s_waitcnt lgkmcnt(10)\n"
    "v_swap_b32 v62, v64\n"
    "v_swap_b32 v63, v65\n"
    "s_nop 1\n"
    "v_mfma_f32_16x16x32_bf16 v[84:87], v[36:39], v[60:63], v[84:87]\n"
    "ds_read_b64_tr_b16 v[24:25], v241 offset:32768\n"
    "v_mfma_f32_16x16x32_bf16 v[88:91], v[36:39], v[64:67], v[88:91]\n"
    "ds_read_b64_tr_b16 v[26:27], v241 offset:36864\n"
    "s_waitcnt lgkmcnt(10)\n"
    "v_mfma_f32_16x16x32_bf16 v[92:95], v[40:43], v[60:63], v[92:95]\n"
    "ds_read_b64_tr_b16 v[28:29], v242 offset:32768\n"
    "v_mfma_f32_16x16x32_bf16 v[96:99], v[40:43], v[64:67], v[96:99]\n"
    "ds_read_b64_tr_b16 v[30:31], v242 offset:36864\n"
    "s_waitcnt lgkmcnt(10)\n"
    "v_mfma_f32_16x16x32_bf16 v[100:103], v[44:47], v[60:63], v[100:103]\n"
    "ds_read_b64_tr_b16 v[32:33], v243 offset:32768\n"
    "v_mfma_f32_16x16x32_bf16 v[104:107], v[44:47], v[64:67], v[104:107]\n"
    "ds_read_b64_tr_b16 v[34:35], v243 offset:36864\n"
    "s_waitcnt lgkmcnt(10)\n"
    "v_mfma_f32_16x16x32_bf16 v[108:111], v[48:51], v[60:63], v[108:111]\n"
    "ds_read_b128 v[52:55], v14 offset:49152\n"
    "ds_read_b64_tr_b16 v[36:37], v240 offset:49152\n"
    "v_mfma_f32_16x16x32_bf16 v[112:115], v[48:51], v[64:67], v[112:115]\n"
    "ds_read_b128 v[56:59], v14 offset:50176\n"
    "ds_read_b64_tr_b16 v[38:39], v240 offset:53248\n"
    "s_waitcnt lgkmcnt(11)\n"
    "v_swap_b32 v70, v72\n"
    "v_swap_b32 v71, v73\n"
    "s_waitcnt lgkmcnt(10)\n"
    "s_nop 0\n"
    "v_mfma_f32_16x16x32_bf16 v[84:87], v[20:23], v[68:71], v[84:87]\n"
    "ds_read_b64_tr_b16 v[40:41], v241 offset:49152\n"
    "v_mfma_f32_16x16x32_bf16 v[88:91], v[20:23], v[72:75], v[88:91]\n"
    "ds_read_b64_tr_b16 v[42:43], v241 offset:53248\n"
    "s_waitcnt lgkmcnt(10)\n"
    "v_mfma_f32_16x16x32_bf16 v[92:95], v[24:27], v[68:71], v[92:95]\n"
    "ds_read_b64_tr_b16 v[44:45], v242 offset:49152\n"
    "v_mfma_f32_16x16x32_bf16 v[96:99], v[24:27], v[72:75], v[96:99]\n"
    "ds_read_b64_tr_b16 v[46:47], v242 offset:53248\n"
    "s_waitcnt lgkmcnt(10)\n"
    "v_mfma_f32_16x16x32_bf16 v[100:103], v[28:31], v[68:71], v[100:103]\n"
    "ds_read_b64_tr_b16 v[48:49], v243 offset:49152\n"
    "v_mfma_f32_16x16x32_bf16 v[104:107], v[28:31], v[72:75], v[104:107]\n"
    "ds_read_b64_tr_b16 v[50:51], v243 offset:53248\n"
    "v_subrev_u32 v240, 98304, v240\n"
    "v_subrev_u32 v241, 98304, v241\n"
    "v_subrev_u32 v242, 98304, v242\n"
    "v_subrev_u32 v243, 98304, v243\n"
    "v_subrev_u32 v14, 98304, v14\n"
    "s_waitcnt lgkmcnt(10)\n"
    "v_mfma_f32_16x16x32_bf16 v[108:111], v[32:35], v[68:71], v[108:111]\n"
    "ds_read_b128 v[60:63], v14 offset:0\n"
    "ds_read_b64_tr_b16 v[20:21], v240 offset:0\n"
    "v_mfma_f32_16x16x32_bf16 v[112:115], v[32:35], v[72:75], v[112:115]\n"
    "ds_read_b128 v[64:67], v14 offset:1024\n"
    "ds_read_b64_tr_b16 v[22:23], v240 offset:4096\n"
    "s_waitcnt lgkmcnt(11)\n"
    "v_swap_b32 v54, v56\n"
    "v_swap_b32 v55, v57\n"
    "s_waitcnt lgkmcnt(10)\n"
    "s_nop 0\n"
    "v_mfma_f32_16x16x32_bf16 v[84:87], v[36:39], v[52:55], v[84:87]\n"
    "ds_read_b64_tr_b16 v[24:25], v241 offset:0\n"
    "v_mfma_f32_16x16x32_bf16 v[88:91], v[36:39], v[56:59], v[88:91]\n"
    "ds_read_b64_tr_b16 v[26:27], v241 offset:4096\n"
    "s_waitcnt lgkmcnt(10)\n"
    "v_mfma_f32_16x16x32_bf16 v[92:95], v[40:43], v[52:55], v[92:95]\n"
    "ds_read_b64_tr_b16 v[28:29], v242 offset:0\n"
    "v_mfma_f32_16x16x32_bf16 v[96:99], v[40:43], v[56:59], v[96:99]\n"
    "ds_read_b64_tr_b16 v[30:31], v242 offset:4096\n"
    "s_waitcnt lgkmcnt(10)\n"
    "v_mfma_f32_16x16x32_bf16 v[100:103], v[44:47], v[52:55], v[100:103]\n"
    "ds_read_b64_tr_b16 v[32:33], v243 offset:0\n"
    "v_mfma_f32_16x16x32_bf16 v[104:107], v[44:47], v[56:59], v[104:107]\n"
    "ds_read_b64_tr_b16 v[34:35], v243 offset:4096\n"
    "s_waitcnt lgkmcnt(10)\n"
    "v_mfma_f32_16x16x32_bf16 v[108:111], v[48:51], v[52:55], v[108:111]\n"
    "ds_read_b128 v[68:71], v14 offset:16384\n"
    "ds_read_b64_tr_b16 v[36:37], v240 offset:16384\n"
    "v_mfma_f32_16x16x32_bf16 v[112:115], v[48:51], v[56:59], v[112:115]\n"
    "ds_read_b128 v[72:75], v14 offset:17408\n"
    "ds_read_b64_tr_b16 v[38:39], v240 offset:20480\n"
    "s_waitcnt lgkmcnt(11)\n"
    "v_swap_b32 v62, v64\n"
    "v_swap_b32 v63, v65\n"
    "s_waitcnt lgkmcnt(10)\n"
    "s_nop 0\n"
    "v_mfma_f32_16x16x32_bf16 v[84:87], v[20:23], v[60:63], v[84:87]\n"
    "ds_read_b64_tr_b16 v[40:41], v241 offset:16384\n"
    "v_mfma_f32_16x16x32_bf16 v[88:91], v[20:23], v[64:67], v[88:91]\n"
    "ds_read_b64_tr_b16 v[42:43], v241 offset:20480\n"
    "s_waitcnt lgkmcnt(10)\n"
    "v_mfma_f32_16x16x32_bf16 v[92:95], v[24:27], v[60:63], v[92:95]\n"
    "ds_read_b64_tr_b16 v[44:45], v242 offset:16384\n"
    "v_mfma_f32_16x16x32_bf16 v[96:99], v[24:27], v[64:67], v[96:99]\n"
    "ds_read_b64_tr_b16 v[46:47], v242 offset:20480\n"
    "s_waitcnt lgkmcnt(10)\n"
    "v_mfma_f32_16x16x32_bf16 v[100:103], v[28:31], v[60:63], v[100:103]\n"
    "ds_read_b64_tr_b16 v[48:49], v243 offset:16384\n"
    "v_mfma_f32_16x16x32_bf16 v[104:107], v[28:31], v[64:67], v[104:107]\n"
    "ds_read_b64_tr_b16 v[50:51], v243 offset:20480\n"
    "s_waitcnt lgkmcnt(10)\n"
    "v_mfma_f32_16x16x32_bf16 v[108:111], v[32:35], v[60:63], v[108:111]\n"
    "ds_read_b128 v[52:55], v14 offset:32768\n"
    "ds_read_b64_tr_b16 v[20:21], v240 offset:32768\n"
    "v_mfma_f32_16x16x32_bf16 v[112:115], v[32:35], v[64:67], v[112:115]\n"
    "ds_read_b128 v[56:59], v14 offset:33792\n"
    "ds_read_b64_tr_b16 v[22:23], v240 offset:36864\n"
    "s_waitcnt lgkmcnt(11)\n"
    "v_swap_b32 v70, v72\n"
    "v_swap_b32 v71, v73\n"
    "s_waitcnt lgkmcnt(10)\n"
    "s_nop 0\n"
    "v_mfma_f32_16x16x32_bf16 v[84:87], v[36:39], v[68:71], v[84:87]\n"
    "ds_read_b64_tr_b16 v[24:25], v241 offset:32768\n"
    "v_mfma_f32_16x16x32_bf16 v[88:91], v[36:39], v[72:75], v[88:91]\n"
    "ds_read_b64_tr_b16 v[26:27], v241 offset:36864\n"
    "s_waitcnt lgkmcnt(10)\n"
    "v_mfma_f32_16x16x32_bf16 v[92:95], v[40:43], v[68:71], v[92:95]\n"
    "ds_read_b64_tr_b16 v[28:29], v242 offset:32768\n"
    "v_mfma_f32_16x16x32_bf16 v[96:99], v[40:43], v[72:75], v[96:99]\n"
    "ds_read_b64_tr_b16 v[30:31], v242 offset:36864\n"
    "s_waitcnt lgkmcnt(10)\n"
    "v_mfma_f32_16x16x32_bf16 v[100:103], v[44:47], v[68:71], v[100:103]\n"
    "ds_read_b64_tr_b16 v[32:33], v243 offset:32768\n"
    "v_mfma_f32_16x16x32_bf16 v[104:107], v[44:47], v[72:75], v[104:107]\n"
    "ds_read_b64_tr_b16 v[34:35], v243 offset:36864\n"
    "s_waitcnt lgkmcnt(10)\n"
    "v_mfma_f32_16x16x32_bf16 v[108:111], v[48:51], v[68:71], v[108:111]\n"
    "v_mfma_f32_16x16x32_bf16 v[112:115], v[48:51], v[72:75], v[112:115]\n"
    "s_waitcnt lgkmcnt(7)\n"
    "v_swap_b32 v54, v56\n"
    "v_swap_b32 v55, v57\n"
    "s_waitcnt lgkmcnt(6)\n"
    "s_nop 0\n"
    "v_mfma_f32_16x16x32_bf16 v[84:87], v[20:23], v[52:55], v[84:87]\n"
    "v_mfma_f32_16x16x32_bf16 v[88:91], v[20:23], v[56:59], v[88:91]\n"
    "s_waitcnt lgkmcnt(4)\n"
    "v_mfma_f32_16x16x32_bf16 v[92:95], v[24:27], v[52:55], v[92:95]\n"
    "v_mfma_f32_16x16x32_bf16 v[96:99], v[24:27], v[56:59], v[96:99]\n"
    "s_waitcnt lgkmcnt(2)\n"
    "v_mfma_f32_16x16x32_bf16 v[100:103], v[28:31], v[52:55], v[100:103]\n"
    "v_mfma_f32_16x16x32_bf16 v[104:107], v[28:31], v[56:59], v[104:107]\n"
    "s_waitcnt lgkmcnt(0)\n"
    "v_mfma_f32_16x16x32_bf16 v[108:111], v[32:35], v[52:55], v[108:111]\n"
    "v_mfma_f32_16x16x32_bf16 v[112:115], v[32:35], v[56:59], v[112:115]\n"
    "L_join_%=:\n"
    "s_waitcnt vmcnt(0)\n"
    "s_waitcnt lgkmcnt(0)\n"
    "s_barrier\n"
    "s_cmp_lg_u32 s7, 0\n"
    "s_cbranch_scc1 L_nf_o_%=\n"
    "s_lshl_b32 s64, s61, 5\n"
    "s_add_u32 s64, s64, s60\n"
    "s_add_u32 s64, s64, 0x100\n"
    "s_lshl_b32 s64, s64, 3\n"
    "v_mov_b32 v249, s64\n"
    "v_mov_b32 v250, s84\n"
    "v_mov_b32 v251, s85\n"
    "s_mov_b64 exec, 1\n"
    "global_store_dwordx2 v249, v[250:251], s[58:59] offset:2048 sc1\n"
    "s_mov_b64 exec, -1\n"
    "L_nf_o_%=:\n"
    "ds_read_b64_tr_b16 v[36:37], v240 offset:49152\n"
    "ds_read_b64_tr_b16 v[38:39], v240 offset:53248\n"
    "ds_read_b128 v[60:63], v14 offset:49152\n"
    "ds_read_b128 v[64:67], v14 offset:50176\n"
    "global_load_dwordx2 v[252:253], v5, s[58:59] sc1\n"
    "s_cmp_eq_u32 s70, 3\n"
    "s_cselect_b32 s79, 0x40000000, s79\n"
    "s_add_u32 m0, s75, 98304\n"
    "s_nop 0\n"
    "buffer_load_dwordx4 v10, s[36:39], s79 offen sc1 lds\n"
    "ds_read_b64_tr_b16 v[40:41], v241 offset:49152\n"
    "ds_read_b64_tr_b16 v[42:43], v241 offset:53248\n"
    "s_add_u32 s62, s79, s71\n"
    "s_add_u32 m0, s75, 99328\n"
    "s_nop 0\n"
    "buffer_load_dwordx4 v10, s[36:39], s62 offen sc1 lds\n"
    "s_add_u32 s79, s79, s74\n"
    "ds_read_b64_tr_b16 v[44:45], v242 offset:49152\n"
    "ds_read_b64_tr_b16 v[46:47], v242 offset:53248\n"
    "s_add_u32 s62, s76, 0\n"
    "s_add_u32 m0, s75, 114688\n"
    "s_nop 0\n"
    "buffer_load_dwordx4 v18, s[8:11], s62 offen lds\n"
    "ds_read_b64_tr_b16 v[48:49], v243 offset:49152\n"
    "ds_read_b64_tr_b16 v[50:51], v243 offset:53248\n"
    "s_add_u32 s62, s76, 128\n"
    "s_add_u32 m0, s75, 115712\n"
    "s_nop 0\n"
    "buffer_load_dwordx4 v18, s[8:11], s62 offen lds\n"
    "v_add_u32 v240, 65536, v240\n"
    "v_add_u32 v241, 65536, v241\n"
    "v_add_u32 v242, 65536, v242\n"
    "v_add_u32 v243, 65536, v243\n"
    "v_add_u32 v14, 65536, v14\n"
    "ds_read_b64_tr_b16 v[20:21], v240 offset:0\n"
    "ds_read_b64_tr_b16 v[22:23], v240 offset:4096\n"
    "ds_read_b128 v[68:71], v14 offset:0\n"
    "ds_read_b128 v[72:75], v14 offset:1024\n"
    "s_add_u32 s62, s76, 32768\n"
    "s_add_u32 m0, s75, 131072\n"
    "s_nop 0\n"
    "buffer_load_dwordx4 v18, s[8:11], s62 offen lds\n"
    "s_add_u32 s62, s76, 32896\n"
    "s_add_u32 m0, s75, 132096\n"
    "s_nop 0\n"
    "buffer_load_dwordx4 v18, s[8:11], s62 offen lds\n"
    "s_waitcnt lgkmcnt(10)\n"
    "v_swap_b32 v62, v64\n"
    "v_swap_b32 v63, v65\n"
    "s_nop 1\n"
    "v_mfma_f32_16x16x32_bf16 v[84:87], v[36:39], v[60:63], v[84:87]\n"
    "s_add_u32 s62, s76, 65536\n"
    "ds_read_b64_tr_b16 v[24:25], v241 offset:0\n"
    "v_mfma_f32_16x16x32_bf16 v[88:91], v[36:39], v[64:67], v[88:91]\n"
    "s_add_u32 m0, s75, 147456\n"
    "s_nop 0\n"
    "buffer_load_dwordx4 v18, s[8:11], s62 offen lds\n"
    "ds_read_b64_tr_b16 v[26:27], v241 offset:4096\n"
    "s_waitcnt lgkmcnt(10)\n"
    "v_mfma_f32_16x16x32_bf16 v[92:95], v[40:43], v[60:63], v[92:95]\n"
    "s_add_u32 s62, s76, 65664\n"
    "ds_read_b64_tr_b16 v[28:29], v242 offset:0\n"
    "v_mfma_f32_16x16x32_bf16 v[96:99], v[40:43], v[64:67], v[96:99]\n"
    "s_add_u32 m0, s75, 148480\n"
    "s_nop 0\n"
    "buffer_load_dwordx4 v18, s[8:11], s62 offen lds\n"
    "ds_read_b64_tr_b16 v[30:31], v242 offset:4096\n"
    "s_waitcnt lgkmcnt(10)\n"
    "v_mfma_f32_16x16x32_bf16 v[100:103], v[44:47], v[60:63], v[100:103]\n"
    "s_add_u32 s62, s76, 98304\n"
    "ds_read_b64_tr_b16 v[32:33], v243 offset:0\n"
    "v_mfma_f32_16x16x32_bf16 v[104:107], v[44:47], v[64:67], v[104:107]\n"
    "s_add_u32 m0, s75, 0\n"
    "s_nop 0\n"
    "buffer_load_dwordx4 v18, s[8:11], s62 offen lds\n"
    "ds_read_b64_tr_b16 v[34:35], v243 offset:4096\n"
    "s_waitcnt lgkmcnt(10)\n"
    "v_mfma_f32_16x16x32_bf16 v[108:111], v[48:51], v[60:63], v[108:111]\n"
    "ds_read_b128 v[52:55], v14 offset:16384\n"
    "ds_read_b128 v[56:59], v14 offset:17408\n"
    "s_add_u32 s62, s76, 98432\n"
    "s_add_u32 m0, s75, 1024\n"
    "ds_read_b64_tr_b16 v[36:37], v240 offset:16384\n"
    "v_mfma_f32_16x16x32_bf16 v[112:115], v[48:51], v[64:67], v[112:115]\n"
    "buffer_load_dwordx4 v18, s[8:11], s62 offen lds\n"
    "s_add_u32 s62, s76, 131072\n"
    "s_add_u32 m0, s75, 16384\n"
    "s_nop 0\n"
    "buffer_load_dwordx4 v18, s[8:11], s62 offen lds\n"
    "ds_read_b64_tr_b16 v[38:39], v240 offset:20480\n"
    "s_waitcnt lgkmcnt(10)\n"
    "v_swap_b32 v70, v72\n"
    "v_swap_b32 v71, v73\n"
    "s_nop 1\n"
    "v_mfma_f32_16x16x32_bf16 v[84:87], v[20:23], v[68:71], v[84:87]\n"
    "s_add_u32 s62, s76, 131200\n"
    "ds_read_b64_tr_b16 v[40:41], v241 offset:16384\n"
    "v_mfma_f32_16x16x32_bf16 v[88:91], v[20:23], v[72:75], v[88:91]\n"
    "s_add_u32 m0, s75, 17408\n"
    "s_nop 0\n"
    "buffer_load_dwordx4 v18, s[8:11], s62 offen lds\n"
    "ds_read_b64_tr_b16 v[42:43], v241 offset:20480\n"
    "s_waitcnt lgkmcnt(10)\n"
    "v_mfma_f32_16x16x32_bf16 v[92:95], v[24:27], v[68:71], v[92:95]\n"
    "s_add_u32 s62, s76, 163840\n"
    "ds_read_b64_tr_b16 v[44:45], v242 offset:16384\n"
    "v_mfma_f32_16x16x32_bf16 v[96:99], v[24:27], v[72:75], v[96:99]\n"
    "s_add_u32 m0, s75, 32768\n"
    "s_nop 0\n"
    "buffer_load_dwordx4 v18, s[8:11], s62 offen lds\n"
    "ds_read_b64_tr_b16 v[46:47], v242 offset:20480\n"
    "s_waitcnt lgkmcnt(10)\n"
    "v_mfma_f32_16x16x32_bf16 v[100:103], v[28:31], v[68:71], v[100:103]\n"
    "s_add_u32 s62, s76, 163968\n"
    "ds_read_b64_tr_b16 v[48:49], v243 offset:16384\n"
    "v_mfma_f32_16x16x32_bf16 v[104:107], v[28:31], v[72:75], v[104:107]\n"
    "s_add_u32 m0, s75, 33792\n"
    "s_nop 0\n"
    "buffer_load_dwordx4 v18, s[8:11], s62 offen lds\n"
    "ds_read_b64_tr_b16 v[50:51], v243 offset:20480\n"
    "s_waitcnt lgkmcnt(10)\n"
    "v_mfma_f32_16x16x32_bf16 v[108:111], v[32:35], v[68:71], v[108:111]\n"
    "v_mfma_f32_16x16x32_bf16 v[112:115], v[32:35], v[72:75], v[112:115]\n"
    "s_waitcnt lgkmcnt(8)\n"
    "v_swap_b32 v54, v56\n"
    "v_swap_b32 v55, v57\n"
    "s_waitcnt lgkmcnt(6)\n"
    "s_nop 0\n"
    "v_mfma_f32_16x16x32_bf16 v[84:87], v[36:39], v[52:55], v[84:87]\n"
    "v_mfma_f32_16x16x32_bf16 v[88:91], v[36:39], v[56:59], v[88:91]\n"
    "s_waitcnt lgkmcnt(4)\n"
    "v_mfma_f32_16x16x32_bf16 v[92:95], v[40:43], v[52:55], v[92:95]\n"
    "v_mfma_f32_16x16x32_bf16 v[96:99], v[40:43], v[56:59], v[96:99]\n"
    "s_waitcnt lgkmcnt(2)\n"
    "v_mfma_f32_16x16x32_bf16 v[100:103], v[44:47], v[52:55], v[100:103]\n"
    "v_mfma_f32_16x16x32_bf16 v[104:107], v[44:47], v[56:59], v[104:107]\n"
    "s_waitcnt lgkmcnt(0)\n"
    "v_mfma_f32_16x16x32_bf16 v[108:111], v[48:51], v[52:55], v[108:111]\n"
    "v_mfma_f32_16x16x32_bf16 v[112:115], v[48:51], v[56:59], v[112:115]\n"
    "s_and_b32 s62, s7, 1\n"
    "s_lshl_b32 s62, s62, 13\n"
    "s_add_u32 s69, s62, 49152\n"
    "v_add_u32 v244, s69, v240\n"
    "v_add_u32 v245, s69, v241\n"
    "v_add_u32 v246, s69, v242\n"
    "v_add_u32 v247, s69, v243\n"
    "v_xor_b32 v244, 1024, v244\n"
    "v_xor_b32 v245, 1024, v245\n"
    "v_xor_b32 v246, 1024, v246\n"
    "v_xor_b32 v247, 1024, v247\n"
    "s_waitcnt vmcnt(14)\n"
    "v_cmp_ne_u64 vcc, s[84:85], v[252:253]\n"
    "s_nop 0\n"
    "s_cbranch_vccnz L_oretry_%=\n"
    "L_ook_%=:\n"
    "s_add_u32 s62, s77, 0\n"
    "buffer_load_dwordx4 v[180:183], v15, s[16:19], s62 offen sc1\n"
    "s_add_u32 s62, s77, 132096\n"
    "buffer_load_dwordx4 v[184:187], v15, s[16:19], s62 offen sc1\n"
    "s_add_u32 s62, s77, 1024\n"
    "buffer_load_dwordx4 v[188:191], v15, s[16:19], s62 offen sc1\n"
    "s_add_u32 s62, s77, 133120\n"
    "buffer_load_dwordx4 v[192:195], v15, s[16:19], s62 offen sc1\n"
    "s_add_u32 s62, s77, 2048\n"
    "buffer_load_dwordx4 v[196:199], v15, s[16:19], s62 offen sc1\n"
    "s_add_u32 s62, s77, 134144\n"
    "buffer_load_dwordx4 v[200:203], v15, s[16:19], s62 offen sc1\n"
    "s_add_u32 s62, s77, 3072\n"
    "buffer_load_dwordx4 v[204:207], v15, s[16:19], s62 offen sc1\n"
    "s_add_u32 s62, s77, 135168\n"
    "buffer_load_dwordx4 v[208:211], v15, s[16:19], s62 offen sc1\n"
    "s_add_u32 s62, s77, 4096\n"
    "buffer_load_dwordx4 v[212:215], v15, s[16:19], s62 offen sc1\n"
    "s_add_u32 s62, s77, 136192\n"
    "buffer_load_dwordx4 v[216:219], v15, s[16:19], s62 offen sc1\n"
    "s_waitcnt vmcnt(16)\n"
    "s_barrier\n"
    "ds_read_b64_tr_b16 v[20:21], v240 offset:32768\n"
    "ds_read_b64_tr_b16 v[22:23], v240 offset:36864\n"
    "ds_read_b128 v[60:63], v14 offset:32768\n"
    "ds_read_b128 v[64:67], v14 offset:33792\n"
    "s_add_u32 s62, s76, 196608\n"
    "s_add_u32 m0, s75, 49152\n"
    "s_nop 0\n"
    "buffer_load_dwordx4 v18, s[8:11], s62 offen lds\n"
    "ds_read_b64_tr_b16 v[24:25], v241 offset:32768\n"
    "ds_read_b64_tr_b16 v[26:27], v241 offset:36864\n"
    "ds_read_b64_tr_b16 v[28:29], v242 offset:32768\n"
    "ds_read_b64_tr_b16 v[30:31], v242 offset:36864\n"
    "ds_read_b64_tr_b16 v[32:33], v243 offset:32768\n"
    "ds_read_b64_tr_b16 v[34:35], v243 offset:36864\n"
    "s_add_u32 s62, s76, 196736\n"
    "s_add_u32 m0, s75, 50176\n"
    "s_nop 0\n"
    "buffer_load_dwordx4 v18, s[8:11], s62 offen lds\n"
    "v_add_u32 v240, s69, v240\n"
    "v_add_u32 v241, s69, v241\n"
    "v_add_u32 v242, s69, v242\n"
    "v_add_u32 v243, s69, v243\n"
    "ds_read_b64_tr_b16 v[36:37], v240 offset:0\n"
    "ds_read_b64_tr_b16 v[38:39], v240 offset:4096\n"
    "s_waitcnt lgkmcnt(8)\n"
    "v_swap_b32 v62, v64\n"
    "v_swap_b32 v63, v65\n"
    "s_nop 1\n"
    "v_mfma_f32_16x16x32_bf16 v[84:87], v[20:23], v[60:63], v[84:87]\n"
    "ds_read_b64_tr_b16 v[40:41], v241 offset:0\n"
    "v_mfma_f32_16x16x32_bf16 v[88:91], v[20:23], v[64:67], v[88:91]\n"
    "ds_read_b64_tr_b16 v[42:43], v241 offset:4096\n"
    "s_waitcnt lgkmcnt(8)\n"
    "v_mfma_f32_16x16x32_bf16 v[92:95], v[24:27], v[60:63], v[92:95]\n"
    "s_add_u32 s62, s76, 229376\n"
    "ds_read_b64_tr_b16 v[44:45], v242 offset:0\n"
    "v_mfma_f32_16x16x32_bf16 v[96:99], v[24:27], v[64:67], v[96:99]\n"
    "s_add_u32 m0, s75, 65536\n"
    "s_nop 0\n"
    "buffer_load_dwordx4 v18, s[8:11], s62 offen lds\n"
    "ds_read_b64_tr_b16 v[46:47], v242 offset:4096\n"
    "s_waitcnt lgkmcnt(8)\n"
    "v_mfma_f32_16x16x32_bf16 v[100:103], v[28:31], v[60:63], v[100:103]\n"
    "ds_read_b64_tr_b16 v[48:49], v243 offset:0\n"
    "v_mfma_f32_16x16x32_bf16 v[104:107], v[28:31], v[64:67], v[104:107]\n"
    "ds_read_b64_tr_b16 v[50:51], v243 offset:4096\n"
    "s_waitcnt lgkmcnt(8)\n"
    "v_mfma_f32_16x16x32_bf16 v[108:111], v[32:35], v[60:63], v[108:111]\n"
    "ds_read_b64_tr_b16 v[20:21], v244 offset:0\n"
    "v_mfma_f32_16x16x32_bf16 v[112:115], v[32:35], v[64:67], v[112:115]\n"
    "ds_read_b64_tr_b16 v[22:23], v244 offset:4096\n"
    "s_waitcnt vmcnt(12) lgkmcnt(8)\n"
    "v_mfma_f32_16x16x32_bf16 v[116:119], v[36:39], v[180:183], 0\n"
    "ds_read_b64_tr_b16 v[24:25], v245 offset:0\n"
    "s_waitcnt vmcnt(11)\n"
    "v_mfma_f32_16x16x32_bf16 v[120:123], v[36:39], v[184:187], 0\n"
    "ds_read_b64_tr_b16 v[26:27], v245 offset:4096\n"
    "s_waitcnt lgkmcnt(8)\n"
    "v_mfma_f32_16x16x32_bf16 v[124:127], v[40:43], v[180:183], 0\n"
    "s_add_u32 s62, s76, 229504\n"
    "s_add_u32 m0, s75, 66560\n"
    "ds_read_b64_tr_b16 v[28:29], v246 offset:0\n"
    "v_mfma_f32_16x16x32_bf16 v[128:131], v[40:43], v[184:187], 0\n"
    "buffer_load_dwordx4 v18, s[8:11], s62 offen lds\n"
    "s_add_u32 s62, s77, 5120\n"
    "buffer_load_dwordx4 v[220:223], v15, s[16:19], s62 offen sc1\n"
    "ds_read_b64_tr_b16 v[30:31], v246 offset:4096\n"
    "s_waitcnt lgkmcnt(8)\n"
    "v_mfma_f32_16x16x32_bf16 v[132:135], v[44:47], v[180:183], 0\n"
    "ds_read_b64_tr_b16 v[32:33], v247 offset:0\n"
    "v_mfma_f32_16x16x32_bf16 v[136:139], v[44:47], v[184:187], 0\n"
    "ds_read_b64_tr_b16 v[34:35], v247 offset:4096\n"
    "s_waitcnt lgkmcnt(8)\n"
    "v_mfma_f32_16x16x32_bf16 v[140:143], v[48:51], v[180:183], 0\n"
    "v_cvt_pk_bf16_f32 v60, v92, v93\n"
    "v_cvt_pk_bf16_f32 v61, v94, v95\n"
    "ds_read_b64_tr_b16 v[36:37], v240 offset:16384\n"
    "v_mfma_f32_16x16x32_bf16 v[144:147], v[48:51], v[184:187], 0\n"
    "s_add_u32 s62, s77, 137216\n"
    "buffer_load_dwordx4 v[224:227], v15, s[16:19], s62 offen sc1\n"
    "ds_read_b64_tr_b16 v[38:39], v240 offset:20480\n"
    "s_waitcnt lgkmcnt(8)\n"
    "v_mfma_f32_16x16x32_bf16 v[148:151], v[20:23], v[180:183], 0\n"
    "s_add_u32 s62, s76, 262144\n"
    "s_add_u32 m0, s75, 81920\n"
    "ds_read_b64_tr_b16 v[40:41], v241 offset:16384\n"
    "v_mfma_f32_16x16x32_bf16 v[152:155], v[20:23], v[184:187], 0\n"
    "v_cvt_pk_bf16_f32 v62, v96, v97\n"
    "buffer_load_dwordx4 v18, s[8:11], s62 offen lds\n"
    "ds_read_b64_tr_b16 v[42:43], v241 offset:20480\n"
    "s_waitcnt lgkmcnt(8)\n"
    "v_mfma_f32_16x16x32_bf16 v[156:159], v[24:27], v[180:183], 0\n"
    "ds_read_b64_tr_b16 v[44:45], v242 offset:16384\n"
    "v_mfma_f32_16x16x32_bf16 v[160:163], v[24:27], v[184:187], 0\n"
    "ds_read_b64_tr_b16 v[46:47], v242 offset:20480\n"
    "v_cvt_pk_bf16_f32 v63, v98, v99\n"
    "s_waitcnt lgkmcnt(8)\n"
    "v_mfma_f32_16x16x32_bf16 v[164:167], v[28:31], v[180:183], 0\n"
    "ds_read_b64_tr_b16 v[48:49], v243 offset:16384\n"
    "v_mfma_f32_16x16x32_bf16 v[168:171], v[28:31], v[184:187], 0\n"
    "ds_read_b64_tr_b16 v[50:51], v243 offset:20480\n"
    "buffer_store_dwordx4 v[60:63], v15, s[40:43], s44 offen sc1\n"
    "s_waitcnt lgkmcnt(8)\n"
    "v_mfma_f32_16x16x32_bf16 v[172:175], v[32:35], v[180:183], 0\n"
    "s_add_u32 s62, s76, 262272\n"
    "s_add_u32 m0, s75, 82944\n"
    "ds_read_b64_tr_b16 v[20:21], v244 offset:16384\n"
    "v_mfma_f32_16x16x32_bf16 v[176:179], v[32:35], v[184:187], 0\n"
    "v_cvt_pk_bf16_f32 v64, v100, v101\n"
    "buffer_load_dwordx4 v18, s[8:11], s62 offen lds\n"
    "ds_read_b64_tr_b16 v[22:23], v244 offset:20480\n"
    "s_waitcnt vmcnt(16) lgkmcnt(8)\n"
    "v_mfma_f32_16x16x32_bf16 v[116:119], v[36:39], v[188:191], v[116:119]\n"
    "ds_read_b64_tr_b16 v[24:25], v245 offset:16384\n"
    "s_waitcnt vmcnt(15)\n"
    "v_mfma_f32_16x16x32_bf16 v[120:123], v[36:39], v[192:195], v[120:123]\n"
    "ds_read_b64_tr_b16 v[26:27], v245 offset:20480\n"
    "v_cvt_pk_bf16_f32 v65, v102, v103\n"
    "s_waitcnt lgkmcnt(8)\n"
    "v_mfma_f32_16x16x32_bf16 v[124:127], v[40:43], v[188:191], v[124:127]\n"
    "v_cvt_pk_bf16_f32 v66, v104, v105\n"
    "ds_read_b64_tr_b16 v[28:29], v246 offset:16384\n"
    "v_mfma_f32_16x16x32_bf16 v[128:131], v[40:43], v[192:195], v[128:131]\n"
    "s_add_u32 s62, s77, 6144\n"
    "buffer_load_dwordx4 v[180:183], v15, s[16:19], s62 offen sc1\n"
    "ds_read_b64_tr_b16 v[30:31], v246 offset:20480\n"
    "s_waitcnt lgkmcnt(8)\n"
    "v_mfma_f32_16x16x32_bf16 v[132:135], v[44:47], v[188:191], v[132:135]\n"
    "ds_read_b64_tr_b16 v[32:33], v247 offset:16384\n"
    "v_mfma_f32_16x16x32_bf16 v[136:139], v[44:47], v[192:195], v[136:139]\n"
    "ds_read_b64_tr_b16 v[34:35], v247 offset:20480\n"
    "v_cvt_pk_bf16_f32 v67, v106, v107\n"
    "s_waitcnt lgkmcnt(8)\n"
    "v_mfma_f32_16x16x32_bf16 v[140:143], v[48:51], v[188:191], v[140:143]\n"
    "buffer_store_dwordx4 v[64:67], v15, s[40:43], s45 offen sc1\n"
    "ds_read_b64_tr_b16 v[36:37], v240 offset:32768\n"
    "v_mfma_f32_16x16x32_bf16 v[144:147], v[48:51], v[192:195], v[144:147]\n"
    "s_add_u32 s62, s77, 138240\n"
    "buffer_load_dwordx4 v[184:187], v15, s[16:19], s62 offen sc1\n"
    "ds_read_b64_tr_b16 v[38:39], v240 offset:36864\n"
    "s_waitcnt lgkmcnt(8)\n"
    "v_mfma_f32_16x16x32_bf16 v[148:151], v[20:23], v[188:191], v[148:151]\n"
    "ds_read_b64_tr_b16 v[40:41], v241 offset:32768\n"
    "v_mfma_f32_16x16x32_bf16 v[152:155], v[20:23], v[192:195], v[152:155]\n"
    "ds_read_b64_tr_b16 v[42:43], v241 offset:36864\n"
    "v_cvt_pk_bf16_f32 v68, v108, v109\n"
    "s_waitcnt lgkmcnt(8)\n"
    "v_mfma_f32_16x16x32_bf16 v[156:159], v[24:27], v[188:191], v[156:159]\n"
    "ds_read_b64_tr_b16 v[44:45], v242 offset:32768\n"
    "v_mfma_f32_16x16x32_bf16 v[160:163], v[24:27], v[192:195], v[160:163]\n"
    "ds_read_b64_tr_b16 v[46:47], v242 offset:36864\n"
    "v_cvt_pk_bf16_f32 v69, v110, v111\n"
    "s_waitcnt lgkmcnt(8)\n"
    "v_mfma_f32_16x16x32_bf16 v[164:167], v[28:31], v[188:191], v[164:167]\n"
    "ds_read_b64_tr_b16 v[48:49], v243 offset:32768\n"
    "v_mfma_f32_16x16x32_bf16 v[168:171], v[28:31], v[192:195], v[168:171]\n"
    "ds_read_b64_tr_b16 v[50:51], v243 offset:36864\n"
    "v_cvt_pk_bf16_f32 v70, v112, v113\n"
    "s_waitcnt lgkmcnt(8)\n"
    "v_mfma_f32_16x16x32_bf16 v[172:175], v[32:35], v[188:191], v[172:175]\n"
    "ds_read_b64_tr_b16 v[20:21], v244 offset:32768\n"
    "v_mfma_f32_16x16x32_bf16 v[176:179], v[32:35], v[192:195], v[176:179]\n"
    "ds_read_b64_tr_b16 v[22:23], v244 offset:36864\n"
    "v_cvt_pk_bf16_f32 v71, v114, v115\n"
    "s_waitcnt vmcnt(17) lgkmcnt(8)\n"
    "v_mfma_f32_16x16x32_bf16 v[116:119], v[36:39], v[196:199], v[116:119]\n"
    "ds_read_b64_tr_b16 v[24:25], v245 offset:32768\n"
    "s_waitcnt vmcnt(16)\n"
    "v_mfma_f32_16x16x32_bf16 v[120:123], v[36:39], v[200:203], v[120:123]\n"
    "ds_read_b64_tr_b16 v[26:27], v245 offset:36864\n"
    "buffer_store_dwordx4 v[68:71], v15, s[40:43], s46 offen sc1\n"
    "s_waitcnt lgkmcnt(8)\n"
    "v_mfma_f32_16x16x32_bf16 v[124:127], v[40:43], v[196:199], v[124:127]\n"
    "s_add_u32 s62, s77, 7168\n"
    "ds_read_b64_tr_b16 v[28:29], v246 offset:32768\n"
    "v_mfma_f32_16x16x32_bf16 v[128:131], v[40:43], v[200:203], v[128:131]\n"
    "buffer_load_dwordx4 v[188:191], v15, s[16:19], s62 offen sc1\n"
    "ds_read_b64_tr_b16 v[30:31], v246 offset:36864\n"
    "s_waitcnt lgkmcnt(8)\n"
    "v_mfma_f32_16x16x32_bf16 v[132:135], v[44:47], v[196:199], v[132:135]\n"
    "ds_read_b64_tr_b16 v[32:33], v247 offset:32768\n"
    "v_mfma_f32_16x16x32_bf16 v[136:139], v[44:47], v[200:203], v[136:139]\n"
    "ds_read_b64_tr_b16 v[34:35], v247 offset:36864\n"
    "v_subrev_u32 v240, 114688, v240\n"
    "v_subrev_u32 v241, 114688, v241\n"
    "v_subrev_u32 v242, 114688, v242\n"
    "v_subrev_u32 v243, 114688, v243\n"
    "v_subrev_u32 v244, 114688, v244\n"
    "v_subrev_u32 v245, 114688, v245\n"
    "v_subrev_u32 v246, 114688, v246\n"
    "v_subrev_u32 v247, 114688, v247\n"
    "s_waitcnt vmcnt(63)\n"
    "s_waitcnt lgkmcnt(0)\n"
    "s_barrier\n"
    "v_mfma_f32_16x16x32_bf16 v[140:143], v[48:51], v[196:199], v[140:143]\n"
    "s_add_u32 s62, s77, 139264\n"
    "ds_read_b64_tr_b16 v[36:37], v240 offset:0\n"
    "v_mfma_f32_16x16x32_bf16 v[144:147], v[48:51], v[200:203], v[144:147]\n"
    "buffer_load_dwordx4 v[192:195], v15, s[16:19], s62 offen sc1\n"
    "ds_read_b64_tr_b16 v[38:39], v240 offset:4096\n"
    "v_mfma_f32_16x16x32_bf16 v[148:151], v[20:23], v[196:199], v[148:151]\n"
    "s_add_u32 s62, s76, 294912\n"
    "ds_read_b64_tr_b16 v[40:41], v241 offset:0\n"
    "v_mfma_f32_16x16x32_bf16 v[152:155], v[20:23], v[200:203], v[152:155]\n"
    "s_add_u32 m0, s75, 98304\n"
    "s_nop 0\n"
    "buffer_load_dwordx4 v18, s[8:11], s62 offen lds\n"
    "ds_read_b64_tr_b16 v[42:43], v241 offset:4096\n"
    "v_mfma_f32_16x16x32_bf16 v[156:159], v[24:27], v[196:199], v[156:159]\n"
    "ds_read_b64_tr_b16 v[44:45], v242 offset:0\n"
    "v_mfma_f32_16x16x32_bf16 v[160:163], v[24:27], v[200:203], v[160:163]\n"
    "ds_read_b64_tr_b16 v[46:47], v242 offset:4096\n"
    "v_mfma_f32_16x16x32_bf16 v[164:167], v[28:31], v[196:199], v[164:167]\n"
    "s_add_u32 s62, s76, 295040\n"
    "ds_read_b64_tr_b16 v[48:49], v243 offset:0\n"
    "v_mfma_f32_16x16x32_bf16 v[168:171], v[28:31], v[200:203], v[168:171]\n"
    "s_add_u32 m0, s75, 99328\n"
    "s_nop 0\n"
    "buffer_load_dwordx4 v18, s[8:11], s62 offen lds\n"
    "ds_read_b64_tr_b16 v[50:51], v243 offset:4096\n"
    "v_mfma_f32_16x16x32_bf16 v[172:175], v[32:35], v[196:199], v[172:175]\n"
    "ds_read_b64_tr_b16 v[20:21], v244 offset:0\n"
    "v_mfma_f32_16x16x32_bf16 v[176:179], v[32:35], v[200:203], v[176:179]\n"
    "ds_read_b64_tr_b16 v[22:23], v244 offset:4096\n"
    "s_waitcnt vmcnt(20) lgkmcnt(8)\n"
    "v_mfma_f32_16x16x32_bf16 v[116:119], v[36:39], v[204:207], v[116:119]\n"
    "ds_read_b64_tr_b16 v[24:25], v245 offset:0\n"
    "s_waitcnt vmcnt(19)\n"
    "v_mfma_f32_16x16x32_bf16 v[120:123], v[36:39], v[208:211], v[120:123]\n"
    "ds_read_b64_tr_b16 v[26:27], v245 offset:4096\n"
    "s_waitcnt lgkmcnt(8)\n"
    "v_mfma_f32_16x16x32_bf16 v[124:127], v[40:43], v[204:207], v[124:127]\n"
    "s_add_u32 s62, s77, 8192\n"
    "buffer_load_dwordx4 v[196:199], v15, s[16:19], s62 offen sc1\n"
    "ds_read_b64_tr_b16 v[28:29], v246 offset:0\n"
    "v_mfma_f32_16x16x32_bf16 v[128:131], v[40:43], v[208:211], v[128:131]\n"
    "s_add_u32 s62, s76, 327680\n"
    "s_add_u32 m0, s75, 114688\n"
    "s_nop 0\n"
    "buffer_load_dwordx4 v18, s[8:11], s62 offen lds\n"
    "ds_read_b64_tr_b16 v[30:31], v246 offset:4096\n"
    "s_waitcnt lgkmcnt(8)\n"
    "v_mfma_f32_16x16x32_bf16 v[132:135], v[44:47], v[204:207], v[132:135]\n"
    "ds_read_b64_tr_b16 v[32:33], v247 offset:0\n"
    "v_mfma_f32_16x16x32_bf16 v[136:139], v[44:47], v[208:211], v[136:139]\n"
    "ds_read_b64_tr_b16 v[34:35], v247 offset:4096\n"
    "s_waitcnt lgkmcnt(8)\n"
    "v_mfma_f32_16x16x32_bf16 v[140:143], v[48:51], v[204:207], v[140:143]\n"
    "s_add_u32 s62, s77, 140288\n"
    "buffer_load_dwordx4 v[200:203], v15, s[16:19], s62 offen sc1\n"
    "ds_read_b64_tr_b16 v[36:37], v240 offset:16384\n"
    "v_mfma_f32_16x16x32_bf16 v[144:147], v[48:51], v[208:211], v[144:147]\n"
    "s_add_u32 s62, s76, 327808\n"
    "s_add_u32 m0, s75, 115712\n"
    "s_nop 0\n"
    "buffer_load_dwordx4 v18, s[8:11], s62 offen lds\n"
    "ds_read_b64_tr_b16 v[38:39], v240 offset:20480\n"
    "s_waitcnt lgkmcnt(8)\n"
    "v_mfma_f32_16x16x32_bf16 v[148:151], v[20:23], v[204:207], v[148:151]\n"
    "ds_read_b64_tr_b16 v[40:41], v241 offset:16384\n"
    "v_mfma_f32_16x16x32_bf16 v[152:155], v[20:23], v[208:211], v[152:155]\n"
    "ds_read_b64_tr_b16 v[42:43], v241 offset:20480\n"
    "s_waitcnt lgkmcnt(8)\n"
    "v_mfma_f32_16x16x32_bf16 v[156:159], v[24:27], v[204:207], v[156:159]\n"
    "ds_read_b64_tr_b16 v[44:45], v242 offset:16384\n"
    "v_mfma_f32_16x16x32_bf16 v[160:163], v[24:27], v[208:211], v[160:163]\n"
    "ds_read_b64_tr_b16 v[46:47], v242 offset:20480\n"
    "s_waitcnt lgkmcnt(8)\n"
    "v_mfma_f32_16x16x32_bf16 v[164:167], v[28:31], v[204:207], v[164:167]\n"
    "s_add_u32 s62, s76, 360448\n"
    "ds_read_b64_tr_b16 v[48:49], v243 offset:16384\n"
    "v_mfma_f32_16x16x32_bf16 v[168:171], v[28:31], v[208:211], v[168:171]\n"
    "s_add_u32 m0, s75, 131072\n"
    "s_nop 0\n"
    "buffer_load_dwordx4 v18, s[8:11], s62 offen lds\n"
    "ds_read_b64_tr_b16 v[50:51], v243 offset:20480\n"
    "s_waitcnt lgkmcnt(8)\n"
    "v_mfma_f32_16x16x32_bf16 v[172:175], v[32:35], v[204:207], v[172:175]\n"
    "ds_read_b64_tr_b16 v[20:21], v244 offset:16384\n"
    "v_mfma_f32_16x16x32_bf16 v[176:179], v[32:35], v[208:211], v[176:179]\n"
    "ds_read_b64_tr_b16 v[22:23], v244 offset:20480\n"
    "s_waitcnt vmcnt(23) lgkmcnt(8)\n"
    "v_mfma_f32_16x16x32_bf16 v[116:119], v[36:39], v[212:215], v[116:119]\n"
    "ds_read_b64_tr_b16 v[24:25], v245 offset:16384\n"
    "s_waitcnt vmcnt(22)\n"
    "v_mfma_f32_16x16x32_bf16 v[120:123], v[36:39], v[216:219], v[120:123]\n"
    "ds_read_b64_tr_b16 v[26:27], v245 offset:20480\n"
    "s_waitcnt lgkmcnt(8)\n"
    "v_mfma_f32_16x16x32_bf16 v[124:127], v[40:43], v[212:215], v[124:127]\n"
    "s_add_u32 s62, s77, 9216\n"
    "buffer_load_dwordx4 v[204:207], v15, s[16:19], s62 offen sc1\n"
    "ds_read_b64_tr_b16 v[28:29], v246 offset:16384\n"
    "v_mfma_f32_16x16x32_bf16 v[128:131], v[40:43], v[216:219], v[128:131]\n"
    "s_add_u32 s62, s76, 360576\n"
    "s_add_u32 m0, s75, 132096\n"
    "s_nop 0\n"
    "buffer_load_dwordx4 v18, s[8:11], s62 offen lds\n"
    "ds_read_b64_tr_b16 v[30:31], v246 offset:20480\n"
    "s_waitcnt lgkmcnt(8)\n"
    "v_mfma_f32_16x16x32_bf16 v[132:135], v[44:47], v[212:215], v[132:135]\n"
    "ds_read_b64_tr_b16 v[32:33], v247 offset:16384\n"
    "v_mfma_f32_16x16x32_bf16 v[136:139], v[44:47], v[216:219], v[136:139]\n"
    "ds_read_b64_tr_b16 v[34:35], v247 offset:20480\n"
    "s_waitcnt lgkmcnt(8)\n"
    "v_mfma_f32_16x16x32_bf16 v[140:143], v[48:51], v[212:215], v[140:143]\n"
    "s_add_u32 s62, s77, 141312\n"
    "buffer_load_dwordx4 v[208:211], v15, s[16:19], s62 offen sc1\n"
    "ds_read_b64_tr_b16 v[36:37], v240 offset:32768\n"
    "v_mfma_f32_16x16x32_bf16 v[144:147], v[48:51], v[216:219], v[144:147]\n"
    "s_add_u32 s62, s76, 393216\n"
    "s_add_u32 m0, s75, 147456\n"
    "s_nop 0\n"
    "buffer_load_dwordx4 v18, s[8:11], s62 offen lds\n"
    "ds_read_b64_tr_b16 v[38:39], v240 offset:36864\n"
    "s_waitcnt lgkmcnt(8)\n"
    "v_mfma_f32_16x16x32_bf16 v[148:151], v[20:23], v[212:215], v[148:151]\n"
    "ds_read_b64_tr_b16 v[40:41], v241 offset:32768\n"
    "v_mfma_f32_16x16x32_bf16 v[152:155], v[20:23], v[216:219], v[152:155]\n"
    "ds_read_b64_tr_b16 v[42:43], v241 offset:36864\n"
    "s_waitcnt lgkmcnt(8)\n"
    "v_mfma_f32_16x16x32_bf16 v[156:159], v[24:27], v[212:215], v[156:159]\n"
    "ds_read_b64_tr_b16 v[44:45], v242 offset:32768\n"
    "v_mfma_f32_16x16x32_bf16 v[160:163], v[24:27], v[216:219], v[160:163]\n"
    "ds_read_b64_tr_b16 v[46:47], v242 offset:36864\n"
    "s_waitcnt lgkmcnt(8)\n"
    "v_mfma_f32_16x16x32_bf16 v[164:167], v[28:31], v[212:215], v[164:167]\n"
    "s_add_u32 s62, s76, 393344\n"
    "ds_read_b64_tr_b16 v[48:49], v243 offset:32768\n"
    "v_mfma_f32_16x16x32_bf16 v[168:171], v[28:31], v[216:219], v[168:171]\n"
    "s_add_u32 m0, s75, 148480\n"
    "s_nop 0\n"
    "buffer_load_dwordx4 v18, s[8:11], s62 offen lds\n"
    "ds_read_b64_tr_b16 v[50:51], v243 offset:36864\n"
    "s_waitcnt lgkmcnt(8)\n"
    "v_mfma_f32_16x16x32_bf16 v[172:175], v[32:35], v[212:215], v[172:175]\n"
    "ds_read_b64_tr_b16 v[20:21], v244 offset:32768\n"
    "v_mfma_f32_16x16x32_bf16 v[176:179], v[32:35], v[216:219], v[176:179]\n"
    "ds_read_b64_tr_b16 v[22:23], v244 offset:36864\n"
    "s_waitcnt vmcnt(22) lgkmcnt(8)\n"
    "v_mfma_f32_16x16x32_bf16 v[116:119], v[36:39], v[220:223], v[116:119]\n"
    "ds_read_b64_tr_b16 v[24:25], v245 offset:32768\n"
    "s_waitcnt vmcnt(21)\n"
    "v_mfma_f32_16x16x32_bf16 v[120:123], v[36:39], v[224:227], v[120:123]\n"
    "ds_read_b64_tr_b16 v[26:27], v245 offset:36864\n"
    "s_waitcnt lgkmcnt(8)\n"
    "v_mfma_f32_16x16x32_bf16 v[124:127], v[40:43], v[220:223], v[124:127]\n"
    "s_add_u32 s62, s77, 10240\n"
    "ds_read_b64_tr_b16 v[28:29], v246 offset:32768\n"
    "v_mfma_f32_16x16x32_bf16 v[128:131], v[40:43], v[224:227], v[128:131]\n"
    "buffer_load_dwordx4 v[212:215], v15, s[16:19], s62 offen sc1\n"
    "ds_read_b64_tr_b16 v[30:31], v246 offset:36864\n"
    "s_waitcnt lgkmcnt(8)\n"
    "v_mfma_f32_16x16x32_bf16 v[132:135], v[44:47], v[220:223], v[132:135]\n"
    "ds_read_b64_tr_b16 v[32:33], v247 offset:32768\n"
    "v_mfma_f32_16x16x32_bf16 v[136:139], v[44:47], v[224:227], v[136:139]\n"
    "ds_read_b64_tr_b16 v[34:35], v247 offset:36864\n"
    "s_waitcnt vmcnt(15)\n"
    "s_waitcnt lgkmcnt(0)\n"
    "s_barrier\n"
    "s_cmp_lg_u32 s7, 0\n"
    "s_cbranch_scc1 L_nf_g_%=\n"
    "s_lshl_b32 s64, s72, 2\n"
    "s_add_u32 s64, s64, s73\n"
    "s_lshl_b32 s64, s64, 2\n"
    "s_add_u32 s64, s64, s70\n"
    "s_lshl_b32 s64, s64, 3\n"
    "s_add_u32 s64, s64, 40960\n"
    "v_mov_b32 v252, s64\n"
    "v_mov_b32 v254, s84\n"
    "v_mov_b32 v255, s85\n"
    "s_mov_b64 exec, 1\n"
    "global_store_dwordx2 v252, v[254:255], s[58:59] sc1\n"
    "s_mov_b64 exec, -1\n"
    "L_nf_g_%=:\n"
    "v_mfma_f32_16x16x32_bf16 v[140:143], v[48:51], v[220:223], v[140:143]\n"
    "s_add_u32 s62, s77, 142336\n"
    "ds_read_b64_tr_b16 v[36:37], v240 offset:49152\n"
    "v_mfma_f32_16x16x32_bf16 v[144:147], v[48:51], v[224:227], v[144:147]\n"
    "buffer_load_dwordx4 v[216:219], v15, s[16:19], s62 offen sc1\n"
    "ds_read_b64_tr_b16 v[38:39], v240 offset:53248\n"
    "v_mfma_f32_16x16x32_bf16 v[148:151], v[20:23], v[220:223], v[148:151]\n"
    "ds_read_b64_tr_b16 v[40:41], v241 offset:49152\n"
    "v_mfma_f32_16x16x32_bf16 v[152:155], v[20:23], v[224:227], v[152:155]\n"
    "ds_read_b64_tr_b16 v[42:43], v241 offset:53248\n"
    "v_mfma_f32_16x16x32_bf16 v[156:159], v[24:27], v[220:223], v[156:159]\n"
    "s_add_u32 s62, s76, 425984\n"
    "ds_read_b64_tr_b16 v[44:45], v242 offset:49152\n"
    "v_mfma_f32_16x16x32_bf16 v[160:163], v[24:27], v[224:227], v[160:163]\n"
    "s_add_u32 m0, s75, 0\n"
    "s_nop 0\n"
    "buffer_load_dwordx4 v18, s[8:11], s62 offen lds\n"
    "ds_read_b64_tr_b16 v[46:47], v242 offset:53248\n"
    "v_mfma_f32_16x16x32_bf16 v[164:167], v[28:31], v[220:223], v[164:167]\n"
    "ds_read_b64_tr_b16 v[48:49], v243 offset:49152\n"
    "v_mfma_f32_16x16x32_bf16 v[168:171], v[28:31], v[224:227], v[168:171]\n"
    "ds_read_b64_tr_b16 v[50:51], v243 offset:53248\n"
    "v_mfma_f32_16x16x32_bf16 v[172:175], v[32:35], v[220:223], v[172:175]\n"
    "ds_read_b64_tr_b16 v[20:21], v244 offset:49152\n"
    "v_mfma_f32_16x16x32_bf16 v[176:179], v[32:35], v[224:227], v[176:179]\n"
    "ds_read_b64_tr_b16 v[22:23], v244 offset:53248\n"
    "s_waitcnt lgkmcnt(8)\n"
    "v_mfma_f32_16x16x32_bf16 v[116:119], v[36:39], v[180:183], v[116:119]\n"
    "s_add_u32 s62, s76, 426112\n"
    "ds_read_b64_tr_b16 v[24:25], v245 offset:49152\n"
    "v_mfma_f32_16x16x32_bf16 v[120:123], v[36:39], v[184:187], v[120:123]\n"
    "s_add_u32 m0, s75, 1024\n"
    "s_nop 0\n"
    "buffer_load_dwordx4 v18, s[8:11], s62 offen lds\n"
    "ds_read_b64_tr_b16 v[26:27], v245 offset:53248\n"
    "s_waitcnt lgkmcnt(8)\n"
    "v_mfma_f32_16x16x32_bf16 v[124:127], v[40:43], v[180:183], v[124:127]\n"
    "s_add_u32 s62, s77, 11264\n"
    "ds_read_b64_tr_b16 v[28:29], v246 offset:49152\n"
    "v_mfma_f32_16x16x32_bf16 v[128:131], v[40:43], v[184:187], v[128:131]\n"
    "buffer_load_dwordx4 v[220:223], v15, s[16:19], s62 offen sc1\n"
    "ds_read_b64_tr_b16 v[30:31], v246 offset:53248\n"
    "s_waitcnt lgkmcnt(8)\n"
    "v_mfma_f32_16x16x32_bf16 v[132:135], v[44:47], v[180:183], v[132:135]\n"
    "ds_read_b64_tr_b16 v[32:33], v247 offset:49152\n"
    "v_mfma_f32_16x16x32_bf16 v[136:139], v[44:47], v[184:187], v[136:139]\n"
    "ds_read_b64_tr_b16 v[34:35], v247 offset:53248\n"
    "v_add_u32 v240, 65536, v240\n"
    "v_add_u32 v241, 65536, v241\n"
    "v_add_u32 v242, 65536, v242\n"
    "v_add_u32 v243, 65536, v243\n"
    "v_add_u32 v244, 65536, v244\n"
    "v_add_u32 v245, 65536, v245\n"
    "v_add_u32 v246, 65536, v246\n"
    "v_add_u32 v247, 65536, v247\n"
    "s_waitcnt lgkmcnt(8)\n"
    "v_mfma_f32_16x16x32_bf16 v[140:143], v[48:51], v[180:183], v[140:143]\n"
    "s_add_u32 s62, s77, 143360\n"
    "buffer_load_dwordx4 v[224:227], v15, s[16:19], s62 offen sc1\n"
    "ds_read_b64_tr_b16 v[36:37], v240 offset:0\n"
    "v_mfma_f32_16x16x32_bf16 v[144:147], v[48:51], v[184:187], v[144:147]\n"
    "s_add_u32 s62, s76, 458752\n"
    "s_add_u32 m0, s75, 16384\n"
    "s_nop 0\n"
    "buffer_load_dwordx4 v18, s[8:11], s62 offen lds\n"
    "ds_read_b64_tr_b16 v[38:39], v240 offset:4096\n"
    "s_waitcnt lgkmcnt(8)\n"
    "v_mfma_f32_16x16x32_bf16 v[148:151], v[20:23], v[180:183], v[148:151]\n"
    "ds_read_b64_tr_b16 v[40:41], v241 offset:0\n"
    "v_mfma_f32_16x16x32_bf16 v[152:155], v[20:23], v[184:187], v[152:155]\n"
    "ds_read_b64_tr_b16 v[42:43], v241 offset:4096\n"
    "s_waitcnt lgkmcnt(8)\n"
    "v_mfma_f32_16x16x32_bf16 v[156:159], v[24:27], v[180:183], v[156:159]\n"
    "ds_read_b64_tr_b16 v[44:45], v242 offset:0\n"
    "v_mfma_f32_16x16x32_bf16 v[160:163], v[24:27], v[184:187], v[160:163]\n"
    "ds_read_b64_tr_b16 v[46:47], v242 offset:4096\n"
    "s_waitcnt lgkmcnt(8)\n"
    "v_mfma_f32_16x16x32_bf16 v[164:167], v[28:31], v[180:183], v[164:167]\n"
    "ds_read_b64_tr_b16 v[48:49], v243 offset:0\n"
    "v_mfma_f32_16x16x32_bf16 v[168:171], v[28:31], v[184:187], v[168:171]\n"
    "ds_read_b64_tr_b16 v[50:51], v243 offset:4096\n"
    "s_waitcnt lgkmcnt(8)\n"
    "v_mfma_f32_16x16x32_bf16 v[172:175], v[32:35], v[180:183], v[172:175]\n"
    "s_add_u32 s62, s76, 458880\n"
    "ds_read_b64_tr_b16 v[20:21], v244 offset:0\n"
    "v_mfma_f32_16x16x32_bf16 v[176:179], v[32:35], v[184:187], v[176:179]\n"
    "s_add_u32 m0, s75, 17408\n"
    "s_nop 0\n"
    "buffer_load_dwordx4 v18, s[8:11], s62 offen lds\n"
    "ds_read_b64_tr_b16 v[22:23], v244 offset:4096\n"
    "s_waitcnt vmcnt(21) lgkmcnt(8)\n"
    "v_mfma_f32_16x16x32_bf16 v[116:119], v[36:39], v[188:191], v[116:119]\n"
    "ds_read_b64_tr_b16 v[24:25], v245 offset:0\n"
    "s_waitcnt vmcnt(20)\n"
    "v_mfma_f32_16x16x32_bf16 v[120:123], v[36:39], v[192:195], v[120:123]\n"
    "ds_read_b64_tr_b16 v[26:27], v245 offset:4096\n"
    "s_waitcnt lgkmcnt(8)\n"
    "v_mfma_f32_16x16x32_bf16 v[124:127], v[40:43], v[188:191], v[124:127]\n"
    "s_add_u32 s62, s77, 12288\n"
    "ds_read_b64_tr_b16 v[28:29], v246 offset:0\n"
    "v_mfma_f32_16x16x32_bf16 v[128:131], v[40:43], v[192:195], v[128:131]\n"
    "buffer_load_dwordx4 v[180:183], v15, s[16:19], s62 offen sc1\n"
    "ds_read_b64_tr_b16 v[30:31], v246 offset:4096\n"
    "s_waitcnt lgkmcnt(8)\n"
    "v_mfma_f32_16x16x32_bf16 v[132:135], v[44:47], v[188:191], v[132:135]\n"
    "s_add_u32 s62, s76, 491520\n"
    "ds_read_b64_tr_b16 v[32:33], v247 offset:0\n"
    "v_mfma_f32_16x16x32_bf16 v[136:139], v[44:47], v[192:195], v[136:139]\n"
    "s_add_u32 m0, s75, 32768\n"
    "s_nop 0\n"
    "buffer_load_dwordx4 v18, s[8:11], s62 offen lds\n"
    "ds_read_b64_tr_b16 v[34:35], v247 offset:4096\n"
    "s_waitcnt lgkmcnt(8)\n"
    "v_mfma_f32_16x16x32_bf16 v[140:143], v[48:51], v[188:191], v[140:143]\n"
    "s_add_u32 s62, s77, 144384\n"
    "ds_read_b64_tr_b16 v[36:37], v240 offset:16384\n"
    "v_mfma_f32_16x16x32_bf16 v[144:147], v[48:51], v[192:195], v[144:147]\n"
    "buffer_load_dwordx4 v[184:187], v15, s[16:19], s62 offen sc1\n"
    "ds_read_b64_tr_b16 v[38:39], v240 offset:20480\n"
    "s_waitcnt lgkmcnt(8)\n"
    "v_mfma_f32_16x16x32_bf16 v[148:151], v[20:23], v[188:191], v[148:151]\n"
    "ds_read_b64_tr_b16 v[40:41], v241 offset:16384\n"
    "v_mfma_f32_16x16x32_bf16 v[152:155], v[20:23], v[192:195], v[152:155]\n"
    "ds_read_b64_tr_b16 v[42:43], v241 offset:20480\n"
    "s_waitcnt lgkmcnt(8)\n"
    "v_mfma_f32_16x16x32_bf16 v[156:159], v[24:27], v[188:191], v[156:159]\n"
    "s_add_u32 s62, s76, 491648\n"
    "ds_read_b64_tr_b16 v[44:45], v242 offset:16384\n"
    "v_mfma_f32_16x16x32_bf16 v[160:163], v[24:27], v[192:195], v[160:163]\n"
    "s_add_u32 m0, s75, 33792\n"
    "s_nop 0\n"
    "buffer_load_dwordx4 v18, s[8:11], s62 offen lds\n"
    "ds_read_b64_tr_b16 v[46:47], v242 offset:20480\n"
    "s_waitcnt lgkmcnt(8)\n"
    "v_mfma_f32_16x16x32_bf16 v[164:167], v[28:31], v[188:191], v[164:167]\n"
    "ds_read_b64_tr_b16 v[48:49], v243 offset:16384\n"
    "v_mfma_f32_16x16x32_bf16 v[168:171], v[28:31], v[192:195], v[168:171]\n"
    "ds_read_b64_tr_b16 v[50:51], v243 offset:20480\n"
    "s_waitcnt lgkmcnt(8)\n"
    "v_mfma_f32_16x16x32_bf16 v[172:175], v[32:35], v[188:191], v[172:175]\n"
    "ds_read_b64_tr_b16 v[20:21], v244 offset:16384\n"
    "v_mfma_f32_16x16x32_bf16 v[176:179], v[32:35], v[192:195], v[176:179]\n"
    "ds_read_b64_tr_b16 v[22:23], v244 offset:20480\n"
    "s_waitcnt vmcnt(21) lgkmcnt(8)\n"
    "v_mfma_f32_16x16x32_bf16 v[116:119], v[36:39], v[196:199], v[116:119]\n"
    "ds_read_b64_tr_b16 v[24:25], v245 offset:16384\n"
    "s_waitcnt vmcnt(19)\n"
    "v_mfma_f32_16x16x32_bf16 v[120:123], v[36:39], v[200:203], v[120:123]\n"
    "ds_read_b64_tr_b16 v[26:27], v245 offset:20480\n"
    "s_waitcnt lgkmcnt(8)\n"
    "v_mfma_f32_16x16x32_bf16 v[124:127], v[40:43], v[196:199], v[124:127]\n"
    "s_add_u32 s62, s77, 13312\n"
    "ds_read_b64_tr_b16 v[28:29], v246 offset:16384\n"
    "v_mfma_f32_16x16x32_bf16 v[128:131], v[40:43], v[200:203], v[128:131]\n"
    "buffer_load_dwordx4 v[188:191], v15, s[16:19], s62 offen sc1\n"
    "ds_read_b64_tr_b16 v[30:31], v246 offset:20480\n"
    "s_waitcnt lgkmcnt(8)\n"
    "v_mfma_f32_16x16x32_bf16 v[132:135], v[44:47], v[196:199], v[132:135]\n"
    "ds_read_b64_tr_b16 v[32:33], v247 offset:16384\n"
    "v_mfma_f32_16x16x32_bf16 v[136:139], v[44:47], v[200:203], v[136:139]\n"
    "ds_read_b64_tr_b16 v[34:35], v247 offset:20480\n"
    "s_waitcnt vmcnt(13)\n"
    "s_waitcnt lgkmcnt(0)\n"
    "s_barrier\n"
    "v_mfma_f32_16x16x32_bf16 v[140:143], v[48:51], v[196:199], v[140:143]\n"
    "s_add_u32 s62, s77, 145408\n"
    "ds_read_b64_tr_b16 v[36:37], v240 offset:32768\n"
    "v_mfma_f32_16x16x32_bf16 v[144:147], v[48:51], v[200:203], v[144:147]\n"
    "buffer_load_dwordx4 v[192:195], v15, s[16:19], s62 offen sc1\n"
    "ds_read_b64_tr_b16 v[38:39], v240 offset:36864\n"
    "v_mfma_f32_16x16x32_bf16 v[148:151], v[20:23], v[196:199], v[148:151]\n"
    "ds_read_b64_tr_b16 v[40:41], v241 offset:32768\n"
    "v_mfma_f32_16x16x32_bf16 v[152:155], v[20:23], v[200:203], v[152:155]\n"
    "ds_read_b64_tr_b16 v[42:43], v241 offset:36864\n"
    "v_mfma_f32_16x16x32_bf16 v[156:159], v[24:27], v[196:199], v[156:159]\n"
    "ds_read_b64_tr_b16 v[44:45], v242 offset:32768\n"
    "v_mfma_f32_16x16x32_bf16 v[160:163], v[24:27], v[200:203], v[160:163]\n"
    "ds_read_b64_tr_b16 v[46:47], v242 offset:36864\n"
    "v_mfma_f32_16x16x32_bf16 v[164:167], v[28:31], v[196:199], v[164:167]\n"
    "ds_read_b64_tr_b16 v[48:49], v243 offset:32768\n"
    "v_mfma_f32_16x16x32_bf16 v[168:171], v[28:31], v[200:203], v[168:171]\n"
    "ds_read_b64_tr_b16 v[50:51], v243 offset:36864\n"
    "v_mfma_f32_16x16x32_bf16 v[172:175], v[32:35], v[196:199], v[172:175]\n"
    "ds_read_b64_tr_b16 v[20:21], v244 offset:32768\n"
    "v_mfma_f32_16x16x32_bf16 v[176:179], v[32:35], v[200:203], v[176:179]\n"
    "ds_read_b64_tr_b16 v[22:23], v244 offset:36864\n"
    "buffer_load_dwordx2 v[76:77], v16, s[24:27], 0 offen\n"
    "s_waitcnt lgkmcnt(8)\n"
    "v_mfma_f32_16x16x32_bf16 v[116:119], v[36:39], v[204:207], v[116:119]\n"
    "ds_read_b64_tr_b16 v[24:25], v245 offset:32768\n"
    "v_mfma_f32_16x16x32_bf16 v[120:123], v[36:39], v[208:211], v[120:123]\n"
    "ds_read_b64_tr_b16 v[26:27], v245 offset:36864\n"
    "s_waitcnt lgkmcnt(8)\n"
    "v_mfma_f32_16x16x32_bf16 v[124:127], v[40:43], v[204:207], v[124:127]\n"
    "s_add_u32 s62, s77, 14336\n"
    "ds_read_b64_tr_b16 v[28:29], v246 offset:32768\n"
    "v_mfma_f32_16x16x32_bf16 v[128:131], v[40:43], v[208:211], v[128:131]\n"
    "buffer_load_dwordx4 v[196:199], v15, s[16:19], s62 offen sc1\n"
    "ds_read_b64_tr_b16 v[30:31], v246 offset:36864\n"
    "s_waitcnt lgkmcnt(8)\n"
    "v_mfma_f32_16x16x32_bf16 v[132:135], v[44:47], v[204:207], v[132:135]\n"
    "ds_read_b64_tr_b16 v[32:33], v247 offset:32768\n"
    "v_mfma_f32_16x16x32_bf16 v[136:139], v[44:47], v[208:211], v[136:139]\n"
    "ds_read_b64_tr_b16 v[34:35], v247 offset:36864\n"
    "v_add_u32 v240, 49152, v240\n"
    "v_add_u32 v241, 49152, v241\n"
    "v_add_u32 v242, 49152, v242\n"
    "v_add_u32 v243, 49152, v243\n"
    "v_add_u32 v244, 49152, v244\n"
    "v_add_u32 v245, 49152, v245\n"
    "v_add_u32 v246, 49152, v246\n"
    "v_add_u32 v247, 49152, v247\n"
    "s_waitcnt lgkmcnt(8)\n"
    "v_mfma_f32_16x16x32_bf16 v[140:143], v[48:51], v[204:207], v[140:143]\n"
    "s_add_u32 s62, s77, 146432\n"
    "ds_read_b64_tr_b16 v[36:37], v241 offset:0\n"
    "v_mfma_f32_16x16x32_bf16 v[144:147], v[48:51], v[208:211], v[144:147]\n"
    "buffer_load_dwordx4 v[200:203], v15, s[16:19], s62 offen sc1\n"
    "buffer_load_dwordx2 v[78:79], v16, s[24:27], s78 offen\n"
    "ds_read_b64_tr_b16 v[38:39], v241 offset:4096\n"
    "s_waitcnt lgkmcnt(8)\n"
    "v_mfma_f32_16x16x32_bf16 v[148:151], v[20:23], v[204:207], v[148:151]\n"
    "ds_read_b64_tr_b16 v[40:41], v242 offset:0\n"
    "v_mfma_f32_16x16x32_bf16 v[152:155], v[20:23], v[208:211], v[152:155]\n"
    "ds_read_b64_tr_b16 v[42:43], v242 offset:4096\n"
    "s_waitcnt lgkmcnt(8)\n"
    "v_mfma_f32_16x16x32_bf16 v[156:159], v[24:27], v[204:207], v[156:159]\n"
    "ds_read_b64_tr_b16 v[44:45], v243 offset:0\n"
    "v_mfma_f32_16x16x32_bf16 v[160:163], v[24:27], v[208:211], v[160:163]\n"
    "ds_read_b64_tr_b16 v[46:47], v243 offset:4096\n"
    "s_waitcnt lgkmcnt(8)\n"
    "v_mfma_f32_16x16x32_bf16 v[164:167], v[28:31], v[204:207], v[164:167]\n"
    "ds_read_b64_tr_b16 v[48:49], v245 offset:0\n"
    "v_mfma_f32_16x16x32_bf16 v[168:171], v[28:31], v[208:211], v[168:171]\n"
    "ds_read_b64_tr_b16 v[50:51], v245 offset:4096\n"
    "s_waitcnt lgkmcnt(8)\n"
    "v_mfma_f32_16x16x32_bf16 v[172:175], v[32:35], v[204:207], v[172:175]\n"
    "ds_read_b64_tr_b16 v[20:21], v246 offset:0\n"
    "v_mfma_f32_16x16x32_bf16 v[176:179], v[32:35], v[208:211], v[176:179]\n"
    "ds_read_b64_tr_b16 v[22:23], v246 offset:4096\n"
    "s_waitcnt vmcnt(17) lgkmcnt(8)\n"
    "v_mfma_f32_16x16x32_bf16 v[124:127], v[36:39], v[212:215], v[124:127]\n"
    "ds_read_b64_tr_b16 v[24:25], v247 offset:0\n"
    "s_waitcnt vmcnt(16)\n"
    "v_mfma_f32_16x16x32_bf16 v[128:131], v[36:39], v[216:219], v[128:131]\n"
    "ds_read_b64_tr_b16 v[26:27], v247 offset:4096\n"
    "s_waitcnt lgkmcnt(8)\n"
    "v_mfma_f32_16x16x32_bf16 v[132:135], v[40:43], v[212:215], v[132:135]\n"
    "s_add_u32 s62, s77, 15360\n"
    "ds_read_b64_tr_b16 v[28:29], v241 offset:16384\n"
    "v_mfma_f32_16x16x32_bf16 v[136:139], v[40:43], v[216:219], v[136:139]\n"
    "buffer_load_dwordx4 v[204:207], v15, s[16:19], s62 offen sc1\n"
    "ds_read_b64_tr_b16 v[30:31], v241 offset:20480\n"
    "s_waitcnt lgkmcnt(8)\n"
    "v_mfma_f32_16x16x32_bf16 v[140:143], v[44:47], v[212:215], v[140:143]\n"
    "ds_read_b64_tr_b16 v[32:33], v242 offset:16384\n"
    "v_mfma_f32_16x16x32_bf16 v[144:147], v[44:47], v[216:219], v[144:147]\n"
    "ds_read_b64_tr_b16 v[34:35], v242 offset:20480\n"
    "s_waitcnt lgkmcnt(8)\n"
    "v_mfma_f32_16x16x32_bf16 v[156:159], v[48:51], v[212:215], v[156:159]\n"
    "ds_read_b64_tr_b16 v[36:37], v243 offset:16384\n"
    "v_mfma_f32_16x16x32_bf16 v[160:163], v[48:51], v[216:219], v[160:163]\n"
    "ds_read_b64_tr_b16 v[38:39], v243 offset:20480\n"
    "s_add_u32 s62, s77, 147456\n"
    "buffer_load_dwordx4 v[208:211], v15, s[16:19], s62 offen sc1\n"
    "s_or_b32 s62, s6, s7\n"
    "s_cmp_lg_u32 s62, 0\n"
    "s_cbranch_scc1 L_ne_%=\n"
    "v_lshrrev_b32 v6, 1, v15\n"
    "v_add_u32 v6, 4096, v6\n"
    "global_load_dwordx2 v[104:105], v6, s[58:59] offset:0 sc1\n"
    "global_load_dwordx2 v[106:107], v6, s[58:59] offset:512 sc1\n"
    "global_load_dwordx2 v[108:109], v6, s[58:59] offset:1024 sc1\n"
    "global_load_dwordx2 v[110:111], v6, s[58:59] offset:1536 sc1\n"
    "L_ne_%=:\n"
    "s_waitcnt lgkmcnt(8)\n"
    "v_mfma_f32_16x16x32_bf16 v[164:167], v[20:23], v[212:215], v[164:167]\n"
    "ds_read_b64_tr_b16 v[40:41], v245 offset:16384\n"
    "v_mfma_f32_16x16x32_bf16 v[168:171], v[20:23], v[216:219], v[168:171]\n"
    "ds_read_b64_tr_b16 v[42:43], v245 offset:20480\n"
    "s_waitcnt lgkmcnt(8)\n"
    "v_mfma_f32_16x16x32_bf16 v[172:175], v[24:27], v[212:215], v[172:175]\n"
    "ds_read_b64_tr_b16 v[44:45], v246 offset:16384\n"
    "v_mfma_f32_16x16x32_bf16 v[176:179], v[24:27], v[216:219], v[176:179]\n"
    "ds_read_b64_tr_b16 v[46:47], v246 offset:20480\n"
    "s_waitcnt vmcnt(15) lgkmcnt(8)\n"
    "v_mfma_f32_16x16x32_bf16 v[124:127], v[28:31], v[220:223], v[124:127]\n"
    "ds_read_b64_tr_b16 v[48:49], v247 offset:16384\n"
    "s_waitcnt vmcnt(14)\n"
    "v_mfma_f32_16x16x32_bf16 v[128:131], v[28:31], v[224:227], v[128:131]\n"
    "ds_read_b64_tr_b16 v[50:51], v247 offset:20480\n"
    "s_waitcnt lgkmcnt(8)\n"
    "v_mfma_f32_16x16x32_bf16 v[132:135], v[32:35], v[220:223], v[132:135]\n"
    "ds_read_b64_tr_b16 v[20:21], v241 offset:32768\n"
    "v_mfma_f32_16x16x32_bf16 v[136:139], v[32:35], v[224:227], v[136:139]\n"
    "ds_read_b64_tr_b16 v[22:23], v241 offset:36864\n"
    "global_load_dwordx2 v[252:253], v11, s[58:59] sc1\n"
    "s_waitcnt lgkmcnt(8)\n"
    "v_mfma_f32_16x16x32_bf16 v[140:143], v[36:39], v[220:223], v[140:143]\n"
    "ds_read_b64_tr_b16 v[24:25], v242 offset:32768\n"
    "v_mfma_f32_16x16x32_bf16 v[144:147], v[36:39], v[224:227], v[144:147]\n"
    "ds_read_b64_tr_b16 v[26:27], v242 offset:36864\n"
    "s_waitcnt lgkmcnt(8)\n"
    "v_mfma_f32_16x16x32_bf16 v[156:159], v[40:43], v[220:223], v[156:159]\n"
    "ds_read_b64_tr_b16 v[28:29], v243 offset:32768\n"
    "v_mfma_f32_16x16x32_bf16 v[160:163], v[40:43], v[224:227], v[160:163]\n"
    "ds_read_b64_tr_b16 v[30:31], v243 offset:36864\n"
    "s_waitcnt lgkmcnt(8)\n"
    "v_mfma_f32_16x16x32_bf16 v[164:167], v[44:47], v[220:223], v[164:167]\n"
    "ds_read_b64_tr_b16 v[32:33], v245 offset:32768\n"
    "v_mfma_f32_16x16x32_bf16 v[168:171], v[44:47], v[224:227], v[168:171]\n"
    "ds_read_b64_tr_b16 v[34:35], v245 offset:36864\n"
    "s_waitcnt lgkmcnt(8)\n"
    "v_mfma_f32_16x16x32_bf16 v[172:175], v[48:51], v[220:223], v[172:175]\n"
    "ds_read_b64_tr_b16 v[36:37], v246 offset:32768\n"
    "v_mfma_f32_16x16x32_bf16 v[176:179], v[48:51], v[224:227], v[176:179]\n"
    "ds_read_b64_tr_b16 v[38:39], v246 offset:36864\n"
    "s_waitcnt vmcnt(12) lgkmcnt(8)\n"
    "v_mfma_f32_16x16x32_bf16 v[124:127], v[20:23], v[180:183], v[124:127]\n"
    "ds_read_b64_tr_b16 v[40:41], v247 offset:32768\n"
    "s_waitcnt vmcnt(10)\n"
    "v_mfma_f32_16x16x32_bf16 v[128:131], v[20:23], v[184:187], v[128:131]\n"
    "ds_read_b64_tr_b16 v[42:43], v247 offset:36864\n"
    "v_subrev_u32 v240, 114688, v240\n"
    "v_subrev_u32 v241, 114688, v241\n"
    "v_subrev_u32 v242, 114688, v242\n"
    "v_subrev_u32 v243, 114688, v243\n"
    "v_subrev_u32 v244, 114688, v244\n"
    "v_subrev_u32 v245, 114688, v245\n"
    "v_subrev_u32 v246, 114688, v246\n"
    "v_subrev_u32 v247, 114688, v247\n"
    "s_waitcnt vmcnt(9)\n"
    "s_waitcnt lgkmcnt(0)\n"
    "s_barrier\n"
    "v_mfma_f32_16x16x32_bf16 v[132:135], v[24:27], v[180:183], v[132:135]\n"
    "ds_read_b64_tr_b16 v[44:45], v241 offset:0\n"
    "v_mfma_f32_16x16x32_bf16 v[136:139], v[24:27], v[184:187], v[136:139]\n"
    "ds_read_b64_tr_b16 v[46:47], v241 offset:4096\n"
    "v_mfma_f32_16x16x32_bf16 v[140:143], v[28:31], v[180:183], v[140:143]\n"
    "ds_read_b64_tr_b16 v[48:49], v242 offset:0\n"
    "v_mfma_f32_16x16x32_bf16 v[144:147], v[28:31], v[184:187], v[144:147]\n"
    "ds_read_b64_tr_b16 v[50:51], v242 offset:4096\n"
    "v_mfma_f32_16x16x32_bf16 v[156:159], v[32:35], v[180:183], v[156:159]\n"
    "ds_read_b64_tr_b16 v[20:21], v243 offset:0\n"
    "v_mfma_f32_16x16x32_bf16 v[160:163], v[32:35], v[184:187], v[160:163]\n"
    "ds_read_b64_tr_b16 v[22:23], v243 offset:4096\n"
    "v_mfma_f32_16x16x32_bf16 v[164:167], v[36:39], v[180:183], v[164:167]\n"
    "ds_read_b64_tr_b16 v[24:25], v245 offset:0\n"
    "v_mfma_f32_16x16x32_bf16 v[168:171], v[36:39], v[184:187], v[168:171]\n"
    "ds_read_b64_tr_b16 v[26:27], v245 offset:4096\n"
    "v_mfma_f32_16x16x32_bf16 v[172:175], v[40:43], v[180:183], v[172:175]\n"
    "ds_read_b64_tr_b16 v[28:29], v246 offset:0\n"
    "v_mfma_f32_16x16x32_bf16 v[176:179], v[40:43], v[184:187], v[176:179]\n"
    "ds_read_b64_tr_b16 v[30:31], v246 offset:4096\n"
    "s_waitcnt vmcnt(8) lgkmcnt(8)\n"
    "v_mfma_f32_16x16x32_bf16 v[124:127], v[44:47], v[188:191], v[124:127]\n"
    "ds_read_b64_tr_b16 v[32:33], v247 offset:0\n"
    "s_waitcnt vmcnt(7)\n"
    "v_mfma_f32_16x16x32_bf16 v[128:131], v[44:47], v[192:195], v[128:131]\n"
    "ds_read_b64_tr_b16 v[34:35], v247 offset:4096\n"
    "s_waitcnt lgkmcnt(8)\n"
    "v_mfma_f32_16x16x32_bf16 v[132:135], v[48:51], v[188:191], v[132:135]\n"
    "ds_read_b64_tr_b16 v[36:37], v241 offset:16384\n"
    "v_mfma_f32_16x16x32_bf16 v[136:139], v[48:51], v[192:195], v[136:139]\n"
    "ds_read_b64_tr_b16 v[38:39], v241 offset:20480\n"
    "s_waitcnt lgkmcnt(8)\n"
    "v_mfma_f32_16x16x32_bf16 v[140:143], v[20:23], v[188:191], v[140:143]\n"
    "ds_read_b64_tr_b16 v[40:41], v242 offset:16384\n"
    "v_mfma_f32_16x16x32_bf16 v[144:147], v[20:23], v[192:195], v[144:147]\n"
    "ds_read_b64_tr_b16 v[42:43], v242 offset:20480\n"
    "s_waitcnt lgkmcnt(8)\n"
    "v_mfma_f32_16x16x32_bf16 v[156:159], v[24:27], v[188:191], v[156:159]\n"
    "ds_read_b64_tr_b16 v[44:45], v243 offset:16384\n"
    "v_mfma_f32_16x16x32_bf16 v[160:163], v[24:27], v[192:195], v[160:163]\n"
    "ds_read_b64_tr_b16 v[46:47], v243 offset:20480\n"
    "s_waitcnt vmcnt(0)\n"
    "v_cmp_ne_u64 vcc, s[84:85], v[252:253]\n"
    "s_nop 0\n"
    "s_cbranch_vccnz L_gretry_%=\n"
    "L_gok_%=:\n"
    "buffer_load_dwordx4 v[92:95], v15, s[40:43], s47 offen sc1\n"
    "buffer_load_dwordx4 v[96:99], v15, s[40:43], s48 offen sc1\n"
    "buffer_load_dwordx4 v[100:103], v15, s[40:43], s49 offen sc1\n"
    "s_waitcnt lgkmcnt(8)\n"
    "v_mfma_f32_16x16x32_bf16 v[164:167], v[28:31], v[188:191], v[164:167]\n"
    "ds_read_b64_tr_b16 v[48:49], v245 offset:16384\n"
    "v_mfma_f32_16x16x32_bf16 v[168:171], v[28:31], v[192:195], v[168:171]\n"
    "ds_read_b64_tr_b16 v[50:51], v245 offset:20480\n"
    "s_waitcnt lgkmcnt(8)\n"
    "v_mfma_f32_16x16x32_bf16 v[172:175], v[32:35], v[188:191], v[172:175]\n"
    "ds_read_b64_tr_b16 v[20:21], v246 offset:16384\n"
    "v_mfma_f32_16x16x32_bf16 v[176:179], v[32:35], v[192:195], v[176:179]\n"
    "ds_read_b64_tr_b16 v[22:23], v246 offset:20480\n"
    "s_waitcnt lgkmcnt(8)\n"
    "v_mfma_f32_16x16x32_bf16 v[124:127], v[36:39], v[196:199], v[124:127]\n"
    "ds_read_b64_tr_b16 v[24:25], v247 offset:16384\n"
    "v_mfma_f32_16x16x32_bf16 v[128:131], v[36:39], v[200:203], v[128:131]\n"
    "ds_read_b64_tr_b16 v[26:27], v247 offset:20480\n"
    "s_waitcnt lgkmcnt(8)\n"
    "v_mfma_f32_16x16x32_bf16 v[132:135], v[40:43], v[196:199], v[132:135]\n"
    "ds_read_b64_tr_b16 v[28:29], v245 offset:32768\n"
    "v_mfma_f32_16x16x32_bf16 v[136:139], v[40:43], v[200:203], v[136:139]\n"
    "ds_read_b64_tr_b16 v[30:31], v245 offset:36864\n"
    "s_waitcnt lgkmcnt(8)\n"
    "v_mfma_f32_16x16x32_bf16 v[140:143], v[44:47], v[196:199], v[140:143]\n"
    "ds_read_b64_tr_b16 v[32:33], v246 offset:32768\n"
    "v_mfma_f32_16x16x32_bf16 v[144:147], v[44:47], v[200:203], v[144:147]\n"
    "ds_read_b64_tr_b16 v[34:35], v246 offset:36864\n"
    "s_waitcnt lgkmcnt(8)\n"
    "v_mfma_f32_16x16x32_bf16 v[156:159], v[48:51], v[196:199], v[156:159]\n"
    "ds_read_b64_tr_b16 v[36:37], v247 offset:32768\n"
    "v_mfma_f32_16x16x32_bf16 v[160:163], v[48:51], v[200:203], v[160:163]\n"
    "ds_read_b64_tr_b16 v[38:39], v247 offset:36864\n"
    "s_waitcnt lgkmcnt(8)\n"
    "v_mfma_f32_16x16x32_bf16 v[164:167], v[20:23], v[196:199], v[164:167]\n"
    "ds_read_b64_tr_b16 v[40:41], v241 offset:32768\n"
    "v_mfma_f32_16x16x32_bf16 v[168:171], v[20:23], v[200:203], v[168:171]\n"
    "ds_read_b64_tr_b16 v[42:43], v241 offset:36864\n"
    "s_waitcnt lgkmcnt(8)\n"
    "v_mfma_f32_16x16x32_bf16 v[172:175], v[24:27], v[196:199], v[172:175]\n"
    "ds_read_b64_tr_b16 v[44:45], v242 offset:32768\n"
    "v_mfma_f32_16x16x32_bf16 v[176:179], v[24:27], v[200:203], v[176:179]\n"
    "ds_read_b64_tr_b16 v[46:47], v242 offset:36864\n"
    "s_waitcnt lgkmcnt(8)\n"
    "v_mfma_f32_16x16x32_bf16 v[156:159], v[28:31], v[204:207], v[156:159]\n"
    "ds_read_b64_tr_b16 v[48:49], v243 offset:32768\n"
    "v_mfma_f32_16x16x32_bf16 v[160:163], v[28:31], v[208:211], v[160:163]\n"
    "ds_read_b64_tr_b16 v[50:51], v243 offset:36864\n"
    "s_waitcnt lgkmcnt(8)\n"
    "v_mfma_f32_16x16x32_bf16 v[164:167], v[32:35], v[204:207], v[164:167]\n"
    "v_mfma_f32_16x16x32_bf16 v[168:171], v[32:35], v[208:211], v[168:171]\n"
    "s_waitcnt lgkmcnt(6)\n"
    "v_mfma_f32_16x16x32_bf16 v[172:175], v[36:39], v[204:207], v[172:175]\n"
    "v_mfma_f32_16x16x32_bf16 v[176:179], v[36:39], v[208:211], v[176:179]\n"
    "s_waitcnt lgkmcnt(4)\n"
    "v_mfma_f32_16x16x32_bf16 v[124:127], v[40:43], v[204:207], v[124:127]\n"
    "v_mfma_f32_16x16x32_bf16 v[128:131], v[40:43], v[208:211], v[128:131]\n"
    "ds_write_b128 v12, v[156:159] offset:2048\n"
    "ds_write_b128 v12, v[160:163] offset:3072\n"
    "s_waitcnt lgkmcnt(4)\n"
    "v_mfma_f32_16x16x32_bf16 v[132:135], v[44:47], v[204:207], v[132:135]\n"
    "v_mfma_f32_16x16x32_bf16 v[136:139], v[44:47], v[208:211], v[136:139]\n"
    "ds_write_b128 v12, v[164:167] offset:4096\n"
    "ds_write_b128 v12, v[168:171] offset:5120\n"
    "s_waitcnt lgkmcnt(4)\n"
    "v_mfma_f32_16x16x32_bf16 v[140:143], v[48:51], v[204:207], v[140:143]\n"
    "v_mfma_f32_16x16x32_bf16 v[144:147], v[48:51], v[208:211], v[144:147]\n"
    "ds_write_b128 v12, v[172:175] offset:6144\n"
    "ds_write_b128 v12, v[176:179] offset:7168\n"
    "s_waitcnt lgkmcnt(0)\n"
    "s_barrier\n"
    "ds_read_b128 v[156:159], v13 offset:2048\n"
    "ds_read_b128 v[160:163], v13 offset:3072\n"
    "ds_read_b128 v[164:167], v13 offset:4096\n"
    "ds_read_b128 v[168:171], v13 offset:5120\n"
    "ds_read_b128 v[172:175], v13 offset:6144\n"
    "ds_read_b128 v[176:179], v13 offset:7168\n"
    "ds_read_b64_tr_b16 v[20:21], v244 offset:0\n"
    "ds_read_b64_tr_b16 v[22:23], v244 offset:4096\n"
    "ds_read_b64_tr_b16 v[24:25], v240 offset:0\n"
    "ds_read_b64_tr_b16 v[26:27], v240 offset:4096\n"
    "ds_read_b64_tr_b16 v[28:29], v244 offset:16384\n"
    "ds_read_b64_tr_b16 v[30:31], v244 offset:20480\n"
    "ds_read_b64_tr_b16 v[32:33], v240 offset:16384\n"
    "ds_read_b64_tr_b16 v[34:35], v240 offset:20480\n"
    "s_waitcnt lgkmcnt(13)\n"
    "v_pk_add_f32 v[124:125], v[124:125], v[156:157]\n"
    "v_pk_add_f32 v[126:127], v[126:127], v[158:159]\n"
    "s_waitcnt lgkmcnt(12)\n"
    "v_pk_add_f32 v[128:129], v[128:129], v[160:161]\n"
    "v_pk_add_f32 v[130:131], v[130:131], v[162:163]\n"
    "v_cvt_pk_bf16_f32 v60, v124, v125\n"
    "v_cvt_pk_bf16_f32 v61, v126, v127\n"
    "v_cvt_pk_bf16_f32 v62, v128, v129\n"
    "v_cvt_pk_bf16_f32 v63, v130, v131\n"
    "s_add_u32 s44, s44, 8388608\n"
    "buffer_store_dwordx4 v[60:63], v15, s[40:43], s44 offen sc1\n"
    "s_waitcnt lgkmcnt(11)\n"
    "v_pk_add_f32 v[132:133], v[132:133], v[164:165]\n"
    "v_pk_add_f32 v[134:135], v[134:135], v[166:167]\n"
    "s_waitcnt lgkmcnt(10)\n"
    "v_pk_add_f32 v[136:137], v[136:137], v[168:169]\n"
    "v_pk_add_f32 v[138:139], v[138:139], v[170:171]\n"
    "v_cvt_pk_bf16_f32 v64, v132, v133\n"
    "v_cvt_pk_bf16_f32 v65, v134, v135\n"
    "v_cvt_pk_bf16_f32 v66, v136, v137\n"
    "v_cvt_pk_bf16_f32 v67, v138, v139\n"
    "s_add_u32 s45, s45, 8388608\n"
    "buffer_store_dwordx4 v[64:67], v15, s[40:43], s45 offen sc1\n"
    "s_waitcnt lgkmcnt(9)\n"
    "v_pk_add_f32 v[140:141], v[140:141], v[172:173]\n"
    "v_pk_add_f32 v[142:143], v[142:143], v[174:175]\n"
    "s_waitcnt lgkmcnt(8)\n"
    "v_pk_add_f32 v[144:145], v[144:145], v[176:177]\n"
    "v_pk_add_f32 v[146:147], v[146:147], v[178:179]\n"
    "v_cvt_pk_bf16_f32 v68, v140, v141\n"
    "v_cvt_pk_bf16_f32 v69, v142, v143\n"
    "v_cvt_pk_bf16_f32 v70, v144, v145\n"
    "v_cvt_pk_bf16_f32 v71, v146, v147\n"
    "s_add_u32 s46, s46, 8388608\n"
    "buffer_store_dwordx4 v[68:71], v15, s[40:43], s46 offen sc1\n"
    "s_waitcnt lgkmcnt(6)\n"
    "v_mfma_f32_16x16x32_bf16 v[148:151], v[20:23], v[188:191], v[148:151]\n"
    "ds_read_b64_tr_b16 v[36:37], v244 offset:32768\n"
    "v_mfma_f32_16x16x32_bf16 v[152:155], v[20:23], v[192:195], v[152:155]\n"
    "ds_read_b64_tr_b16 v[38:39], v244 offset:36864\n"
    "s_waitcnt lgkmcnt(6)\n"
    "v_mfma_f32_16x16x32_bf16 v[116:119], v[24:27], v[188:191], v[116:119]\n"
    "ds_read_b64_tr_b16 v[40:41], v240 offset:32768\n"
    "v_mfma_f32_16x16x32_bf16 v[120:123], v[24:27], v[192:195], v[120:123]\n"
    "ds_read_b64_tr_b16 v[42:43], v240 offset:36864\n"
    "v_add_u32 v240, 114688, v240\n"
    "v_add_u32 v244, 114688, v244\n"
    "s_waitcnt lgkmcnt(6)\n"
    "v_mfma_f32_16x16x32_bf16 v[148:151], v[28:31], v[196:199], v[148:151]\n"
    "ds_read_b64_tr_b16 v[44:45], v244 offset:0\n"
    "v_mfma_f32_16x16x32_bf16 v[152:155], v[28:31], v[200:203], v[152:155]\n"
    "ds_read_b64_tr_b16 v[46:47], v244 offset:4096\n"
    "s_waitcnt lgkmcnt(6)\n"
    "v_mfma_f32_16x16x32_bf16 v[116:119], v[32:35], v[196:199], v[116:119]\n"
    "ds_read_b64_tr_b16 v[48:49], v240 offset:0\n"
    "v_mfma_f32_16x16x32_bf16 v[120:123], v[32:35], v[200:203], v[120:123]\n"
    "ds_read_b64_tr_b16 v[50:51], v240 offset:4096\n"
    "s_waitcnt lgkmcnt(6)\n"
    "v_mfma_f32_16x16x32_bf16 v[148:151], v[36:39], v[204:207], v[148:151]\n"
    "ds_read_b64_tr_b16 v[20:21], v244 offset:16384\n"
    "v_mfma_f32_16x16x32_bf16 v[152:155], v[36:39], v[208:211], v[152:155]\n"
    "ds_read_b64_tr_b16 v[22:23], v244 offset:20480\n"
    "s_waitcnt lgkmcnt(6)\n"
    "v_mfma_f32_16x16x32_bf16 v[116:119], v[40:43], v[204:207], v[116:119]\n"
    "ds_read_b64_tr_b16 v[24:25], v240 offset:16384\n"
    "v_mfma_f32_16x16x32_bf16 v[120:123], v[40:43], v[208:211], v[120:123]\n"
    "ds_read_b64_tr_b16 v[26:27], v240 offset:20480\n"
    "s_waitcnt lgkmcnt(6)\n"
    "v_mfma_f32_16x16x32_bf16 v[148:151], v[44:47], v[212:215], v[148:151]\n"
    "ds_read_b64_tr_b16 v[28:29], v244 offset:32768\n"
    "v_mfma_f32_16x16x32_bf16 v[152:155], v[44:47], v[216:219], v[152:155]\n"
    "ds_read_b64_tr_b16 v[30:31], v244 offset:36864\n"
    "s_waitcnt lgkmcnt(6)\n"
    "v_mfma_f32_16x16x32_bf16 v[116:119], v[48:51], v[212:215], v[116:119]\n"
    "ds_read_b64_tr_b16 v[32:33], v240 offset:32768\n"
    "v_mfma_f32_16x16x32_bf16 v[120:123], v[48:51], v[216:219], v[120:123]\n"
    "ds_read_b64_tr_b16 v[34:35], v240 offset:36864\n"
    "s_waitcnt lgkmcnt(6)\n"
    "v_mfma_f32_16x16x32_bf16 v[148:151], v[20:23], v[220:223], v[148:151]\n"
    "v_mfma_f32_16x16x32_bf16 v[152:155], v[20:23], v[224:227], v[152:155]\n"
    "s_waitcnt lgkmcnt(4)\n"
    "v_mfma_f32_16x16x32_bf16 v[116:119], v[24:27], v[220:223], v[116:119]\n"
    "v_mfma_f32_16x16x32_bf16 v[120:123], v[24:27], v[224:227], v[120:123]\n"
    "s_waitcnt lgkmcnt(2)\n"
    "v_mfma_f32_16x16x32_bf16 v[148:151], v[28:31], v[180:183], v[148:151]\n"
    "v_mfma_f32_16x16x32_bf16 v[152:155], v[28:31], v[184:187], v[152:155]\n"
    "s_waitcnt lgkmcnt(0)\n"
    "v_mfma_f32_16x16x32_bf16 v[116:119], v[32:35], v[180:183], v[116:119]\n"
    "v_mfma_f32_16x16x32_bf16 v[120:123], v[32:35], v[184:187], v[120:123]\n"
    "s_nop 3\n"
    "ds_write_b128 v12, v[148:151] offset:0\n"
    "ds_write_b128 v12, v[152:155] offset:1024\n"
    "v_subrev_u32 v11, 40960, v11\n"
    "v_and_b32 v250, 24, v11\n"
    "v_and_b32 v11, 0xffe0, v11\n"
    "s_lshl_b32 s62, s7, 5\n"
    "s_add_u32 s62, s62, 45056\n"
    "v_lshl_add_u32 v11, v11, 3, s62\n"
    "v_add_u32 v11, v11, v250\n"
    "v_mov_b32 v254, s84\n"
    "v_mov_b32 v255, s85\n"
    "s_lshl_b64 s[64:65], 1, s70\n"
    "s_waitcnt vmcnt(0)\n"
    "s_mov_b64 exec, s[64:65]\n"
    "global_store_dwordx2 v11, v[254:255], s[58:59] sc1\n"
    "s_mov_b64 exec, -1\n"
    "s_waitcnt lgkmcnt(0)\n"
    "s_barrier\n"
    "ds_read_b128 v[148:151], v13 offset:0\n"
    "ds_read_b128 v[152:155], v13 offset:1024\n"
    "v_lshlrev_b32 v20, 16, v92\n"
    "v_and_b32 v21, 0xffff0000, v92\n"
    "v_lshlrev_b32 v22, 16, v93\n"
    "v_and_b32 v23, 0xffff0000, v93\n"
    "v_lshlrev_b32 v24, 16, v94\n"
    "v_and_b32 v25, 0xffff0000, v94\n"
    "v_lshlrev_b32 v26, 16, v95\n"
    "v_and_b32 v27, 0xffff0000, v95\n"
    "v_pk_add_f32 v[84:85], v[84:85], v[20:21]\n"
    "v_pk_add_f32 v[86:87], v[86:87], v[22:23]\n"
    "v_pk_add_f32 v[88:89], v[88:89], v[24:25]\n"
    "v_pk_add_f32 v[90:91], v[90:91], v[26:27]\n"
    "v_lshlrev_b32 v20, 16, v96\n"
    "v_and_b32 v21, 0xffff0000, v96\n"
    "v_lshlrev_b32 v22, 16, v97\n"
    "v_and_b32 v23, 0xffff0000, v97\n"
    "v_lshlrev_b32 v24, 16, v98\n"
    "v_and_b32 v25, 0xffff0000, v98\n"
    "v_lshlrev_b32 v26, 16, v99\n"
    "v_and_b32 v27, 0xffff0000, v99\n"
    "v_pk_add_f32 v[84:85], v[84:85], v[20:21]\n"
    "v_pk_add_f32 v[86:87], v[86:87], v[22:23]\n"
    "v_pk_add_f32 v[88:89], v[88:89], v[24:25]\n"
    "v_pk_add_f32 v[90:91], v[90:91], v[26:27]\n"
    "v_lshlrev_b32 v20, 16, v100\n"
    "v_and_b32 v21, 0xffff0000, v100\n"
    "v_lshlrev_b32 v22, 16, v101\n"
    "v_and_b32 v23, 0xffff0000, v101\n"
    "v_lshlrev_b32 v24, 16, v102\n"
    "v_and_b32 v25, 0xffff0000, v102\n"
    "v_lshlrev_b32 v26, 16, v103\n"
    "v_and_b32 v27, 0xffff0000, v103\n"
    "v_pk_add_f32 v[84:85], v[84:85], v[20:21]\n"
    "v_pk_add_f32 v[86:87], v[86:87], v[22:23]\n"
    "v_pk_add_f32 v[88:89], v[88:89], v[24:25]\n"
    "v_pk_add_f32 v[90:91], v[90:91], v[26:27]\n"
    "s_waitcnt lgkmcnt(1)\n"
    "v_pk_add_f32 v[116:117], v[116:117], v[148:149]\n"
    "v_pk_add_f32 v[118:119], v[118:119], v[150:151]\n"
    "s_waitcnt lgkmcnt(0)\n"
    "v_pk_add_f32 v[120:121], v[120:121], v[152:153]\n"
    "v_pk_add_f32 v[122:123], v[122:123], v[154:155]\n"
    "v_mul_f32 v28, 0xbfb8aa3b, v84\n"
    "v_mul_f32 v29, 0xbfb8aa3b, v85\n"
    "v_mul_f32 v30, 0xbfb8aa3b, v86\n"
    "v_mul_f32 v31, 0xbfb8aa3b, v87\n"
    "v_mul_f32 v32, 0xbfb8aa3b, v88\n"
    "v_mul_f32 v33, 0xbfb8aa3b, v89\n"
    "v_mul_f32 v34, 0xbfb8aa3b, v90\n"
    "v_mul_f32 v35, 0xbfb8aa3b, v91\n"
    "v_exp_f32 v28, v28\n"
    "v_exp_f32 v29, v29\n"
    "v_exp_f32 v30, v30\n"
    "v_exp_f32 v31, v31\n"
    "v_exp_f32 v32, v32\n"
    "v_exp_f32 v33, v33\n"
    "v_exp_f32 v34, v34\n"
    "v_exp_f32 v35, v35\n"
    "v_add_f32 v28, 1.0, v28\n"
    "v_add_f32 v29, 1.0, v29\n"
    "v_add_f32 v30, 1.0, v30\n"
    "v_add_f32 v31, 1.0, v31\n"
    "v_add_f32 v32, 1.0, v32\n"
    "v_add_f32 v33, 1.0, v33\n"
    "v_add_f32 v34, 1.0, v34\n"
    "v_add_f32 v35, 1.0, v35\n"
    "v_rcp_f32 v28, v28\n"
    "v_rcp_f32 v29, v29\n"
    "v_rcp_f32 v30, v30\n"
    "v_rcp_f32 v31, v31\n"
    "v_rcp_f32 v32, v32\n"
    "v_rcp_f32 v33, v33\n"
    "v_rcp_f32 v34, v34\n"
    "v_rcp_f32 v35, v35\n"
    "v_mul_f32 v84, v84, v28\n"
    "v_mul_f32 v85, v85, v29\n"
    "v_mul_f32 v86, v86, v30\n"
    "v_mul_f32 v87, v87, v31\n"
    "v_mul_f32 v88, v88, v32\n"
    "v_mul_f32 v89, v89, v33\n"
    "v_mul_f32 v90, v90, v34\n"
    "v_mul_f32 v91, v91, v35\n"
    "v_lshlrev_b32 v28, 16, v76\n"
    "v_and_b32 v29, 0xffff0000, v76\n"
    "v_lshlrev_b32 v30, 16, v77\n"
    "v_and_b32 v31, 0xffff0000, v77\n"
    "v_lshlrev_b32 v32, 16, v78\n"
    "v_and_b32 v33, 0xffff0000, v78\n"
    "v_lshlrev_b32 v34, 16, v79\n"
    "v_and_b32 v35, 0xffff0000, v79\n"
    "s_mov_b32 s89, 0x40000\n"
    "L_sp_s_%=:\n"
    "global_load_dwordx2 v[252:253], v11, s[58:59] sc1\n"
    "s_waitcnt vmcnt(0)\n"
    "v_cmp_ne_u64 vcc, s[84:85], v[252:253]\n"
    "s_nop 0\n"
    "s_cbranch_vccz L_spd_s_%=\n"
    "s_sleep 1\n"
    "s_sub_u32 s89, s89, 1\n"
    "s_cmp_lg_u32 s89, 0\n"
    "s_cbranch_scc1 L_sp_s_%=\n"
    "L_spd_s_%=:\n"
    "s_add_u32 s47, s47, 8388608\n"
    "buffer_load_dwordx4 v[92:95], v15, s[40:43], s47 offen sc1\n"
    "s_add_u32 s48, s48, 8388608\n"
    "buffer_load_dwordx4 v[96:99], v15, s[40:43], s48 offen sc1\n"
    "s_add_u32 s49, s49, 8388608\n"
    "buffer_load_dwordx4 v[100:103], v15, s[40:43], s49 offen sc1\n"
    "s_waitcnt vmcnt(2)\n"
    "v_lshlrev_b32 v20, 16, v92\n"
    "v_and_b32 v21, 0xffff0000, v92\n"
    "v_lshlrev_b32 v22, 16, v93\n"
    "v_and_b32 v23, 0xffff0000, v93\n"
    "v_lshlrev_b32 v24, 16, v94\n"
    "v_and_b32 v25, 0xffff0000, v94\n"
    "v_lshlrev_b32 v26, 16, v95\n"
    "v_and_b32 v27, 0xffff0000, v95\n"
    "v_pk_add_f32 v[116:117], v[116:117], v[20:21]\n"
    "v_pk_add_f32 v[118:119], v[118:119], v[22:23]\n"
    "v_pk_add_f32 v[120:121], v[120:121], v[24:25]\n"
    "v_pk_add_f32 v[122:123], v[122:123], v[26:27]\n"
    "s_waitcnt vmcnt(1)\n"
    "v_lshlrev_b32 v20, 16, v96\n"
    "v_and_b32 v21, 0xffff0000, v96\n"
    "v_lshlrev_b32 v22, 16, v97\n"
    "v_and_b32 v23, 0xffff0000, v97\n"
    "v_lshlrev_b32 v24, 16, v98\n"
    "v_and_b32 v25, 0xffff0000, v98\n"
    "v_lshlrev_b32 v26, 16, v99\n"
    "v_and_b32 v27, 0xffff0000, v99\n"
    "v_pk_add_f32 v[116:117], v[116:117], v[20:21]\n"
    "v_pk_add_f32 v[118:119], v[118:119], v[22:23]\n"
    "v_pk_add_f32 v[120:121], v[120:121], v[24:25]\n"
    "v_pk_add_f32 v[122:123], v[122:123], v[26:27]\n"
    "s_waitcnt vmcnt(0)\n"
    "v_lshlrev_b32 v20, 16, v100\n"
    "v_and_b32 v21, 0xffff0000, v100\n"
    "v_lshlrev_b32 v22, 16, v101\n"
    "v_and_b32 v23, 0xffff0000, v101\n"
    "v_lshlrev_b32 v24, 16, v102\n"
    "v_and_b32 v25, 0xffff0000, v102\n"
    "v_lshlrev_b32 v26, 16, v103\n"
    "v_and_b32 v27, 0xffff0000, v103\n"
    "v_pk_add_f32 v[116:117], v[116:117], v[20:21]\n"
    "v_pk_add_f32 v[118:119], v[118:119], v[22:23]\n"
    "v_pk_add_f32 v[120:121], v[120:121], v[24:25]\n"
    "v_pk_add_f32 v[122:123], v[122:123], v[26:27]\n"
    "v_pk_fma_f32 v[28:29], v[116:117], v[84:85], v[28:29]\n"
    "v_pk_fma_f32 v[30:31], v[118:119], v[86:87], v[30:31]\n"
    "v_pk_fma_f32 v[32:33], v[120:121], v[88:89], v[32:33]\n"
    "v_pk_fma_f32 v[34:35], v[122:123], v[90:91], v[34:35]\n"
    "v_cvt_pk_bf16_f32 v20, v28, v29\n"
    "v_cvt_pk_bf16_f32 v21, v30, v31\n"
    "v_cvt_pk_bf16_f32 v22, v32, v33\n"
    "v_cvt_pk_bf16_f32 v23, v34, v35\n"
    "buffer_store_dwordx2 v[20:21], v16, s[28:31], 0 offen sc1\n"
    "buffer_store_dwordx2 v[22:23], v16, s[28:31], s78 offen sc1\n"
    "s_or_b32 s62, s6, s7\n"
    "s_cmp_lg_u32 s62, 0\n"
    "s_cbranch_scc1 L_end_%=\n"
    "v_cmp_ne_u64 vcc, s[84:85], v[104:105]\n"
    "v_cmp_ne_u64 s[64:65], s[84:85], v[106:107]\n"
    "s_nop 0\n"
    "s_or_b64 vcc, vcc, s[64:65]\n"
    "v_cmp_ne_u64 s[64:65], s[84:85], v[108:109]\n"
    "s_nop 0\n"
    "s_or_b64 vcc, vcc, s[64:65]\n"
    "v_cmp_ne_u64 s[64:65], s[84:85], v[110:111]\n"
    "s_nop 0\n"
    "s_or_b64 vcc, vcc, s[64:65]\n"
    "s_cbranch_vccz L_eok_%=\n"
    "s_mov_b32 s89, 0x40000\n"
    "L_sp_e_%=:\n"
    "global_load_dwordx2 v[104:105], v6, s[58:59] offset:0 sc1\n"
    "global_load_dwordx2 v[106:107], v6, s[58:59] offset:512 sc1\n"
    "global_load_dwordx2 v[108:109], v6, s[58:59] offset:1024 sc1\n"
    "global_load_dwordx2 v[110:111], v6, s[58:59] offset:1536 sc1\n"
    "s_waitcnt vmcnt(0)\n"
    "v_cmp_ne_u64 vcc, s[84:85], v[104:105]\n"
    "v_cmp_ne_u64 s[64:65], s[84:85], v[106:107]\n"
    "s_nop 0\n"
    "s_or_b64 vcc, vcc, s[64:65]\n"
    "v_cmp_ne_u64 s[64:65], s[84:85], v[108:109]\n"
    "s_nop 0\n"
    "s_or_b64 vcc, vcc, s[64:65]\n"
    "v_cmp_ne_u64 s[64:65], s[84:85], v[110:111]\n"
    "s_nop 0\n"
    "s_or_b64 vcc, vcc, s[64:65]\n"
    "s_cbranch_vccz L_spd_e_%=\n"
    "s_sleep 1\n"
    "s_sub_u32 s89, s89, 1\n"
    "s_cmp_lg_u32 s89, 0\n"
    "s_cbranch_scc1 L_sp_e_%=\n"
    "L_spd_e_%=:\n"
    "L_eok_%=:\n"
    "v_mov_b32 v6, 0\n"
    "v_mov_b32 v104, s84\n"
    "v_mov_b32 v105, s85\n"
    "s_mov_b64 exec, 1\n"
    "global_store_dwordx2 v6, v[104:105], s[58:59] sc1\n"
    "s_mov_b64 exec, -1\n"
    "L_end_%=:\n"
    "s_endpgm\n"
    "L_retry_%=:\n"
    "s_mov_b32 s89, 0x40000\n"
    "L_sp_st_%=:\n"
    "global_load_dwordx4 v[188:191], v248, s[58:59] sc1\n"
    "global_load_dwordx4 v[196:199], v250, s[58:59] sc1\n"
    "global_load_dwordx4 v[200:203], v250, s[58:59] offset:256 sc1\n"
    "s_waitcnt vmcnt(0)\n"
    "v_cmp_ne_u32 vcc, s84, v189\n"
    "v_cmp_ne_u32 s[64:65], s84, v191\n"
    "s_nop 0\n"
    "s_or_b64 vcc, vcc, s[64:65]\n"
    "v_cmp_ne_u32 s[64:65], s84, v197\n"
    "s_nop 0\n"
    "s_or_b64 vcc, vcc, s[64:65]\n"
    "v_cmp_ne_u32 s[64:65], s84, v199\n"
    "s_nop 0\n"
    "s_or_b64 vcc, vcc, s[64:65]\n"
    "v_cmp_ne_u32 s[64:65], s84, v201\n"
    "s_nop 0\n"
    "s_or_b64 vcc, vcc, s[64:65]\n"
    "v_cmp_ne_u32 s[64:65], s84, v203\n"
    "s_nop 0\n"
    "s_or_b64 vcc, vcc, s[64:65]\n"
    "s_cbranch_vccz L_spd_st_%=\n"
    "s_sleep 1\n"
    "s_sub_u32 s89, s89, 1\n"
    "s_cmp_lg_u32 s89, 0\n"
    "s_cbranch_scc1 L_sp_st_%=\n"
    "L_spd_st_%=:\n"
    "s_branch L_retried_%=\n"
    "L_rare_p0_%=:\n"
    "s_nop 15\n"
    "v_max3_f32 v248, v52, v53, v54\n"
    "v_max3_f32 v248, v248, v55, v60\n"
    "v_max3_f32 v248, v248, v61, v62\n"
    "v_max_f32 v248, v248, v63\n"
    "v_mov_b32 v249, v248\n"
    "s_nop 1\n"
    "v_permlane32_swap_b32 v248, v249\n"
    "v_max_f32 v248, v248, v249\n"
    "v_mov_b32 v249, v248\n"
    "s_nop 1\n"
    "v_permlane16_swap_b32 v248, v249\n"
    "v_max_f32 v248, v248, v249\n"
    "v_sub_f32 v232, v232, v248\n"
    "v_sub_f32 v233, v233, v248\n"
    "v_sub_f32 v234, v234, v248\n"
    "v_sub_f32 v235, v235, v248\n"
    "v_sub_f32 v68, v68, v248\n"
    "v_sub_f32 v69, v69, v248\n"
    "v_sub_f32 v70, v70, v248\n"
    "v_sub_f32 v71, v71, v248\n"
    "v_sub_f32 v76, v76, v248\n"
    "v_sub_f32 v77, v77, v248\n"
    "v_sub_f32 v78, v78, v248\n"
    "v_sub_f32 v79, v79, v248\n"
    "v_sub_f32 v52, v52, v248\n"
    "v_sub_f32 v53, v53, v248\n"
    "v_sub_f32 v54, v54, v248\n"
    "v_sub_f32 v55, v55, v248\n"
    "v_sub_f32 v60, v60, v248\n"
    "v_sub_f32 v61, v61, v248\n"
    "v_sub_f32 v62, v62, v248\n"
    "v_sub_f32 v63, v63, v248\n"
    "v_exp_f32 v100, v52\n"
    "v_exp_f32 v101, v53\n"
    "v_exp_f32 v102, v54\n"
    "v_exp_f32 v103, v55\n"
    "v_exp_f32 v104, v60\n"
    "v_exp_f32 v105, v61\n"
    "v_exp_f32 v106, v62\n"
    "v_exp_f32 v107, v63\n"
    "v_cvt_pk_bf16_f32 v84, v100, v101\n"
    "v_cvt_pk_bf16_f32 v85, v102, v103\n"
    "v_cvt_pk_bf16_f32 v86, v104, v105\n"
    "v_cvt_pk_bf16_f32 v87, v106, v107\n"
    "v_max3_f32 v248, v56, v57, v58\n"
    "v_max3_f32 v248, v248, v59, v64\n"
    "v_max3_f32 v248, v248, v65, v66\n"
    "v_max_f32 v248, v248, v67\n"
    "v_mov_b32 v249, v248\n"
    "s_nop 1\n"
    "v_permlane32_swap_b32 v248, v249\n"
    "v_max_f32 v248, v248, v249\n"
    "v_mov_b32 v249, v248\n"
    "s_nop 1\n"
    "v_permlane16_swap_b32 v248, v249\n"
    "v_max_f32 v248, v248, v249\n"
    "v_sub_f32 v236, v236, v248\n"
    "v_sub_f32 v237, v237, v248\n"
    "v_sub_f32 v238, v238, v248\n"
    "v_sub_f32 v239, v239, v248\n"
    "v_sub_f32 v72, v72, v248\n"
    "v_sub_f32 v73, v73, v248\n"
    "v_sub_f32 v74, v74, v248\n"
    "v_sub_f32 v75, v75, v248\n"
    "v_sub_f32 v80, v80, v248\n"
    "v_sub_f32 v81, v81, v248\n"
    "v_sub_f32 v82, v82, v248\n"
    "v_sub_f32 v83, v83, v248\n"
    "v_sub_f32 v56, v56, v248\n"
    "v_sub_f32 v57, v57, v248\n"
    "v_sub_f32 v58, v58, v248\n"
    "v_sub_f32 v59, v59, v248\n"
    "v_sub_f32 v64, v64, v248\n"
    "v_sub_f32 v65, v65, v248\n"
    "v_sub_f32 v66, v66, v248\n"
    "v_sub_f32 v67, v67, v248\n"
    "v_exp_f32 v100, v56\n"
    "v_exp_f32 v101, v57\n"
    "v_exp_f32 v102, v58\n"
    "v_exp_f32 v103, v59\n"
    "v_exp_f32 v104, v64\n"
    "v_exp_f32 v105, v65\n"
    "v_exp_f32 v106, v66\n"
    "v_exp_f32 v107, v67\n"
    "v_cvt_pk_bf16_f32 v88, v100, v101\n"
    "v_cvt_pk_bf16_f32 v89, v102, v103\n"
    "v_cvt_pk_bf16_f32 v90, v104, v105\n"
    "v_cvt_pk_bf16_f32 v91, v106, v107\n"
    "v_mfma_f32_16x16x32_bf16 v[216:219], v[212:215], v[84:87], 0\n"
    "s_nop 0\n"
    "v_mfma_f32_16x16x32_bf16 v[220:223], v[212:215], v[88:91], 0\n"
    "s_nop 9\n"
    "s_branch L_ret_p0_%=\n"
    "L_rare_od0_%=:\n"
    "s_nop 15\n"
    "v_max3_f32 v248, v68, v69, v70\n"
    "v_max3_f32 v248, v248, v71, v76\n"
    "v_max3_f32 v248, v248, v77, v78\n"
    "v_max_f32 v248, v248, v79\n"
    "v_mov_b32 v249, v248\n"
    "s_nop 1\n"
    "v_permlane32_swap_b32 v248, v249\n"
    "v_max_f32 v248, v248, v249\n"
    "v_mov_b32 v249, v248\n"
    "s_nop 1\n"
    "v_permlane16_swap_b32 v248, v249\n"
    "v_max_f32 v248, v248, v249\n"
    "v_max_f32 v248, 0, v248\n"
    "v_exp_f32 v249, -v248\n"
    "s_nop 0\n"
    "v_mul_f32 v216, v216, v249\n"
    "v_mul_f32 v217, v217, v249\n"
    "v_mul_f32 v218, v218, v249\n"
    "v_mul_f32 v219, v219, v249\n"
    "v_mul_f32 v116, v116, v249\n"
    "v_mul_f32 v117, v117, v249\n"
    "v_mul_f32 v118, v118, v249\n"
    "v_mul_f32 v119, v119, v249\n"
    "v_mul_f32 v124, v124, v249\n"
    "v_mul_f32 v125, v125, v249\n"
    "v_mul_f32 v126, v126, v249\n"
    "v_mul_f32 v127, v127, v249\n"
    "v_mul_f32 v132, v132, v249\n"
    "v_mul_f32 v133, v133, v249\n"
    "v_mul_f32 v134, v134, v249\n"
    "v_mul_f32 v135, v135, v249\n"
    "v_mul_f32 v140, v140, v249\n"
    "v_mul_f32 v141, v141, v249\n"
    "v_mul_f32 v142, v142, v249\n"
    "v_mul_f32 v143, v143, v249\n"
    "v_mul_f32 v148, v148, v249\n"
    "v_mul_f32 v149, v149, v249\n"
    "v_mul_f32 v150, v150, v249\n"
    "v_mul_f32 v151, v151, v249\n"
    "v_mul_f32 v156, v156, v249\n"
    "v_mul_f32 v157, v157, v249\n"
    "v_mul_f32 v158, v158, v249\n"
    "v_mul_f32 v159, v159, v249\n"
    "v_mul_f32 v164, v164, v249\n"
    "v_mul_f32 v165, v165, v249\n"
    "v_mul_f32 v166, v166, v249\n"
    "v_mul_f32 v167, v167, v249\n"
    "v_mul_f32 v172, v172, v249\n"
    "v_mul_f32 v173, v173, v249\n"
    "v_mul_f32 v174, v174, v249\n"
    "v_mul_f32 v175, v175, v249\n"
    "v_sub_f32 v232, v232, v248\n"
    "v_sub_f32 v233, v233, v248\n"
    "v_sub_f32 v234, v234, v248\n"
    "v_sub_f32 v235, v235, v248\n"
    "v_sub_f32 v52, v52, v248\n"
    "v_sub_f32 v53, v53, v248\n"
    "v_sub_f32 v54, v54, v248\n"
    "v_sub_f32 v55, v55, v248\n"
    "v_sub_f32 v60, v60, v248\n"
    "v_sub_f32 v61, v61, v248\n"
    "v_sub_f32 v62, v62, v248\n"
    "v_sub_f32 v63, v63, v248\n"
    "v_sub_f32 v68, v68, v248\n"
    "v_sub_f32 v69, v69, v248\n"
    "v_sub_f32 v70, v70, v248\n"
    "v_sub_f32 v71, v71, v248\n"
    "v_sub_f32 v76, v76, v248\n"
    "v_sub_f32 v77, v77, v248\n"
    "v_sub_f32 v78, v78, v248\n"
    "v_sub_f32 v79, v79, v248\n"
    "v_exp_f32 v100, v68\n"
    "v_exp_f32 v101, v69\n"
    "v_exp_f32 v102, v70\n"
    "v_exp_f32 v103, v71\n"
    "v_exp_f32 v104, v76\n"
    "v_exp_f32 v105, v77\n"
    "v_exp_f32 v106, v78\n"
    "v_exp_f32 v107, v79\n"
    "v_cvt_pk_bf16_f32 v92, v100, v101\n"
    "v_cvt_pk_bf16_f32 v93, v102, v103\n"
    "v_cvt_pk_bf16_f32 v94, v104, v105\n"
    "v_cvt_pk_bf16_f32 v95, v106, v107\n"
    "v_max3_f32 v248, v72, v73, v74\n"
    "v_max3_f32 v248, v248, v75, v80\n"
    "v_max3_f32 v248, v248, v81, v82\n"
    "v_max_f32 v248, v248, v83\n"
    "v_mov_b32 v249, v248\n"
    "s_nop 1\n"
    "v_permlane32_swap_b32 v248, v249\n"
    "v_max_f32 v248, v248, v249\n"
    "v_mov_b32 v249, v248\n"
    "s_nop 1\n"
    "v_permlane16_swap_b32 v248, v249\n"
    "v_max_f32 v248, v248, v249\n"
    "v_max_f32 v248, 0, v248\n"
    "v_exp_f32 v249, -v248\n"
    "s_nop 0\n"
    "v_mul_f32 v220, v220, v249\n"
    "v_mul_f32 v221, v221, v249\n"
    "v_mul_f32 v222, v222, v249\n"
    "v_mul_f32 v223, v223, v249\n"
    "v_mul_f32 v120, v120, v249\n"
    "v_mul_f32 v121, v121, v249\n"
    "v_mul_f32 v122, v122, v249\n"
    "v_mul_f32 v123, v123, v249\n"
    "v_mul_f32 v128, v128, v249\n"
    "v_mul_f32 v129, v129, v249\n"
    "v_mul_f32 v130, v130, v249\n"
    "v_mul_f32 v131, v131, v249\n"
    "v_mul_f32 v136, v136, v249\n"
    "v_mul_f32 v137, v137, v249\n"
    "v_mul_f32 v138, v138, v249\n"
    "v_mul_f32 v139, v139, v249\n"
    "v_mul_f32 v144, v144, v249\n"
    "v_mul_f32 v145, v145, v249\n"
    "v_mul_f32 v146, v146, v249\n"
    "v_mul_f32 v147, v147, v249\n"
    "v_mul_f32 v152, v152, v249\n"
    "v_mul_f32 v153, v153, v249\n"
    "v_mul_f32 v154, v154, v249\n"
    "v_mul_f32 v155, v155, v249\n"
    "v_mul_f32 v160, v160, v249\n"
    "v_mul_f32 v161, v161, v249\n"
    "v_mul_f32 v162, v162, v249\n"
    "v_mul_f32 v163, v163, v249\n"
    "v_mul_f32 v168, v168, v249\n"
    "v_mul_f32 v169, v169, v249\n"
    "v_mul_f32 v170, v170, v249\n"
    "v_mul_f32 v171, v171, v249\n"
    "v_mul_f32 v176, v176, v249\n"
    "v_mul_f32 v177, v177, v249\n"
    "v_mul_f32 v178, v178, v249\n"
    "v_mul_f32 v179, v179, v249\n"
    "v_sub_f32 v236, v236, v248\n"
    "v_sub_f32 v237, v237, v248\n"
    "v_sub_f32 v238, v238, v248\n"
    "v_sub_f32 v239, v239, v248\n"
    "v_sub_f32 v56, v56, v248\n"
    "v_sub_f32 v57, v57, v248\n"
    "v_sub_f32 v58, v58, v248\n"
    "v_sub_f32 v59, v59, v248\n"
    "v_sub_f32 v64, v64, v248\n"
    "v_sub_f32 v65, v65, v248\n"
    "v_sub_f32 v66, v66, v248\n"
    "v_sub_f32 v67, v67, v248\n"
    "v_sub_f32 v72, v72, v248\n"
    "v_sub_f32 v73, v73, v248\n"
    "v_sub_f32 v74, v74, v248\n"
    "v_sub_f32 v75, v75, v248\n"
    "v_sub_f32 v80, v80, v248\n"
    "v_sub_f32 v81, v81, v248\n"
    "v_sub_f32 v82, v82, v248\n"
    "v_sub_f32 v83, v83, v248\n"
    "v_exp_f32 v100, v72\n"
    "v_exp_f32 v101, v73\n"
    "v_exp_f32 v102, v74\n"
    "v_exp_f32 v103, v75\n"
    "v_exp_f32 v104, v80\n"
    "v_exp_f32 v105, v81\n"
    "v_exp_f32 v106, v82\n"
    "v_exp_f32 v107, v83\n"
    "v_cvt_pk_bf16_f32 v96, v100, v101\n"
    "v_cvt_pk_bf16_f32 v97, v102, v103\n"
    "v_cvt_pk_bf16_f32 v98, v104, v105\n"
    "v_cvt_pk_bf16_f32 v99, v106, v107\n"
    "v_mfma_f32_16x16x32_bf16 v[224:227], v[212:215], v[92:95], v[216:219]\n"
    "s_nop 0\n"
    "v_mfma_f32_16x16x32_bf16 v[228:231], v[212:215], v[96:99], v[220:223]\n"
    "s_nop 9\n"
    "s_branch L_ret_od0_%=\n"
    "L_rare_ev0_%=:\n"
    "s_nop 15\n"
    "v_max3_f32 v248, v52, v53, v54\n"
    "v_max3_f32 v248, v248, v55, v60\n"
    "v_max3_f32 v248, v248, v61, v62\n"
    "v_max_f32 v248, v248, v63\n"
    "v_mov_b32 v249, v248\n"
    "s_nop 1\n"
    "v_permlane32_swap_b32 v248, v249\n"
    "v_max_f32 v248, v248, v249\n"
    "v_mov_b32 v249, v248\n"
    "s_nop 1\n"
    "v_permlane16_swap_b32 v248, v249\n"
    "v_max_f32 v248, v248, v249\n"
    "v_max_f32 v248, 0, v248\n"
    "v_exp_f32 v249, -v248\n"
    "s_nop 0\n"
    "v_mul_f32 v224, v224, v249\n"
    "v_mul_f32 v225, v225, v249\n"
    "v_mul_f32 v226, v226, v249\n"
    "v_mul_f32 v227, v227, v249\n"
    "v_mul_f32 v116, v116, v249\n"
    "v_mul_f32 v117, v117, v249\n"
    "v_mul_f32 v118, v118, v249\n"
    "v_mul_f32 v119, v119, v249\n"
    "v_mul_f32 v124, v124, v249\n"
    "v_mul_f32 v125, v125, v249\n"
    "v_mul_f32 v126, v126, v249\n"
    "v_mul_f32 v127, v127, v249\n"
    "v_mul_f32 v132, v132, v249\n"
    "v_mul_f32 v133, v133, v249\n"
    "v_mul_f32 v134, v134, v249\n"
    "v_mul_f32 v135, v135, v249\n"
    "v_mul_f32 v140, v140, v249\n"
    "v_mul_f32 v141, v141, v249\n"
    "v_mul_f32 v142, v142, v249\n"
    "v_mul_f32 v143, v143, v249\n"
    "v_mul_f32 v148, v148, v249\n"
    "v_mul_f32 v149, v149, v249\n"
    "v_mul_f32 v150, v150, v249\n"
    "v_mul_f32 v151, v151, v249\n"
    "v_mul_f32 v156, v156, v249\n"
    "v_mul_f32 v157, v157, v249\n"
    "v_mul_f32 v158, v158, v249\n"
    "v_mul_f32 v159, v159, v249\n"
    "v_mul_f32 v164, v164, v249\n"
    "v_mul_f32 v165, v165, v249\n"
    "v_mul_f32 v166, v166, v249\n"
    "v_mul_f32 v167, v167, v249\n"
    "v_mul_f32 v172, v172, v249\n"
    "v_mul_f32 v173, v173, v249\n"
    "v_mul_f32 v174, v174, v249\n"
    "v_mul_f32 v175, v175, v249\n"
    "v_sub_f32 v232, v232, v248\n"
    "v_sub_f32 v233, v233, v248\n"
    "v_sub_f32 v234, v234, v248\n"
    "v_sub_f32 v235, v235, v248\n"
    "v_sub_f32 v68, v68, v248\n"
    "v_sub_f32 v69, v69, v248\n"
    "v_sub_f32 v70, v70, v248\n"
    "v_sub_f32 v71, v71, v248\n"
    "v_sub_f32 v76, v76, v248\n"
    "v_sub_f32 v77, v77, v248\n"
    "v_sub_f32 v78, v78, v248\n"
    "v_sub_f32 v79, v79, v248\n"
    "v_sub_f32 v52, v52, v248\n"
    "v_sub_f32 v53, v53, v248\n"
    "v_sub_f32 v54, v54, v248\n"
    "v_sub_f32 v55, v55, v248\n"
    "v_sub_f32 v60, v60, v248\n"
    "v_sub_f32 v61, v61, v248\n"
    "v_sub_f32 v62, v62, v248\n"
    "v_sub_f32 v63, v63, v248\n"
    "v_exp_f32 v100, v52\n"
    "v_exp_f32 v101, v53\n"
    "v_exp_f32 v102, v54\n"
    "v_exp_f32 v103, v55\n"
    "v_exp_f32 v104, v60\n"
    "v_exp_f32 v105, v61\n"
    "v_exp_f32 v106, v62\n"
    "v_exp_f32 v107, v63\n"
    "v_cvt_pk_bf16_f32 v84, v100, v101\n"
    "v_cvt_pk_bf16_f32 v85, v102, v103\n"
    "v_cvt_pk_bf16_f32 v86, v104, v105\n"
    "v_cvt_pk_bf16_f32 v87, v106, v107\n"
    "v_max3_f32 v248, v56, v57, v58\n"
    "v_max3_f32 v248, v248, v59, v64\n"
    "v_max3_f32 v248, v248, v65, v66\n"
    "v_max_f32 v248, v248, v67\n"
    "v_mov_b32 v249, v248\n"
    "s_nop 1\n"
    "v_permlane32_swap_b32 v248, v249\n"
    "v_max_f32 v248, v248, v249\n"
    "v_mov_b32 v249, v248\n"
    "s_nop 1\n"
    "v_permlane16_swap_b32 v248, v249\n"
    "v_max_f32 v248, v248, v249\n"
    "v_max_f32 v248, 0, v248\n"
    "v_exp_f32 v249, -v248\n"
    "s_nop 0\n"
    "v_mul_f32 v228, v228, v249\n"
    "v_mul_f32 v229, v229, v249\n"
    "v_mul_f32 v230, v230, v249\n"
    "v_mul_f32 v231, v231, v249\n"
    "v_mul_f32 v120, v120, v249\n"
    "v_mul_f32 v121, v121, v249\n"
    "v_mul_f32 v122, v122, v249\n"
    "v_mul_f32 v123, v123, v249\n"
    "v_mul_f32 v128, v128, v249\n"
    "v_mul_f32 v129, v129, v249\n"
    "v_mul_f32 v130, v130, v249\n"
    "v_mul_f32 v131, v131, v249\n"
    "v_mul_f32 v136, v136, v249\n"
    "v_mul_f32 v137, v137, v249\n"
    "v_mul_f32 v138, v138, v249\n"
    "v_mul_f32 v139, v139, v249\n"
    "v_mul_f32 v144, v144, v249\n"
    "v_mul_f32 v145, v145, v249\n"
    "v_mul_f32 v146, v146, v249\n"
    "v_mul_f32 v147, v147, v249\n"
    "v_mul_f32 v152, v152, v249\n"
    "v_mul_f32 v153, v153, v249\n"
    "v_mul_f32 v154, v154, v249\n"
    "v_mul_f32 v155, v155, v249\n"
    "v_mul_f32 v160, v160, v249\n"
    "v_mul_f32 v161, v161, v249\n"
    "v_mul_f32 v162, v162, v249\n"
    "v_mul_f32 v163, v163, v249\n"
    "v_mul_f32 v168, v168, v249\n"
    "v_mul_f32 v169, v169, v249\n"
    "v_mul_f32 v170, v170, v249\n"
    "v_mul_f32 v171, v171, v249\n"
    "v_mul_f32 v176, v176, v249\n"
    "v_mul_f32 v177, v177, v249\n"
    "v_mul_f32 v178, v178, v249\n"
    "v_mul_f32 v179, v179, v249\n"
    "v_sub_f32 v236, v236, v248\n"
    "v_sub_f32 v237, v237, v248\n"
    "v_sub_f32 v238, v238, v248\n"
    "v_sub_f32 v239, v239, v248\n"
    "v_sub_f32 v72, v72, v248\n"
    "v_sub_f32 v73, v73, v248\n"
    "v_sub_f32 v74, v74, v248\n"
    "v_sub_f32 v75, v75, v248\n"
    "v_sub_f32 v80, v80, v248\n"
    "v_sub_f32 v81, v81, v248\n"
    "v_sub_f32 v82, v82, v248\n"
    "v_sub_f32 v83, v83, v248\n"
    "v_sub_f32 v56, v56, v248\n"
    "v_sub_f32 v57, v57, v248\n"
    "v_sub_f32 v58, v58, v248\n"
    "v_sub_f32 v59, v59, v248\n"
    "v_sub_f32 v64, v64, v248\n"
    "v_sub_f32 v65, v65, v248\n"
    "v_sub_f32 v66, v66, v248\n"
    "v_sub_f32 v67, v67, v248\n"
    "v_exp_f32 v100, v56\n"
    "v_exp_f32 v101, v57\n"
    "v_exp_f32 v102, v58\n"
    "v_exp_f32 v103, v59\n"
    "v_exp_f32 v104, v64\n"
    "v_exp_f32 v105, v65\n"
    "v_exp_f32 v106, v66\n"
    "v_exp_f32 v107, v67\n"
    "v_cvt_pk_bf16_f32 v88, v100, v101\n"
    "v_cvt_pk_bf16_f32 v89, v102, v103\n"
    "v_cvt_pk_bf16_f32 v90, v104, v105\n"
    "v_cvt_pk_bf16_f32 v91, v106, v107\n"
    "v_mfma_f32_16x16x32_bf16 v[216:219], v[212:215], v[84:87], v[224:227]\n"
    "s_nop 0\n"
    "v_mfma_f32_16x16x32_bf16 v[220:223], v[212:215], v[88:91], v[228:231]\n"
    "s_nop 9\n"
    "s_branch L_ret_ev0_%=\n"
    "L_rare_oda_%=:\n"
    "s_nop 15\n"
    "v_max3_f32 v248, v68, v69, v70\n"
    "v_max3_f32 v248, v248, v71, v76\n"
    "v_max3_f32 v248, v248, v77, v78\n"
    "v_max_f32 v248, v248, v79\n"
    "v_mov_b32 v249, v248\n"
    "s_nop 1\n"
    "v_permlane32_swap_b32 v248, v249\n"
    "v_max_f32 v248, v248, v249\n"
    "v_mov_b32 v249, v248\n"
    "s_nop 1\n"
    "v_permlane16_swap_b32 v248, v249\n"
    "v_max_f32 v248, v248, v249\n"
    "v_max_f32 v248, 0, v248\n"
    "v_exp_f32 v249, -v248\n"
    "s_nop 0\n"
    "v_mul_f32 v216, v216, v249\n"
    "v_mul_f32 v217, v217, v249\n"
    "v_mul_f32 v218, v218, v249\n"
    "v_mul_f32 v219, v219, v249\n"
    "v_mul_f32 v116, v116, v249\n"
    "v_mul_f32 v117, v117, v249\n"
    "v_mul_f32 v118, v118, v249\n"
    "v_mul_f32 v119, v119, v249\n"
    "v_mul_f32 v124, v124, v249\n"
    "v_mul_f32 v125, v125, v249\n"
    "v_mul_f32 v126, v126, v249\n"
    "v_mul_f32 v127, v127, v249\n"
    "v_mul_f32 v132, v132, v249\n"
    "v_mul_f32 v133, v133, v249\n"
    "v_mul_f32 v134, v134, v249\n"
    "v_mul_f32 v135, v135, v249\n"
    "v_mul_f32 v140, v140, v249\n"
    "v_mul_f32 v141, v141, v249\n"
    "v_mul_f32 v142, v142, v249\n"
    "v_mul_f32 v143, v143, v249\n"
    "v_mul_f32 v148, v148, v249\n"
    "v_mul_f32 v149, v149, v249\n"
    "v_mul_f32 v150, v150, v249\n"
    "v_mul_f32 v151, v151, v249\n"
    "v_mul_f32 v156, v156, v249\n"
    "v_mul_f32 v157, v157, v249\n"
    "v_mul_f32 v158, v158, v249\n"
    "v_mul_f32 v159, v159, v249\n"
    "v_mul_f32 v164, v164, v249\n"
    "v_mul_f32 v165, v165, v249\n"
    "v_mul_f32 v166, v166, v249\n"
    "v_mul_f32 v167, v167, v249\n"
    "v_mul_f32 v172, v172, v249\n"
    "v_mul_f32 v173, v173, v249\n"
    "v_mul_f32 v174, v174, v249\n"
    "v_mul_f32 v175, v175, v249\n"
    "v_sub_f32 v232, v232, v248\n"
    "v_sub_f32 v233, v233, v248\n"
    "v_sub_f32 v234, v234, v248\n"
    "v_sub_f32 v235, v235, v248\n"
    "v_sub_f32 v52, v52, v248\n"
    "v_sub_f32 v53, v53, v248\n"
    "v_sub_f32 v54, v54, v248\n"
    "v_sub_f32 v55, v55, v248\n"
    "v_sub_f32 v60, v60, v248\n"
    "v_sub_f32 v61, v61, v248\n"
    "v_sub_f32 v62, v62, v248\n"
    "v_sub_f32 v63, v63, v248\n"
    "v_sub_f32 v68, v68, v248\n"
    "v_sub_f32 v69, v69, v248\n"
    "v_sub_f32 v70, v70, v248\n"
    "v_sub_f32 v71, v71, v248\n"
    "v_sub_f32 v76, v76, v248\n"
    "v_sub_f32 v77, v77, v248\n"
    "v_sub_f32 v78, v78, v248\n"
    "v_sub_f32 v79, v79, v248\n"
    "v_exp_f32 v100, v68\n"
    "v_exp_f32 v101, v69\n"
    "v_exp_f32 v102, v70\n"
    "v_exp_f32 v103, v71\n"
    "v_exp_f32 v104, v76\n"
    "v_exp_f32 v105, v77\n"
    "v_exp_f32 v106, v78\n"
    "v_exp_f32 v107, v79\n"
    "v_cvt_pk_bf16_f32 v92, v100, v101\n"
    "v_cvt_pk_bf16_f32 v93, v102, v103\n"
    "v_cvt_pk_bf16_f32 v94, v104, v105\n"
    "v_cvt_pk_bf16_f32 v95, v106, v107\n"
    "v_max3_f32 v248, v72, v73, v74\n"
    "v_max3_f32 v248, v248, v75, v80\n"
    "v_max3_f32 v248, v248, v81, v82\n"
    "v_max_f32 v248, v248, v83\n"
    "v_mov_b32 v249, v248\n"
    "s_nop 1\n"
    "v_permlane32_swap_b32 v248, v249\n"
    "v_max_f32 v248, v248, v249\n"
    "v_mov_b32 v249, v248\n"
    "s_nop 1\n"
    "v_permlane16_swap_b32 v248, v249\n"
    "v_max_f32 v248, v248, v249\n"
    "v_max_f32 v248, 0, v248\n"
    "v_exp_f32 v249, -v248\n"
    "s_nop 0\n"
    "v_mul_f32 v220, v220, v249\n"
    "v_mul_f32 v221, v221, v249\n"
    "v_mul_f32 v222, v222, v249\n"
    "v_mul_f32 v223, v223, v249\n"
    "v_mul_f32 v120, v120, v249\n"
    "v_mul_f32 v121, v121, v249\n"
    "v_mul_f32 v122, v122, v249\n"
    "v_mul_f32 v123, v123, v249\n"
    "v_mul_f32 v128, v128, v249\n"
    "v_mul_f32 v129, v129, v249\n"
    "v_mul_f32 v130, v130, v249\n"
    "v_mul_f32 v131, v131, v249\n"
    "v_mul_f32 v136, v136, v249\n"
    "v_mul_f32 v137, v137, v249\n"
    "v_mul_f32 v138, v138, v249\n"
    "v_mul_f32 v139, v139, v249\n"
    "v_mul_f32 v144, v144, v249\n"
    "v_mul_f32 v145, v145, v249\n"
    "v_mul_f32 v146, v146, v249\n"
    "v_mul_f32 v147, v147, v249\n"
    "v_mul_f32 v152, v152, v249\n"
    "v_mul_f32 v153, v153, v249\n"
    "v_mul_f32 v154, v154, v249\n"
    "v_mul_f32 v155, v155, v249\n"
    "v_mul_f32 v160, v160, v249\n"
    "v_mul_f32 v161, v161, v249\n"
    "v_mul_f32 v162, v162, v249\n"
    "v_mul_f32 v163, v163, v249\n"
    "v_mul_f32 v168, v168, v249\n"
    "v_mul_f32 v169, v169, v249\n"
    "v_mul_f32 v170, v170, v249\n"
    "v_mul_f32 v171, v171, v249\n"
    "v_mul_f32 v176, v176, v249\n"
    "v_mul_f32 v177, v177, v249\n"
    "v_mul_f32 v178, v178, v249\n"
    "v_mul_f32 v179, v179, v249\n"
    "v_sub_f32 v236, v236, v248\n"
    "v_sub_f32 v237, v237, v248\n"
    "v_sub_f32 v238, v238, v248\n"
    "v_sub_f32 v239, v239, v248\n"
    "v_sub_f32 v56, v56, v248\n"
    "v_sub_f32 v57, v57, v248\n"
    "v_sub_f32 v58, v58, v248\n"
    "v_sub_f32 v59, v59, v248\n"
    "v_sub_f32 v64, v64, v248\n"
    "v_sub_f32 v65, v65, v248\n"
    "v_sub_f32 v66, v66, v248\n"
    "v_sub_f32 v67, v67, v248\n"
    "v_sub_f32 v72, v72, v248\n"
    "v_sub_f32 v73, v73, v248\n"
    "v_sub_f32 v74, v74, v248\n"
    "v_sub_f32 v75, v75, v248\n"
    "v_sub_f32 v80, v80, v248\n"
    "v_sub_f32 v81, v81, v248\n"
    "v_sub_f32 v82, v82, v248\n"
    "v_sub_f32 v83, v83, v248\n"
    "v_exp_f32 v100, v72\n"
    "v_exp_f32 v101, v73\n"
    "v_exp_f32 v102, v74\n"
    "v_exp_f32 v103, v75\n"
    "v_exp_f32 v104, v80\n"
    "v_exp_f32 v105, v81\n"
    "v_exp_f32 v106, v82\n"
    "v_exp_f32 v107, v83\n"
    "v_cvt_pk_bf16_f32 v96, v100, v101\n"
    "v_cvt_pk_bf16_f32 v97, v102, v103\n"
    "v_cvt_pk_bf16_f32 v98, v104, v105\n"
    "v_cvt_pk_bf16_f32 v99, v106, v107\n"
    "v_mfma_f32_16x16x32_bf16 v[224:227], v[212:215], v[92:95], v[216:219]\n"
    "s_nop 0\n"
    "v_mfma_f32_16x16x32_bf16 v[228:231], v[212:215], v[96:99], v[220:223]\n"
    "s_nop 9\n"
    "s_branch L_ret_oda_%=\n"
    "L_rare_eva_%=:\n"
    "s_nop 15\n"
    "v_max3_f32 v248, v52, v53, v54\n"
    "v_max3_f32 v248, v248, v55, v60\n"
    "v_max3_f32 v248, v248, v61, v62\n"
    "v_max_f32 v248, v248, v63\n"
    "v_mov_b32 v249, v248\n"
    "s_nop 1\n"
    "v_permlane32_swap_b32 v248, v249\n"
    "v_max_f32 v248, v248, v249\n"
    "v_mov_b32 v249, v248\n"
    "s_nop 1\n"
    "v_permlane16_swap_b32 v248, v249\n"
    "v_max_f32 v248, v248, v249\n"
    "v_max_f32 v248, 0, v248\n"
    "v_exp_f32 v249, -v248\n"
    "s_nop 0\n"
    "v_mul_f32 v224, v224, v249\n"
    "v_mul_f32 v225, v225, v249\n"
    "v_mul_f32 v226, v226, v249\n"
    "v_mul_f32 v227, v227, v249\n"
    "v_mul_f32 v116, v116, v249\n"
    "v_mul_f32 v117, v117, v249\n"
    "v_mul_f32 v118, v118, v249\n"
    "v_mul_f32 v119, v119, v249\n"
    "v_mul_f32 v124, v124, v249\n"
    "v_mul_f32 v125, v125, v249\n"
    "v_mul_f32 v126, v126, v249\n"
    "v_mul_f32 v127, v127, v249\n"
    "v_mul_f32 v132, v132, v249\n"
    "v_mul_f32 v133, v133, v249\n"
    "v_mul_f32 v134, v134, v249\n"
    "v_mul_f32 v135, v135, v249\n"
    "v_mul_f32 v140, v140, v249\n"
    "v_mul_f32 v141, v141, v249\n"
    "v_mul_f32 v142, v142, v249\n"
    "v_mul_f32 v143, v143, v249\n"
    "v_mul_f32 v148, v148, v249\n"
    "v_mul_f32 v149, v149, v249\n"
    "v_mul_f32 v150, v150, v249\n"
    "v_mul_f32 v151, v151, v249\n"
    "v_mul_f32 v156, v156, v249\n"
    "v_mul_f32 v157, v157, v249\n"
    "v_mul_f32 v158, v158, v249\n"
    "v_mul_f32 v159, v159, v249\n"
    "v_mul_f32 v164, v164, v249\n"
    "v_mul_f32 v165, v165, v249\n"
    "v_mul_f32 v166, v166, v249\n"
    "v_mul_f32 v167, v167, v249\n"
    "v_mul_f32 v172, v172, v249\n"
    "v_mul_f32 v173, v173, v249\n"
    "v_mul_f32 v174, v174, v249\n"
    "v_mul_f32 v175, v175, v249\n"
    "v_sub_f32 v232, v232, v248\n"
    "v_sub_f32 v233, v233, v248\n"
    "v_sub_f32 v234, v234, v248\n"
    "v_sub_f32 v235, v235, v248\n"
    "v_sub_f32 v68, v68, v248\n"
    "v_sub_f32 v69, v69, v248\n"
    "v_sub_f32 v70, v70, v248\n"
    "v_sub_f32 v71, v71, v248\n"
    "v_sub_f32 v76, v76, v248\n"
    "v_sub_f32 v77, v77, v248\n"
    "v_sub_f32 v78, v78, v248\n"
    "v_sub_f32 v79, v79, v248\n"
    "v_sub_f32 v52, v52, v248\n"
    "v_sub_f32 v53, v53, v248\n"
    "v_sub_f32 v54, v54, v248\n"
    "v_sub_f32 v55, v55, v248\n"
    "v_sub_f32 v60, v60, v248\n"
    "v_sub_f32 v61, v61, v248\n"
    "v_sub_f32 v62, v62, v248\n"
    "v_sub_f32 v63, v63, v248\n"
    "v_exp_f32 v100, v52\n"
    "v_exp_f32 v101, v53\n"
    "v_exp_f32 v102, v54\n"
    "v_exp_f32 v103, v55\n"
    "v_exp_f32 v104, v60\n"
    "v_exp_f32 v105, v61\n"
    "v_exp_f32 v106, v62\n"
    "v_exp_f32 v107, v63\n"
    "v_cvt_pk_bf16_f32 v84, v100, v101\n"
    "v_cvt_pk_bf16_f32 v85, v102, v103\n"
    "v_cvt_pk_bf16_f32 v86, v104, v105\n"
    "v_cvt_pk_bf16_f32 v87, v106, v107\n"
    "v_max3_f32 v248, v56, v57, v58\n"
    "v_max3_f32 v248, v248, v59, v64\n"
    "v_max3_f32 v248, v248, v65, v66\n"
    "v_max_f32 v248, v248, v67\n"
    "v_mov_b32 v249, v248\n"
    "s_nop 1\n"
    "v_permlane32_swap_b32 v248, v249\n"
    "v_max_f32 v248, v248, v249\n"
    "v_mov_b32 v249, v248\n"
    "s_nop 1\n"
    "v_permlane16_swap_b32 v248, v249\n"
    "v_max_f32 v248, v248, v249\n"
    "v_max_f32 v248, 0, v248\n"
    "v_exp_f32 v249, -v248\n"
    "s_nop 0\n"
    "v_mul_f32 v228, v228, v249\n"
    "v_mul_f32 v229, v229, v249\n"
    "v_mul_f32 v230, v230, v249\n"
    "v_mul_f32 v231, v231, v249\n"
    "v_mul_f32 v120, v120, v249\n"
    "v_mul_f32 v121, v121, v249\n"
    "v_mul_f32 v122, v122, v249\n"
    "v_mul_f32 v123, v123, v249\n"
    "v_mul_f32 v128, v128, v249\n"
    "v_mul_f32 v129, v129, v249\n"
    "v_mul_f32 v130, v130, v249\n"
    "v_mul_f32 v131, v131, v249\n"
    "v_mul_f32 v136, v136, v249\n"
    "v_mul_f32 v137, v137, v249\n"
    "v_mul_f32 v138, v138, v249\n"
    "v_mul_f32 v139, v139, v249\n"
    "v_mul_f32 v144, v144, v249\n"
    "v_mul_f32 v145, v145, v249\n"
    "v_mul_f32 v146, v146, v249\n"
    "v_mul_f32 v147, v147, v249\n"
    "v_mul_f32 v152, v152, v249\n"
    "v_mul_f32 v153, v153, v249\n"
    "v_mul_f32 v154, v154, v249\n"
    "v_mul_f32 v155, v155, v249\n"
    "v_mul_f32 v160, v160, v249\n"
    "v_mul_f32 v161, v161, v249\n"
    "v_mul_f32 v162, v162, v249\n"
    "v_mul_f32 v163, v163, v249\n"
    "v_mul_f32 v168, v168, v249\n"
    "v_mul_f32 v169, v169, v249\n"
    "v_mul_f32 v170, v170, v249\n"
    "v_mul_f32 v171, v171, v249\n"
    "v_mul_f32 v176, v176, v249\n"
    "v_mul_f32 v177, v177, v249\n"
    "v_mul_f32 v178, v178, v249\n"
    "v_mul_f32 v179, v179, v249\n"
    "v_sub_f32 v236, v236, v248\n"
    "v_sub_f32 v237, v237, v248\n"
    "v_sub_f32 v238, v238, v248\n"
    "v_sub_f32 v239, v239, v248\n"
    "v_sub_f32 v72, v72, v248\n"
    "v_sub_f32 v73, v73, v248\n"
    "v_sub_f32 v74, v74, v248\n"
    "v_sub_f32 v75, v75, v248\n"
    "v_sub_f32 v80, v80, v248\n"
    "v_sub_f32 v81, v81, v248\n"
    "v_sub_f32 v82, v82, v248\n"
    "v_sub_f32 v83, v83, v248\n"
    "v_sub_f32 v56, v56, v248\n"
    "v_sub_f32 v57, v57, v248\n"
    "v_sub_f32 v58, v58, v248\n"
    "v_sub_f32 v59, v59, v248\n"
    "v_sub_f32 v64, v64, v248\n"
    "v_sub_f32 v65, v65, v248\n"
    "v_sub_f32 v66, v66, v248\n"
    "v_sub_f32 v67, v67, v248\n"
    "v_exp_f32 v100, v56\n"
    "v_exp_f32 v101, v57\n"
    "v_exp_f32 v102, v58\n"
    "v_exp_f32 v103, v59\n"
    "v_exp_f32 v104, v64\n"
    "v_exp_f32 v105, v65\n"
    "v_exp_f32 v106, v66\n"
    "v_exp_f32 v107, v67\n"
    "v_cvt_pk_bf16_f32 v88, v100, v101\n"
    "v_cvt_pk_bf16_f32 v89, v102, v103\n"
    "v_cvt_pk_bf16_f32 v90, v104, v105\n"
    "v_cvt_pk_bf16_f32 v91, v106, v107\n"
    "v_mfma_f32_16x16x32_bf16 v[216:219], v[212:215], v[84:87], v[224:227]\n"
    "s_nop 0\n"
    "v_mfma_f32_16x16x32_bf16 v[220:223], v[212:215], v[88:91], v[228:231]\n"
    "s_nop 9\n"
    "s_branch L_ret_eva_%=\n"
    "L_hretry_%=:\n"
    "s_mov_b32 s89, 0x40000\n"
    "L_sp_h_%=:\n"
    "global_load_dwordx2 v[108:109], v7, s[58:59] offset:1024 sc1\n"
    "s_waitcnt vmcnt(0)\n"
    "v_cmp_ne_u64 vcc, s[84:85], v[108:109]\n"
    "s_nop 0\n"
    "s_cbranch_vccz L_spd_h_%=\n"
    "s_sleep 1\n"
    "s_sub_u32 s89, s89, 1\n"
    "s_cmp_lg_u32 s89, 0\n"
    "s_cbranch_scc1 L_sp_h_%=\n"
    "L_spd_h_%=:\n"
    "s_branch L_hok_%=\n"
    "L_rare_od5_%=:\n"
    "s_nop 15\n"
    "v_max3_f32 v248, v68, v69, v70\n"
    "v_max3_f32 v248, v248, v71, v76\n"
    "v_max3_f32 v248, v248, v77, v78\n"
    "v_max_f32 v248, v248, v79\n"
    "v_mov_b32 v249, v248\n"
    "s_nop 1\n"
    "v_permlane32_swap_b32 v248, v249\n"
    "v_max_f32 v248, v248, v249\n"
    "v_mov_b32 v249, v248\n"
    "s_nop 1\n"
    "v_permlane16_swap_b32 v248, v249\n"
    "v_max_f32 v248, v248, v249\n"
    "v_max_f32 v248, 0, v248\n"
    "v_exp_f32 v249, -v248\n"
    "s_nop 0\n"
    "v_mul_f32 v216, v216, v249\n"
    "v_mul_f32 v217, v217, v249\n"
    "v_mul_f32 v218, v218, v249\n"
    "v_mul_f32 v219, v219, v249\n"
    "v_mul_f32 v116, v116, v249\n"
    "v_mul_f32 v117, v117, v249\n"
    "v_mul_f32 v118, v118, v249\n"
    "v_mul_f32 v119, v119, v249\n"
    "v_mul_f32 v124, v124, v249\n"
    "v_mul_f32 v125, v125, v249\n"
    "v_mul_f32 v126, v126, v249\n"
    "v_mul_f32 v127, v127, v249\n"
    "v_mul_f32 v132, v132, v249\n"
    "v_mul_f32 v133, v133, v249\n"
    "v_mul_f32 v134, v134, v249\n"
    "v_mul_f32 v135, v135, v249\n"
    "v_mul_f32 v140, v140, v249\n"
    "v_mul_f32 v141, v141, v249\n"
    "v_mul_f32 v142, v142, v249\n"
    "v_mul_f32 v143, v143, v249\n"
    "v_mul_f32 v148, v148, v249\n"
    "v_mul_f32 v149, v149, v249\n"
    "v_mul_f32 v150, v150, v249\n"
    "v_mul_f32 v151, v151, v249\n"
    "v_mul_f32 v156, v156, v249\n"
    "v_mul_f32 v157, v157, v249\n"
    "v_mul_f32 v158, v158, v249\n"
    "v_mul_f32 v159, v159, v249\n"
    "v_mul_f32 v164, v164, v249\n"
    "v_mul_f32 v165, v165, v249\n"
    "v_mul_f32 v166, v166, v249\n"
    "v_mul_f32 v167, v167, v249\n"
    "v_mul_f32 v172, v172, v249\n"
    "v_mul_f32 v173, v173, v249\n"
    "v_mul_f32 v174, v174, v249\n"
    "v_mul_f32 v175, v175, v249\n"
    "v_sub_f32 v232, v232, v248\n"
    "v_sub_f32 v233, v233, v248\n"
    "v_sub_f32 v234, v234, v248\n"
    "v_sub_f32 v235, v235, v248\n"
    "v_sub_f32 v52, v52, v248\n"
    "v_sub_f32 v53, v53, v248\n"
    "v_sub_f32 v54, v54, v248\n"
    "v_sub_f32 v55, v55, v248\n"
    "v_sub_f32 v60, v60, v248\n"
    "v_sub_f32 v61, v61, v248\n"
    "v_sub_f32 v62, v62, v248\n"
    "v_sub_f32 v63, v63, v248\n"
    "v_sub_f32 v68, v68, v248\n"
    "v_sub_f32 v69, v69, v248\n"
    "v_sub_f32 v70, v70, v248\n"
    "v_sub_f32 v71, v71, v248\n"
    "v_sub_f32 v76, v76, v248\n"
    "v_sub_f32 v77, v77, v248\n"
    "v_sub_f32 v78, v78, v248\n"
    "v_sub_f32 v79, v79, v248\n"
    "v_exp_f32 v100, v68\n"
    "v_exp_f32 v101, v69\n"
    "v_exp_f32 v102, v70\n"
    "v_exp_f32 v103, v71\n"
    "v_exp_f32 v104, v76\n"
    "v_exp_f32 v105, v77\n"
    "v_exp_f32 v106, v78\n"
    "v_exp_f32 v107, v79\n"
    "v_cvt_pk_bf16_f32 v92, v100, v101\n"
    "v_cvt_pk_bf16_f32 v93, v102, v103\n"
    "v_cvt_pk_bf16_f32 v94, v104, v105\n"
    "v_cvt_pk_bf16_f32 v95, v106, v107\n"
    "v_max3_f32 v248, v72, v73, v74\n"
    "v_max3_f32 v248, v248, v75, v80\n"
    "v_max3_f32 v248, v248, v81, v82\n"
    "v_max_f32 v248, v248, v83\n"
    "v_mov_b32 v249, v248\n"
    "s_nop 1\n"
    "v_permlane32_swap_b32 v248, v249\n"
    "v_max_f32 v248, v248, v249\n"
    "v_mov_b32 v249, v248\n"
    "s_nop 1\n"
    "v_permlane16_swap_b32 v248, v249\n"
    "v_max_f32 v248, v248, v249\n"
    "v_max_f32 v248, 0, v248\n"
    "v_exp_f32 v249, -v248\n"
    "s_nop 0\n"
    "v_mul_f32 v220, v220, v249\n"
    "v_mul_f32 v221, v221, v249\n"
    "v_mul_f32 v222, v222, v249\n"
    "v_mul_f32 v223, v223, v249\n"
    "v_mul_f32 v120, v120, v249\n"
    "v_mul_f32 v121, v121, v249\n"
    "v_mul_f32 v122, v122, v249\n"
    "v_mul_f32 v123, v123, v249\n"
    "v_mul_f32 v128, v128, v249\n"
    "v_mul_f32 v129, v129, v249\n"
    "v_mul_f32 v130, v130, v249\n"
    "v_mul_f32 v131, v131, v249\n"
    "v_mul_f32 v136, v136, v249\n"
    "v_mul_f32 v137, v137, v249\n"
    "v_mul_f32 v138, v138, v249\n"
    "v_mul_f32 v139, v139, v249\n"
    "v_mul_f32 v144, v144, v249\n"
    "v_mul_f32 v145, v145, v249\n"
    "v_mul_f32 v146, v146, v249\n"
    "v_mul_f32 v147, v147, v249\n"
    "v_mul_f32 v152, v152, v249\n"
    "v_mul_f32 v153, v153, v249\n"
    "v_mul_f32 v154, v154, v249\n"
    "v_mul_f32 v155, v155, v249\n"
    "v_mul_f32 v160, v160, v249\n"
    "v_mul_f32 v161, v161, v249\n"
    "v_mul_f32 v162, v162, v249\n"
    "v_mul_f32 v163, v163, v249\n"
    "v_mul_f32 v168, v168, v249\n"
    "v_mul_f32 v169, v169, v249\n"
    "v_mul_f32 v170, v170, v249\n"
    "v_mul_f32 v171, v171, v249\n"
    "v_mul_f32 v176, v176, v249\n"
    "v_mul_f32 v177, v177, v249\n"
    "v_mul_f32 v178, v178, v249\n"
    "v_mul_f32 v179, v179, v249\n"
    "v_sub_f32 v236, v236, v248\n"
    "v_sub_f32 v237, v237, v248\n"
    "v_sub_f32 v238, v238, v248\n"
    "v_sub_f32 v239, v239, v248\n"
    "v_sub_f32 v56, v56, v248\n"
    "v_sub_f32 v57, v57, v248\n"
    "v_sub_f32 v58, v58, v248\n"
    "v_sub_f32 v59, v59, v248\n"
    "v_sub_f32 v64, v64, v248\n"
    "v_sub_f32 v65, v65, v248\n"
    "v_sub_f32 v66, v66, v248\n"
    "v_sub_f32 v67, v67, v248\n"
    "v_sub_f32 v72, v72, v248\n"
    "v_sub_f32 v73, v73, v248\n"
    "v_sub_f32 v74, v74, v248\n"
    "v_sub_f32 v75, v75, v248\n"
    "v_sub_f32 v80, v80, v248\n"
    "v_sub_f32 v81, v81, v248\n"
    "v_sub_f32 v82, v82, v248\n"
    "v_sub_f32 v83, v83, v248\n"
    "v_exp_f32 v100, v72\n"
    "v_exp_f32 v101, v73\n"
    "v_exp_f32 v102, v74\n"
    "v_exp_f32 v103, v75\n"
    "v_exp_f32 v104, v80\n"
    "v_exp_f32 v105, v81\n"
    "v_exp_f32 v106, v82\n"
    "v_exp_f32 v107, v83\n"
    "v_cvt_pk_bf16_f32 v96, v100, v101\n"
    "v_cvt_pk_bf16_f32 v97, v102, v103\n"
    "v_cvt_pk_bf16_f32 v98, v104, v105\n"
    "v_cvt_pk_bf16_f32 v99, v106, v107\n"
    "v_mfma_f32_16x16x32_bf16 v[224:227], v[212:215], v[92:95], v[216:219]\n"
    "s_nop 0\n"
    "v_mfma_f32_16x16x32_bf16 v[228:231], v[212:215], v[96:99], v[220:223]\n"
    "s_nop 9\n"
    "s_branch L_ret_od5_%=\n"
    "L_rare_ev5_%=:\n"
    "s_nop 15\n"
    "v_max3_f32 v248, v52, v53, v54\n"
    "v_max3_f32 v248, v248, v55, v60\n"
    "v_max3_f32 v248, v248, v61, v62\n"
    "v_max_f32 v248, v248, v63\n"
    "v_mov_b32 v249, v248\n"
    "s_nop 1\n"
    "v_permlane32_swap_b32 v248, v249\n"
    "v_max_f32 v248, v248, v249\n"
    "v_mov_b32 v249, v248\n"
    "s_nop 1\n"
    "v_permlane16_swap_b32 v248, v249\n"
    "v_max_f32 v248, v248, v249\n"
    "v_max_f32 v248, 0, v248\n"
    "v_exp_f32 v249, -v248\n"
    "s_nop 0\n"
    "v_mul_f32 v224, v224, v249\n"
    "v_mul_f32 v225, v225, v249\n"
    "v_mul_f32 v226, v226, v249\n"
    "v_mul_f32 v227, v227, v249\n"
    "v_mul_f32 v116, v116, v249\n"
    "v_mul_f32 v117, v117, v249\n"
    "v_mul_f32 v118, v118, v249\n"
    "v_mul_f32 v119, v119, v249\n"
    "v_mul_f32 v124, v124, v249\n"
    "v_mul_f32 v125, v125, v249\n"
    "v_mul_f32 v126, v126, v249\n"
    "v_mul_f32 v127, v127, v249\n"
    "v_mul_f32 v132, v132, v249\n"
    "v_mul_f32 v133, v133, v249\n"
    "v_mul_f32 v134, v134, v249\n"
    "v_mul_f32 v135, v135, v249\n"
    "v_mul_f32 v140, v140, v249\n"
    "v_mul_f32 v141, v141, v249\n"
    "v_mul_f32 v142, v142, v249\n"
    "v_mul_f32 v143, v143, v249\n"
    "v_mul_f32 v148, v148, v249\n"
    "v_mul_f32 v149, v149, v249\n"
    "v_mul_f32 v150, v150, v249\n"
    "v_mul_f32 v151, v151, v249\n"
    "v_mul_f32 v156, v156, v249\n"
    "v_mul_f32 v157, v157, v249\n"
    "v_mul_f32 v158, v158, v249\n"
    "v_mul_f32 v159, v159, v249\n"
    "v_mul_f32 v164, v164, v249\n"
    "v_mul_f32 v165, v165, v249\n"
    "v_mul_f32 v166, v166, v249\n"
    "v_mul_f32 v167, v167, v249\n"
    "v_mul_f32 v172, v172, v249\n"
    "v_mul_f32 v173, v173, v249\n"
    "v_mul_f32 v174, v174, v249\n"
    "v_mul_f32 v175, v175, v249\n"
    "v_sub_f32 v232, v232, v248\n"
    "v_sub_f32 v233, v233, v248\n"
    "v_sub_f32 v234, v234, v248\n"
    "v_sub_f32 v235, v235, v248\n"
    "v_sub_f32 v68, v68, v248\n"
    "v_sub_f32 v69, v69, v248\n"
    "v_sub_f32 v70, v70, v248\n"
    "v_sub_f32 v71, v71, v248\n"
    "v_sub_f32 v76, v76, v248\n"
    "v_sub_f32 v77, v77, v248\n"
    "v_sub_f32 v78, v78, v248\n"
    "v_sub_f32 v79, v79, v248\n"
    "v_sub_f32 v52, v52, v248\n"
    "v_sub_f32 v53, v53, v248\n"
    "v_sub_f32 v54, v54, v248\n"
    "v_sub_f32 v55, v55, v248\n"
    "v_sub_f32 v60, v60, v248\n"
    "v_sub_f32 v61, v61, v248\n"
    "v_sub_f32 v62, v62, v248\n"
    "v_sub_f32 v63, v63, v248\n"
    "v_exp_f32 v100, v52\n"
    "v_exp_f32 v101, v53\n"
    "v_exp_f32 v102, v54\n"
    "v_exp_f32 v103, v55\n"
    "v_exp_f32 v104, v60\n"
    "v_exp_f32 v105, v61\n"
    "v_exp_f32 v106, v62\n"
    "v_exp_f32 v107, v63\n"
    "v_cvt_pk_bf16_f32 v84, v100, v101\n"
    "v_cvt_pk_bf16_f32 v85, v102, v103\n"
    "v_cvt_pk_bf16_f32 v86, v104, v105\n"
    "v_cvt_pk_bf16_f32 v87, v106, v107\n"
    "v_max3_f32 v248, v56, v57, v58\n"
    "v_max3_f32 v248, v248, v59, v64\n"
    "v_max3_f32 v248, v248, v65, v66\n"
    "v_max_f32 v248, v248, v67\n"
    "v_mov_b32 v249, v248\n"
    "s_nop 1\n"
    "v_permlane32_swap_b32 v248, v249\n"
    "v_max_f32 v248, v248, v249\n"
    "v_mov_b32 v249, v248\n"
    "s_nop 1\n"
    "v_permlane16_swap_b32 v248, v249\n"
    "v_max_f32 v248, v248, v249\n"
    "v_max_f32 v248, 0, v248\n"
    "v_exp_f32 v249, -v248\n"
    "s_nop 0\n"
    "v_mul_f32 v228, v228, v249\n"
    "v_mul_f32 v229, v229, v249\n"
    "v_mul_f32 v230, v230, v249\n"
    "v_mul_f32 v231, v231, v249\n"
    "v_mul_f32 v120, v120, v249\n"
    "v_mul_f32 v121, v121, v249\n"
    "v_mul_f32 v122, v122, v249\n"
    "v_mul_f32 v123, v123, v249\n"
    "v_mul_f32 v128, v128, v249\n"
    "v_mul_f32 v129, v129, v249\n"
    "v_mul_f32 v130, v130, v249\n"
    "v_mul_f32 v131, v131, v249\n"
    "v_mul_f32 v136, v136, v249\n"
    "v_mul_f32 v137, v137, v249\n"
    "v_mul_f32 v138, v138, v249\n"
    "v_mul_f32 v139, v139, v249\n"
    "v_mul_f32 v144, v144, v249\n"
    "v_mul_f32 v145, v145, v249\n"
    "v_mul_f32 v146, v146, v249\n"
    "v_mul_f32 v147, v147, v249\n"
    "v_mul_f32 v152, v152, v249\n"
    "v_mul_f32 v153, v153, v249\n"
    "v_mul_f32 v154, v154, v249\n"
    "v_mul_f32 v155, v155, v249\n"
    "v_mul_f32 v160, v160, v249\n"
    "v_mul_f32 v161, v161, v249\n"
    "v_mul_f32 v162, v162, v249\n"
    "v_mul_f32 v163, v163, v249\n"
    "v_mul_f32 v168, v168, v249\n"
    "v_mul_f32 v169, v169, v249\n"
    "v_mul_f32 v170, v170, v249\n"
    "v_mul_f32 v171, v171, v249\n"
    "v_mul_f32 v176, v176, v249\n"
    "v_mul_f32 v177, v177, v249\n"
    "v_mul_f32 v178, v178, v249\n"
    "v_mul_f32 v179, v179, v249\n"
    "v_sub_f32 v236, v236, v248\n"
    "v_sub_f32 v237, v237, v248\n"
    "v_sub_f32 v238, v238, v248\n"
    "v_sub_f32 v239, v239, v248\n"
    "v_sub_f32 v72, v72, v248\n"
    "v_sub_f32 v73, v73, v248\n"
    "v_sub_f32 v74, v74, v248\n"
    "v_sub_f32 v75, v75, v248\n"
    "v_sub_f32 v80, v80, v248\n"
    "v_sub_f32 v81, v81, v248\n"
    "v_sub_f32 v82, v82, v248\n"
    "v_sub_f32 v83, v83, v248\n"
    "v_sub_f32 v56, v56, v248\n"
    "v_sub_f32 v57, v57, v248\n"
    "v_sub_f32 v58, v58, v248\n"
    "v_sub_f32 v59, v59, v248\n"
    "v_sub_f32 v64, v64, v248\n"
    "v_sub_f32 v65, v65, v248\n"
    "v_sub_f32 v66, v66, v248\n"
    "v_sub_f32 v67, v67, v248\n"
    "v_exp_f32 v100, v56\n"
    "v_exp_f32 v101, v57\n"
    "v_exp_f32 v102, v58\n"
    "v_exp_f32 v103, v59\n"
    "v_exp_f32 v104, v64\n"
    "v_exp_f32 v105, v65\n"
    "v_exp_f32 v106, v66\n"
    "v_exp_f32 v107, v67\n"
    "v_cvt_pk_bf16_f32 v88, v100, v101\n"
    "v_cvt_pk_bf16_f32 v89, v102, v103\n"
    "v_cvt_pk_bf16_f32 v90, v104, v105\n"
    "v_cvt_pk_bf16_f32 v91, v106, v107\n"
    "v_mfma_f32_16x16x32_bf16 v[216:219], v[212:215], v[84:87], v[224:227]\n"
    "s_nop 0\n"
    "v_mfma_f32_16x16x32_bf16 v[220:223], v[212:215], v[88:91], v[228:231]\n"
    "s_nop 9\n"
    "s_branch L_ret_ev5_%=\n"
    "L_rare_od6_%=:\n"
    "s_nop 15\n"
    "v_max3_f32 v248, v68, v69, v70\n"
    "v_max3_f32 v248, v248, v71, v76\n"
    "v_max3_f32 v248, v248, v77, v78\n"
    "v_max_f32 v248, v248, v79\n"
    "v_mov_b32 v249, v248\n"
    "s_nop 1\n"
    "v_permlane32_swap_b32 v248, v249\n"
    "v_max_f32 v248, v248, v249\n"
    "v_mov_b32 v249, v248\n"
    "s_nop 1\n"
    "v_permlane16_swap_b32 v248, v249\n"
    "v_max_f32 v248, v248, v249\n"
    "v_max_f32 v248, 0, v248\n"
    "v_exp_f32 v249, -v248\n"
    "s_nop 0\n"
    "v_mul_f32 v216, v216, v249\n"
    "v_mul_f32 v217, v217, v249\n"
    "v_mul_f32 v218, v218, v249\n"
    "v_mul_f32 v219, v219, v249\n"
    "v_mul_f32 v116, v116, v249\n"
    "v_mul_f32 v117, v117, v249\n"
    "v_mul_f32 v118, v118, v249\n"
    "v_mul_f32 v119, v119, v249\n"
    "v_mul_f32 v124, v124, v249\n"
    "v_mul_f32 v125, v125, v249\n"
    "v_mul_f32 v126, v126, v249\n"
    "v_mul_f32 v127, v127, v249\n"
    "v_mul_f32 v132, v132, v249\n"
    "v_mul_f32 v133, v133, v249\n"
    "v_mul_f32 v134, v134, v249\n"
    "v_mul_f32 v135, v135, v249\n"
    "v_mul_f32 v140, v140, v249\n"
    "v_mul_f32 v141, v141, v249\n"
    "v_mul_f32 v142, v142, v249\n"
    "v_mul_f32 v143, v143, v249\n"
    "v_mul_f32 v148, v148, v249\n"
    "v_mul_f32 v149, v149, v249\n"
    "v_mul_f32 v150, v150, v249\n"
    "v_mul_f32 v151, v151, v249\n"
    "v_mul_f32 v156, v156, v249\n"
    "v_mul_f32 v157, v157, v249\n"
    "v_mul_f32 v158, v158, v249\n"
    "v_mul_f32 v159, v159, v249\n"
    "v_mul_f32 v164, v164, v249\n"
    "v_mul_f32 v165, v165, v249\n"
    "v_mul_f32 v166, v166, v249\n"
    "v_mul_f32 v167, v167, v249\n"
    "v_mul_f32 v172, v172, v249\n"
    "v_mul_f32 v173, v173, v249\n"
    "v_mul_f32 v174, v174, v249\n"
    "v_mul_f32 v175, v175, v249\n"
    "v_sub_f32 v232, v232, v248\n"
    "v_sub_f32 v233, v233, v248\n"
    "v_sub_f32 v234, v234, v248\n"
    "v_sub_f32 v235, v235, v248\n"
    "v_sub_f32 v52, v52, v248\n"
    "v_sub_f32 v53, v53, v248\n"
    "v_sub_f32 v54, v54, v248\n"
    "v_sub_f32 v55, v55, v248\n"
    "v_sub_f32 v60, v60, v248\n"
    "v_sub_f32 v61, v61, v248\n"
    "v_sub_f32 v62, v62, v248\n"
    "v_sub_f32 v63, v63, v248\n"
    "v_sub_f32 v68, v68, v248\n"
    "v_sub_f32 v69, v69, v248\n"
    "v_sub_f32 v70, v70, v248\n"
    "v_sub_f32 v71, v71, v248\n"
    "v_sub_f32 v76, v76, v248\n"
    "v_sub_f32 v77, v77, v248\n"
    "v_sub_f32 v78, v78, v248\n"
    "v_sub_f32 v79, v79, v248\n"
    "v_exp_f32 v100, v68\n"
    "v_exp_f32 v101, v69\n"
    "v_exp_f32 v102, v70\n"
    "v_exp_f32 v103, v71\n"
    "v_exp_f32 v104, v76\n"
    "v_exp_f32 v105, v77\n"
    "v_exp_f32 v106, v78\n"
    "v_exp_f32 v107, v79\n"
    "v_cvt_pk_bf16_f32 v92, v100, v101\n"
    "v_cvt_pk_bf16_f32 v93, v102, v103\n"
    "v_cvt_pk_bf16_f32 v94, v104, v105\n"
    "v_cvt_pk_bf16_f32 v95, v106, v107\n"
    "v_max3_f32 v248, v72, v73, v74\n"
    "v_max3_f32 v248, v248, v75, v80\n"
    "v_max3_f32 v248, v248, v81, v82\n"
    "v_max_f32 v248, v248, v83\n"
    "v_mov_b32 v249, v248\n"
    "s_nop 1\n"
    "v_permlane32_swap_b32 v248, v249\n"
    "v_max_f32 v248, v248, v249\n"
    "v_mov_b32 v249, v248\n"
    "s_nop 1\n"
    "v_permlane16_swap_b32 v248, v249\n"
    "v_max_f32 v248, v248, v249\n"
    "v_max_f32 v248, 0, v248\n"
    "v_exp_f32 v249, -v248\n"
    "s_nop 0\n"
    "v_mul_f32 v220, v220, v249\n"
    "v_mul_f32 v221, v221, v249\n"
    "v_mul_f32 v222, v222, v249\n"
    "v_mul_f32 v223, v223, v249\n"
    "v_mul_f32 v120, v120, v249\n"
    "v_mul_f32 v121, v121, v249\n"
    "v_mul_f32 v122, v122, v249\n"
    "v_mul_f32 v123, v123, v249\n"
    "v_mul_f32 v128, v128, v249\n"
    "v_mul_f32 v129, v129, v249\n"
    "v_mul_f32 v130, v130, v249\n"
    "v_mul_f32 v131, v131, v249\n"
    "v_mul_f32 v136, v136, v249\n"
    "v_mul_f32 v137, v137, v249\n"
    "v_mul_f32 v138, v138, v249\n"
    "v_mul_f32 v139, v139, v249\n"
    "v_mul_f32 v144, v144, v249\n"
    "v_mul_f32 v145, v145, v249\n"
    "v_mul_f32 v146, v146, v249\n"
    "v_mul_f32 v147, v147, v249\n"
    "v_mul_f32 v152, v152, v249\n"
    "v_mul_f32 v153, v153, v249\n"
    "v_mul_f32 v154, v154, v249\n"
    "v_mul_f32 v155, v155, v249\n"
    "v_mul_f32 v160, v160, v249\n"
    "v_mul_f32 v161, v161, v249\n"
    "v_mul_f32 v162, v162, v249\n"
    "v_mul_f32 v163, v163, v249\n"
    "v_mul_f32 v168, v168, v249\n"
    "v_mul_f32 v169, v169, v249\n"
    "v_mul_f32 v170, v170, v249\n"
    "v_mul_f32 v171, v171, v249\n"
    "v_mul_f32 v176, v176, v249\n"
    "v_mul_f32 v177, v177, v249\n"
    "v_mul_f32 v178, v178, v249\n"
    "v_mul_f32 v179, v179, v249\n"
    "v_sub_f32 v236, v236, v248\n"
    "v_sub_f32 v237, v237, v248\n"
    "v_sub_f32 v238, v238, v248\n"
    "v_sub_f32 v239, v239, v248\n"
    "v_sub_f32 v56, v56, v248\n"
    "v_sub_f32 v57, v57, v248\n"
    "v_sub_f32 v58, v58, v248\n"
    "v_sub_f32 v59, v59, v248\n"
    "v_sub_f32 v64, v64, v248\n"
    "v_sub_f32 v65, v65, v248\n"
    "v_sub_f32 v66, v66, v248\n"
    "v_sub_f32 v67, v67, v248\n"
    "v_sub_f32 v72, v72, v248\n"
    "v_sub_f32 v73, v73, v248\n"
    "v_sub_f32 v74, v74, v248\n"
    "v_sub_f32 v75, v75, v248\n"
    "v_sub_f32 v80, v80, v248\n"
    "v_sub_f32 v81, v81, v248\n"
    "v_sub_f32 v82, v82, v248\n"
    "v_sub_f32 v83, v83, v248\n"
    "v_exp_f32 v100, v72\n"
    "v_exp_f32 v101, v73\n"
    "v_exp_f32 v102, v74\n"
    "v_exp_f32 v103, v75\n"
    "v_exp_f32 v104, v80\n"
    "v_exp_f32 v105, v81\n"
    "v_exp_f32 v106, v82\n"
    "v_exp_f32 v107, v83\n"
    "v_cvt_pk_bf16_f32 v96, v100, v101\n"
    "v_cvt_pk_bf16_f32 v97, v102, v103\n"
    "v_cvt_pk_bf16_f32 v98, v104, v105\n"
    "v_cvt_pk_bf16_f32 v99, v106, v107\n"
    "v_mfma_f32_16x16x32_bf16 v[224:227], v[212:215], v[92:95], v[216:219]\n"
    "s_nop 0\n"
    "v_mfma_f32_16x16x32_bf16 v[228:231], v[212:215], v[96:99], v[220:223]\n"
    "s_nop 9\n"
    "s_branch L_ret_od6_%=\n"
    "L_rare_ev6_%=:\n"
    "s_nop 15\n"
    "v_max3_f32 v248, v52, v53, v54\n"
    "v_max3_f32 v248, v248, v55, v60\n"
    "v_max3_f32 v248, v248, v61, v62\n"
    "v_max_f32 v248, v248, v63\n"
    "v_mov_b32 v249, v248\n"
    "s_nop 1\n"
    "v_permlane32_swap_b32 v248, v249\n"
    "v_max_f32 v248, v248, v249\n"
    "v_mov_b32 v249, v248\n"
    "s_nop 1\n"
    "v_permlane16_swap_b32 v248, v249\n"
    "v_max_f32 v248, v248, v249\n"
    "v_max_f32 v248, 0, v248\n"
    "v_exp_f32 v249, -v248\n"
    "s_nop 0\n"
    "v_mul_f32 v224, v224, v249\n"
    "v_mul_f32 v225, v225, v249\n"
    "v_mul_f32 v226, v226, v249\n"
    "v_mul_f32 v227, v227, v249\n"
    "v_mul_f32 v116, v116, v249\n"
    "v_mul_f32 v117, v117, v249\n"
    "v_mul_f32 v118, v118, v249\n"
    "v_mul_f32 v119, v119, v249\n"
    "v_mul_f32 v124, v124, v249\n"
    "v_mul_f32 v125, v125, v249\n"
    "v_mul_f32 v126, v126, v249\n"
    "v_mul_f32 v127, v127, v249\n"
    "v_mul_f32 v132, v132, v249\n"
    "v_mul_f32 v133, v133, v249\n"
    "v_mul_f32 v134, v134, v249\n"
    "v_mul_f32 v135, v135, v249\n"
    "v_mul_f32 v140, v140, v249\n"
    "v_mul_f32 v141, v141, v249\n"
    "v_mul_f32 v142, v142, v249\n"
    "v_mul_f32 v143, v143, v249\n"
    "v_mul_f32 v148, v148, v249\n"
    "v_mul_f32 v149, v149, v249\n"
    "v_mul_f32 v150, v150, v249\n"
    "v_mul_f32 v151, v151, v249\n"
    "v_mul_f32 v156, v156, v249\n"
    "v_mul_f32 v157, v157, v249\n"
    "v_mul_f32 v158, v158, v249\n"
    "v_mul_f32 v159, v159, v249\n"
    "v_mul_f32 v164, v164, v249\n"
    "v_mul_f32 v165, v165, v249\n"
    "v_mul_f32 v166, v166, v249\n"
    "v_mul_f32 v167, v167, v249\n"
    "v_mul_f32 v172, v172, v249\n"
    "v_mul_f32 v173, v173, v249\n"
    "v_mul_f32 v174, v174, v249\n"
    "v_mul_f32 v175, v175, v249\n"
    "v_sub_f32 v232, v232, v248\n"
    "v_sub_f32 v233, v233, v248\n"
    "v_sub_f32 v234, v234, v248\n"
    "v_sub_f32 v235, v235, v248\n"
    "v_sub_f32 v68, v68, v248\n"
    "v_sub_f32 v69, v69, v248\n"
    "v_sub_f32 v70, v70, v248\n"
    "v_sub_f32 v71, v71, v248\n"
    "v_sub_f32 v76, v76, v248\n"
    "v_sub_f32 v77, v77, v248\n"
    "v_sub_f32 v78, v78, v248\n"
    "v_sub_f32 v79, v79, v248\n"
    "v_sub_f32 v52, v52, v248\n"
    "v_sub_f32 v53, v53, v248\n"
    "v_sub_f32 v54, v54, v248\n"
    "v_sub_f32 v55, v55, v248\n"
    "v_sub_f32 v60, v60, v248\n"
    "v_sub_f32 v61, v61, v248\n"
    "v_sub_f32 v62, v62, v248\n"
    "v_sub_f32 v63, v63, v248\n"
    "v_exp_f32 v100, v52\n"
    "v_exp_f32 v101, v53\n"
    "v_exp_f32 v102, v54\n"
    "v_exp_f32 v103, v55\n"
    "v_exp_f32 v104, v60\n"
    "v_exp_f32 v105, v61\n"
    "v_exp_f32 v106, v62\n"
    "v_exp_f32 v107, v63\n"
    "v_cvt_pk_bf16_f32 v84, v100, v101\n"
    "v_cvt_pk_bf16_f32 v85, v102, v103\n"
    "v_cvt_pk_bf16_f32 v86, v104, v105\n"
    "v_cvt_pk_bf16_f32 v87, v106, v107\n"
    "v_max3_f32 v248, v56, v57, v58\n"
    "v_max3_f32 v248, v248, v59, v64\n"
    "v_max3_f32 v248, v248, v65, v66\n"
    "v_max_f32 v248, v248, v67\n"
    "v_mov_b32 v249, v248\n"
    "s_nop 1\n"
    "v_permlane32_swap_b32 v248, v249\n"
    "v_max_f32 v248, v248, v249\n"
    "v_mov_b32 v249, v248\n"
    "s_nop 1\n"
    "v_permlane16_swap_b32 v248, v249\n"
    "v_max_f32 v248, v248, v249\n"
    "v_max_f32 v248, 0, v248\n"
    "v_exp_f32 v249, -v248\n"
    "s_nop 0\n"
    "v_mul_f32 v228, v228, v249\n"
    "v_mul_f32 v229, v229, v249\n"
    "v_mul_f32 v230, v230, v249\n"
    "v_mul_f32 v231, v231, v249\n"
    "v_mul_f32 v120, v120, v249\n"
    "v_mul_f32 v121, v121, v249\n"
    "v_mul_f32 v122, v122, v249\n"
    "v_mul_f32 v123, v123, v249\n"
    "v_mul_f32 v128, v128, v249\n"
    "v_mul_f32 v129, v129, v249\n"
    "v_mul_f32 v130, v130, v249\n"
    "v_mul_f32 v131, v131, v249\n"
    "v_mul_f32 v136, v136, v249\n"
    "v_mul_f32 v137, v137, v249\n"
    "v_mul_f32 v138, v138, v249\n"
    "v_mul_f32 v139, v139, v249\n"
    "v_mul_f32 v144, v144, v249\n"
    "v_mul_f32 v145, v145, v249\n"
    "v_mul_f32 v146, v146, v249\n"
    "v_mul_f32 v147, v147, v249\n"
    "v_mul_f32 v152, v152, v249\n"
    "v_mul_f32 v153, v153, v249\n"
    "v_mul_f32 v154, v154, v249\n"
    "v_mul_f32 v155, v155, v249\n"
    "v_mul_f32 v160, v160, v249\n"
    "v_mul_f32 v161, v161, v249\n"
    "v_mul_f32 v162, v162, v249\n"
    "v_mul_f32 v163, v163, v249\n"
    "v_mul_f32 v168, v168, v249\n"
    "v_mul_f32 v169, v169, v249\n"
    "v_mul_f32 v170, v170, v249\n"
    "v_mul_f32 v171, v171, v249\n"
    "v_mul_f32 v176, v176, v249\n"
    "v_mul_f32 v177, v177, v249\n"
    "v_mul_f32 v178, v178, v249\n"
    "v_mul_f32 v179, v179, v249\n"
    "v_sub_f32 v236, v236, v248\n"
    "v_sub_f32 v237, v237, v248\n"
    "v_sub_f32 v238, v238, v248\n"
    "v_sub_f32 v239, v239, v248\n"
    "v_sub_f32 v72, v72, v248\n"
    "v_sub_f32 v73, v73, v248\n"
    "v_sub_f32 v74, v74, v248\n"
    "v_sub_f32 v75, v75, v248\n"
    "v_sub_f32 v80, v80, v248\n"
    "v_sub_f32 v81, v81, v248\n"
    "v_sub_f32 v82, v82, v248\n"
    "v_sub_f32 v83, v83, v248\n"
    "v_sub_f32 v56, v56, v248\n"
    "v_sub_f32 v57, v57, v248\n"
    "v_sub_f32 v58, v58, v248\n"
    "v_sub_f32 v59, v59, v248\n"
    "v_sub_f32 v64, v64, v248\n"
    "v_sub_f32 v65, v65, v248\n"
    "v_sub_f32 v66, v66, v248\n"
    "v_sub_f32 v67, v67, v248\n"
    "v_exp_f32 v100, v56\n"
    "v_exp_f32 v101, v57\n"
    "v_exp_f32 v102, v58\n"
    "v_exp_f32 v103, v59\n"
    "v_exp_f32 v104, v64\n"
    "v_exp_f32 v105, v65\n"
    "v_exp_f32 v106, v66\n"
    "v_exp_f32 v107, v67\n"
    "v_cvt_pk_bf16_f32 v88, v100, v101\n"
    "v_cvt_pk_bf16_f32 v89, v102, v103\n"
    "v_cvt_pk_bf16_f32 v90, v104, v105\n"
    "v_cvt_pk_bf16_f32 v91, v106, v107\n"
    "v_mfma_f32_16x16x32_bf16 v[216:219], v[212:215], v[84:87], v[224:227]\n"
    "s_nop 0\n"
    "v_mfma_f32_16x16x32_bf16 v[220:223], v[212:215], v[88:91], v[228:231]\n"
    "s_nop 9\n"
    "s_branch L_ret_ev6_%=\n"
    "L_rare_la_%=:\n"
    "s_nop 15\n"
    "v_max3_f32 v248, v68, v69, v70\n"
    "v_max3_f32 v248, v248, v71, v76\n"
    "v_max3_f32 v248, v248, v77, v78\n"
    "v_max_f32 v248, v248, v79\n"
    "v_mov_b32 v249, v248\n"
    "s_nop 1\n"
    "v_permlane32_swap_b32 v248, v249\n"
    "v_max_f32 v248, v248, v249\n"
    "v_mov_b32 v249, v248\n"
    "s_nop 1\n"
    "v_permlane16_swap_b32 v248, v249\n"
    "v_max_f32 v248, v248, v249\n"
    "v_max_f32 v248, 0, v248\n"
    "v_exp_f32 v249, -v248\n"
    "s_nop 0\n"
    "v_mul_f32 v216, v216, v249\n"
    "v_mul_f32 v217, v217, v249\n"
    "v_mul_f32 v218, v218, v249\n"
    "v_mul_f32 v219, v219, v249\n"
    "v_mul_f32 v116, v116, v249\n"
    "v_mul_f32 v117, v117, v249\n"
    "v_mul_f32 v118, v118, v249\n"
    "v_mul_f32 v119, v119, v249\n"
    "v_mul_f32 v124, v124, v249\n"
    "v_mul_f32 v125, v125, v249\n"
    "v_mul_f32 v126, v126, v249\n"
    "v_mul_f32 v127, v127, v249\n"
    "v_mul_f32 v132, v132, v249\n"
    "v_mul_f32 v133, v133, v249\n"
    "v_mul_f32 v134, v134, v249\n"
    "v_mul_f32 v135, v135, v249\n"
    "v_mul_f32 v140, v140, v249\n"
    "v_mul_f32 v141, v141, v249\n"
    "v_mul_f32 v142, v142, v249\n"
    "v_mul_f32 v143, v143, v249\n"
    "v_mul_f32 v148, v148, v249\n"
    "v_mul_f32 v149, v149, v249\n"
    "v_mul_f32 v150, v150, v249\n"
    "v_mul_f32 v151, v151, v249\n"
    "v_mul_f32 v156, v156, v249\n"
    "v_mul_f32 v157, v157, v249\n"
    "v_mul_f32 v158, v158, v249\n"
    "v_mul_f32 v159, v159, v249\n"
    "v_mul_f32 v164, v164, v249\n"
    "v_mul_f32 v165, v165, v249\n"
    "v_mul_f32 v166, v166, v249\n"
    "v_mul_f32 v167, v167, v249\n"
    "v_mul_f32 v172, v172, v249\n"
    "v_mul_f32 v173, v173, v249\n"
    "v_mul_f32 v174, v174, v249\n"
    "v_mul_f32 v175, v175, v249\n"
    "v_sub_f32 v232, v232, v248\n"
    "v_sub_f32 v233, v233, v248\n"
    "v_sub_f32 v234, v234, v248\n"
    "v_sub_f32 v235, v235, v248\n"
    "v_sub_f32 v52, v52, v248\n"
    "v_sub_f32 v53, v53, v248\n"
    "v_sub_f32 v54, v54, v248\n"
    "v_sub_f32 v55, v55, v248\n"
    "v_sub_f32 v60, v60, v248\n"
    "v_sub_f32 v61, v61, v248\n"
    "v_sub_f32 v62, v62, v248\n"
    "v_sub_f32 v63, v63, v248\n"
    "v_sub_f32 v68, v68, v248\n"
    "v_sub_f32 v69, v69, v248\n"
    "v_sub_f32 v70, v70, v248\n"
    "v_sub_f32 v71, v71, v248\n"
    "v_sub_f32 v76, v76, v248\n"
    "v_sub_f32 v77, v77, v248\n"
    "v_sub_f32 v78, v78, v248\n"
    "v_sub_f32 v79, v79, v248\n"
    "v_exp_f32 v100, v68\n"
    "v_exp_f32 v101, v69\n"
    "v_exp_f32 v102, v70\n"
    "v_exp_f32 v103, v71\n"
    "v_exp_f32 v104, v76\n"
    "v_exp_f32 v105, v77\n"
    "v_exp_f32 v106, v78\n"
    "v_exp_f32 v107, v79\n"
    "v_cvt_pk_bf16_f32 v92, v100, v101\n"
    "v_cvt_pk_bf16_f32 v93, v102, v103\n"
    "v_cvt_pk_bf16_f32 v94, v104, v105\n"
    "v_cvt_pk_bf16_f32 v95, v106, v107\n"
    "v_max3_f32 v248, v72, v73, v74\n"
    "v_max3_f32 v248, v248, v75, v80\n"
    "v_max3_f32 v248, v248, v81, v82\n"
    "v_max_f32 v248, v248, v83\n"
    "v_mov_b32 v249, v248\n"
    "s_nop 1\n"
    "v_permlane32_swap_b32 v248, v249\n"
    "v_max_f32 v248, v248, v249\n"
    "v_mov_b32 v249, v248\n"
    "s_nop 1\n"
    "v_permlane16_swap_b32 v248, v249\n"
    "v_max_f32 v248, v248, v249\n"
    "v_max_f32 v248, 0, v248\n"
    "v_exp_f32 v249, -v248\n"
    "s_nop 0\n"
    "v_mul_f32 v220, v220, v249\n"
    "v_mul_f32 v221, v221, v249\n"
    "v_mul_f32 v222, v222, v249\n"
    "v_mul_f32 v223, v223, v249\n"
    "v_mul_f32 v120, v120, v249\n"
    "v_mul_f32 v121, v121, v249\n"
    "v_mul_f32 v122, v122, v249\n"
    "v_mul_f32 v123, v123, v249\n"
    "v_mul_f32 v128, v128, v249\n"
    "v_mul_f32 v129, v129, v249\n"
    "v_mul_f32 v130, v130, v249\n"
    "v_mul_f32 v131, v131, v249\n"
    "v_mul_f32 v136, v136, v249\n"
    "v_mul_f32 v137, v137, v249\n"
    "v_mul_f32 v138, v138, v249\n"
    "v_mul_f32 v139, v139, v249\n"
    "v_mul_f32 v144, v144, v249\n"
    "v_mul_f32 v145, v145, v249\n"
    "v_mul_f32 v146, v146, v249\n"
    "v_mul_f32 v147, v147, v249\n"
    "v_mul_f32 v152, v152, v249\n"
    "v_mul_f32 v153, v153, v249\n"
    "v_mul_f32 v154, v154, v249\n"
    "v_mul_f32 v155, v155, v249\n"
    "v_mul_f32 v160, v160, v249\n"
    "v_mul_f32 v161, v161, v249\n"
    "v_mul_f32 v162, v162, v249\n"
    "v_mul_f32 v163, v163, v249\n"
    "v_mul_f32 v168, v168, v249\n"
    "v_mul_f32 v169, v169, v249\n"
    "v_mul_f32 v170, v170, v249\n"
    "v_mul_f32 v171, v171, v249\n"
    "v_mul_f32 v176, v176, v249\n"
    "v_mul_f32 v177, v177, v249\n"
    "v_mul_f32 v178, v178, v249\n"
    "v_mul_f32 v179, v179, v249\n"
    "v_sub_f32 v236, v236, v248\n"
    "v_sub_f32 v237, v237, v248\n"
    "v_sub_f32 v238, v238, v248\n"
    "v_sub_f32 v239, v239, v248\n"
    "v_sub_f32 v56, v56, v248\n"
    "v_sub_f32 v57, v57, v248\n"
    "v_sub_f32 v58, v58, v248\n"
    "v_sub_f32 v59, v59, v248\n"
    "v_sub_f32 v64, v64, v248\n"
    "v_sub_f32 v65, v65, v248\n"
    "v_sub_f32 v66, v66, v248\n"
    "v_sub_f32 v67, v67, v248\n"
    "v_sub_f32 v72, v72, v248\n"
    "v_sub_f32 v73, v73, v248\n"
    "v_sub_f32 v74, v74, v248\n"
    "v_sub_f32 v75, v75, v248\n"
    "v_sub_f32 v80, v80, v248\n"
    "v_sub_f32 v81, v81, v248\n"
    "v_sub_f32 v82, v82, v248\n"
    "v_sub_f32 v83, v83, v248\n"
    "v_exp_f32 v100, v72\n"
    "v_exp_f32 v101, v73\n"
    "v_exp_f32 v102, v74\n"
    "v_exp_f32 v103, v75\n"
    "v_exp_f32 v104, v80\n"
    "v_exp_f32 v105, v81\n"
    "v_exp_f32 v106, v82\n"
    "v_exp_f32 v107, v83\n"
    "v_cvt_pk_bf16_f32 v96, v100, v101\n"
    "v_cvt_pk_bf16_f32 v97, v102, v103\n"
    "v_cvt_pk_bf16_f32 v98, v104, v105\n"
    "v_cvt_pk_bf16_f32 v99, v106, v107\n"
    "v_mfma_f32_16x16x32_bf16 v[224:227], v[212:215], v[92:95], v[216:219]\n"
    "s_nop 0\n"
    "v_mfma_f32_16x16x32_bf16 v[228:231], v[212:215], v[96:99], v[220:223]\n"
    "s_nop 9\n"
    "s_branch L_ret_la_%=\n"
    "L_oretry_%=:\n"
    "s_mov_b32 s89, 0x40000\n"
    "L_sp_o_%=:\n"
    "global_load_dwordx2 v[252:253], v5, s[58:59] sc1\n"
    "s_waitcnt vmcnt(0)\n"
    "v_cmp_ne_u64 vcc, s[84:85], v[252:253]\n"
    "s_nop 0\n"
    "s_cbranch_vccz L_spd_o_%=\n"
    "s_sleep 1\n"
    "s_sub_u32 s89, s89, 1\n"
    "s_cmp_lg_u32 s89, 0\n"
    "s_cbranch_scc1 L_sp_o_%=\n"
    "L_spd_o_%=:\n"
    "s_branch L_ook_%=\n"
    "L_gretry_%=:\n"
    "s_mov_b32 s89, 0x40000\n"
    "L_sp_g_%=:\n"
    "global_load_dwordx2 v[252:253], v11, s[58:59] sc1\n"
    "s_waitcnt vmcnt(0)\n"
    "v_cmp_ne_u64 vcc, s[84:85], v[252:253]\n"
    "s_nop 0\n"
    "s_cbranch_vccz L_spd_g_%=\n"
    "s_sleep 1\n"
    "s_sub_u32 s89, s89, 1\n"
    "s_cmp_lg_u32 s89, 0\n"
    "s_cbranch_scc1 L_sp_g_%=\n"
    "L_spd_g_%=:\n"
    "s_branch L_gok_%=\n"
    "L_code1_%=:\n"
    : "+v"(tid) : "s"(__builtin_amdgcn_kernarg_segment_ptr()), "s"(__builtin_amdgcn_workgroup_id_x()) : "v1", "v2", "v3", "v4", "v5", "v6", "v7", "v8", "v9", "v10", "v11", "v12", "v13", "v14", "v15", "v16", "v17", "v18", "v19", "v20", "v21", "v22", "v23", "v24", "v25", "v26", "v27", "v28", "v29", "v30", "v31", "v32", "v33", "v34", "v35", "v36", "v37", "v38", "v39", "v40", "v41", "v42", "v43", "v44", "v45", "v46", "v47", "v48", "v49", "v50", "v51", "v52", "v53", "v54", "v55", "v56", "v57", "v58", "v59", "v60", "v61", "v62", "v63", "v64", "v65", "v66", "v67", "v68", "v69", "v70", "v71", "v72", "v73", "v74", "v75", "v76", "v77", "v78", "v79", "v80", "v81", "v82", "v83", "v84", "v85", "v86", "v87", "v88", "v89", "v90", "v91", "v92", "v93", "v94", "v95", "v96", "v97", "v98", "v99", "v100", "v101", "v102", "v103", "v104", "v105", "v106", "v107", "v108", "v109", "v110", "v111", "v112", "v113", "v114", "v115", "v116", "v117", "v118", "v119", "v120", "v121", "v122", "v123", "v124", "v125", "v126", "v127", "v128", "v129", "v130", "v131", "v132", "v133", "v134", "v135", "v136", "v137", "v138", "v139", "v140", "v141", "v142", "v143", "v144", "v145", "v146", "v147", "v148", "v149", "v150", "v151", "v152", "v153", "v154", "v155", "v156", "v157", "v158", "v159", "v160", "v161", "v162", "v163", "v164", "v165", "v166", "v167", "v168", "v169", "v170", "v171", "v172", "v173", "v174", "v175", "v176", "v177", "v178", "v179", "v180", "v181", "v182", "v183", "v184", "v185", "v186", "v187", "v188", "v189", "v190", "v191", "v192", "v193", "v194", "v195", "v196", "v197", "v198", "v199", "v200", "v201", "v202", "v203", "v204", "v205", "v206", "v207", "v208", "v209", "v210", "v211", "v212", "v213", "v214", "v215", "v216", "v217", "v218", "v219", "v220", "v221", "v222", "v223", "v224", "v225", "v226", "v227", "v228", "v229", "v230", "v231", "v232", "v233", "v234", "v235", "v236", "v237", "v238", "v239", "v240", "v241", "v242", "v243", "v244", "v245", "v246", "v247", "v248", "v249", "v250", "v251", "v252", "v253", "v254", "v255", "s4", "s5", "s6", "s7", "s8", "s9", "s10", "s11", "s12", "s13", "s14", "s15", "s16", "s17", "s18", "s19", "s20", "s21", "s22", "s23", "s24", "s25", "s26", "s27", "s28", "s29", "s30", "s31", "s33", "s34", "s35", "s36", "s37", "s38", "s39", "s40", "s41", "s42", "s43", "s44", "s45", "s46", "s47", "s48", "s49", "s50", "s51", "s52", "s53", "s54", "s55", "s56", "s57", "s58", "s59", "s60", "s61", "s62", "s63", "s64", "s65", "s66", "s67", "s68", "s69", "s70", "s71", "s72", "s73", "s74", "s75", "s76", "s77", "s78", "s79", "s80", "s81", "s82", "s83", "s84", "s85", "s86", "s87", "s88", "s89", "vcc", "memory");
}

extern "C" void kernel_launch(void* const* d_in, const int* in_sizes, int n_in, void* d_out, int out_size, void* d_ws, size_t ws_size, hipStream_t stream) {
  fused_block<<<256, 512, 0, stream>>>(d_in[0], d_in[1], d_in[2], d_in[3], d_in[4], d_in[6], d_in[7], d_in[8], d_out, d_ws);
}
```
